```python
import torch

def _softmax(x, axis=-1):
    return torch.softmax(x, dim=axis)
def _gelu(x):
    return torch.nn.functional.gelu(x, approximate="tanh")
def _silu(x):
    return torch.nn.functional.silu(x)
def _sigmoid(x):
    return torch.sigmoid(x)
def _layernorm(x, axis=-1, eps=1e-5):
    m = x.mean(dim=axis, keepdim=True)
    v = x.var(dim=axis, keepdim=True, unbiased=False)
    return (x - m) / torch.sqrt(v + eps)
def _scatter_add(tgt, idx, src, axis=0):
    return tgt.clone().index_add_(axis, idx, src)
def _segment_sum(src, seg, n):
    out = torch.zeros((n,) + src.shape[1:], dtype=src.dtype)
    return out.index_add_(0, seg, src)
def _pad(x, pw):
    flat = tuple(v for pair in reversed(pw) for v in pair)
    return torch.nn.functional.pad(x, flat)

kernel_name = 'concat_heads__gqa_block_cached__mha_block_cached__near_ridge'

def setup_inputs():

    _w = (lambda a: (lambda t: t.to(torch.bfloat16) if t.is_floating_point() else t)(torch.as_tensor(a)))
    def _A_setup():
        B, Sq, D = 32, 80, 448
        Hq, Hkv, Sk, Dh = 16, 8, 512, 256
        assert Hq % Hkv == 0, (Hq, Hkv)
        return {
            "x": _w((lambda *s: torch.randn(*s, dtype=torch.bfloat16) * 0.8)(B, Sq, D)),
            "k_cache": _w((lambda *s: torch.randn(*s, dtype=torch.bfloat16) * 0.8)(B, Hkv, Sk, Dh)),
            "v_cache": _w((lambda *s: torch.randn(*s, dtype=torch.bfloat16) * 0.05)(B, Hkv, Sk, Dh)),
            "wq": _w((lambda *s: torch.randn(*s, dtype=torch.bfloat16) * 0.8)(D, Hq * Dh) * (D ** -0.5)),
            "wo": _w((lambda *s: torch.randn(*s, dtype=torch.bfloat16) * 0.05)(Hq * Dh, D) * ((Hq * Dh) ** -0.5)),
        }
    _A_in = _A_setup()

    def _B_setup():
        B, Sq, D = 32, 80, 448
        H, Sk, Dh = 16, 512, 256
        return {
            "x": _w((lambda *s: torch.randn(*s, dtype=torch.bfloat16) * 0.8)(B, Sq, D)),
            "k_cache": _w((lambda *s: torch.randn(*s, dtype=torch.bfloat16) * 0.8)(B, H, Sk, Dh)),
            "v_cache": _w((lambda *s: torch.randn(*s, dtype=torch.bfloat16) * 0.05)(B, H, Sk, Dh)),
            "wq": _w((lambda *s: torch.randn(*s, dtype=torch.bfloat16) * 0.8)(D, H * Dh) * (D ** -0.5)),
            "wo": _w((lambda *s: torch.randn(*s, dtype=torch.bfloat16) * 0.05)(H * Dh, D) * ((H * Dh) ** -0.5)),
        }
    _B_in = _B_setup()

    return {
        "A_x": _A_in["x"],
        "A_k_cache": _A_in["k_cache"],
        "A_v_cache": _A_in["v_cache"],
        "A_wq": _A_in["wq"],
        "A_wo": _A_in["wo"],
        "B_x": _B_in["x"],
        "B_k_cache": _B_in["k_cache"],
        "B_v_cache": _B_in["v_cache"],
        "B_wq": _B_in["wq"],
        "B_wo": _B_in["wo"],
    }

def reference(A_x, A_k_cache, A_v_cache, A_wq, A_wo, B_x, B_k_cache, B_v_cache, B_wq, B_wo):
    def _A_ref(x, k_cache, v_cache, wq, wo):
        B, Sq, D = x.shape
        Hq, Dh = 16, 256
        Hkv = k_cache.shape[1]
        rep = Hq // Hkv
        q = (lambda x, *d: x.permute(*d))((x @ wq).reshape(B, Sq, Hq, Dh), 0, 2, 1, 3)
        k = (lambda x, n, axis: torch.repeat_interleave(x, n, dim=axis))(k_cache, rep, axis=1)
        v = (lambda x, n, axis: torch.repeat_interleave(x, n, dim=axis))(v_cache, rep, axis=1)
        scores = torch.einsum("bhqd,bhkd->bhqk", q, k) * (Dh ** -0.5)
        p = _softmax(scores, axis=-1)
        o = torch.einsum("bhqk,bhkd->bhqd", p, v)
        return (lambda x, *d: x.permute(*d))(o, 0, 2, 1, 3).reshape(B, Sq, Hq * Dh) @ wo

    def _B_ref(x, k_cache, v_cache, wq, wo):
        B, Sq, D = x.shape
        H, Dh = 16, 256
        q = (lambda x, *d: x.permute(*d))((x @ wq).reshape(B, Sq, H, Dh), 0, 2, 1, 3)
        scores = torch.einsum("bhqd,bhkd->bhqk", q, k_cache) * (Dh ** -0.5)
        p = _softmax(scores, axis=-1)
        o = torch.einsum("bhqk,bhkd->bhqd", p, v_cache)
        return (lambda x, *d: x.permute(*d))(o, 0, 2, 1, 3).reshape(B, Sq, H * Dh) @ wo

    a = _A_ref(A_x, A_k_cache, A_v_cache, A_wq, A_wo)
    b = _B_ref(B_x, B_k_cache, B_v_cache, B_wq, B_wo)
    return torch.concatenate([a, b], axis=-1)
```

```cpp
#include <hip/hip_runtime.h>
#include <hip/hip_bf16.h>
#include <stdint.h>
#include <type_traits>

typedef short bf16x8 __attribute__((ext_vector_type(8)));
typedef short s16x4 __attribute__((ext_vector_type(4)));
typedef float f32x4 __attribute__((ext_vector_type(4)));
typedef float f32x2 __attribute__((ext_vector_type(2)));
typedef __bf16 bf16x2 __attribute__((ext_vector_type(2)));
typedef unsigned u32x4 __attribute__((ext_vector_type(4)));
typedef unsigned u32x2 __attribute__((ext_vector_type(2)));
#define LAS __attribute__((address_space(3)))
typedef __amdgpu_buffer_rsrc_t rsrc_t;
typedef LAS char* lds_t;

#define DIAG 0

constexpr int LDS_RING = 81920;
constexpr int LDS_EXM = 147456;
constexpr int LDS_EXL = 155136;
constexpr int LDS_TOTAL = 162816;
constexpr unsigned X_BYTES = 80 * 448 * 2;
constexpr unsigned W_BYTES = 448 * 4096 * 2;
constexpr unsigned KV_HEAD = 512 * 256 * 2;
constexpr unsigned SLAB = 80 * 448 * 2;
constexpr float SCL = 0.0625f * 1.4426950408889634f;

template <int I, int N, class F>
__device__ __forceinline__ void sfor(F&& f) {
  if constexpr (I < N) { f(std::integral_constant<int, I>{}); sfor<I + 1, N>(f); }
}
#define SBAR() __builtin_amdgcn_sched_barrier(0)
#define MFMA(a, b, c) __builtin_amdgcn_mfma_f32_16x16x32_bf16(a, b, c, 0, 0, 0)
template <int N> __device__ __forceinline__ void wait_vm() { asm volatile("s_waitcnt vmcnt(%0)" ::"i"(N) : "memory"); }
__device__ __forceinline__ void bar() { asm volatile("s_waitcnt lgkmcnt(0)\n\ts_barrier" ::: "memory"); }
template <int N> __device__ __forceinline__ void wait_vm_bar_raw() { asm volatile("s_waitcnt vmcnt(%0)\n\ts_barrier" ::"i"(N) : "memory"); }
template <int N> __device__ __forceinline__ void wait_vm_bar() { asm volatile("s_waitcnt vmcnt(%0) lgkmcnt(0)\n\ts_barrier" ::"i"(N) : "memory"); }
__device__ __forceinline__ int lane_id() {
  int l; asm volatile("v_mbcnt_lo_u32_b32 %0, -1, 0\n\tv_mbcnt_hi_u32_b32 %0, -1, %0" : "=v"(l)); return l;
}
__device__ __forceinline__ unsigned pk2(float a, float b) {
  return __builtin_bit_cast(unsigned, __builtin_convertvector(f32x2{a, b}, bf16x2));
}
__device__ __forceinline__ rsrc_t mk_rsrc(const void* p, unsigned off, unsigned bytes) {
  return __builtin_amdgcn_make_buffer_rsrc((void*)((const char*)p + off), (short)0, (int)bytes, 0x00020000);
}
__device__ __forceinline__ void need(const bf16x8 (&f)[4]) { asm volatile("" ::"v"(f[0]), "v"(f[1]), "v"(f[2]), "v"(f[3]) : "memory"); }
template <bool NT, unsigned STEP>
__device__ __forceinline__ void dma4(unsigned m0v, rsrc_t r, unsigned ve, unsigned vo, unsigned s) {
  if constexpr (NT)
    asm volatile("s_mov_b32 m0, %0\n\ts_nop 0\n\t"
                 "buffer_load_dwordx4 %1, %3, %4 offen nt lds\n\tbuffer_load_dwordx4 %2, %3, %5 offen offset:1024 nt lds\n\t"
                 "buffer_load_dwordx4 %1, %3, %6 offen offset:2048 nt lds\n\tbuffer_load_dwordx4 %2, %3, %7 offen offset:3072 nt lds"
                 ::"s"(m0v), "v"(ve), "v"(vo), "s"(r), "s"(s), "s"(s + STEP), "s"(s + 2 * STEP), "s"(s + 3 * STEP) : "memory");
  else
    asm volatile("s_mov_b32 m0, %0\n\ts_nop 0\n\t"
                 "buffer_load_dwordx4 %1, %3, %4 offen lds\n\tbuffer_load_dwordx4 %2, %3, %5 offen offset:1024 lds\n\t"
                 "buffer_load_dwordx4 %1, %3, %6 offen offset:2048 lds\n\tbuffer_load_dwordx4 %2, %3, %7 offen offset:3072 lds"
                 ::"s"(m0v), "v"(ve), "v"(vo), "s"(r), "s"(s), "s"(s + STEP), "s"(s + 2 * STEP), "s"(s + 3 * STEP) : "memory");
}
template <bool NT, unsigned STEP>
__device__ __forceinline__ void dma2(unsigned m0v, rsrc_t r, unsigned v, unsigned s) {
  if constexpr (NT)
    asm volatile("s_mov_b32 m0, %0\n\ts_nop 0\n\t"
                 "buffer_load_dwordx4 %1, %2, %3 offen nt lds\n\tbuffer_load_dwordx4 %1, %2, %4 offen offset:1024 nt lds"
                 ::"s"(m0v), "v"(v), "s"(r), "s"(s), "s"(s + STEP) : "memory");
  else
    asm volatile("s_mov_b32 m0, %0\n\ts_nop 0\n\t"
                 "buffer_load_dwordx4 %1, %2, %3 offen lds\n\tbuffer_load_dwordx4 %1, %2, %4 offen offset:1024 lds"
                 ::"s"(m0v), "v"(v), "s"(r), "s"(s), "s"(s + STEP) : "memory");
}
__device__ __forceinline__ void dma1(unsigned m0v, rsrc_t r, unsigned v) {
  asm volatile("s_mov_b32 m0, %0\n\ts_nop 0\n\tbuffer_load_dwordx4 %1, %2, 0 offen lds" ::"s"(m0v), "v"(v), "s"(r) : "memory");
}
__device__ __forceinline__ bf16x8 ld128(lds_t p) { return *(LAS const bf16x8*)p; }
__device__ __forceinline__ bf16x8 ldtr(lds_t p0, lds_t p1) {
  s16x4 lo = __builtin_amdgcn_ds_read_tr16_b64_v4i16((LAS s16x4*)p0);
  s16x4 hi = __builtin_amdgcn_ds_read_tr16_b64_v4i16((LAS s16x4*)p1);
  return __builtin_shufflevector(lo, hi, 0, 1, 2, 3, 4, 5, 6, 7);
}
__device__ __forceinline__ float max3(float a, float b, float c) { return __builtin_fmaxf(__builtin_fmaxf(a, b), c); }
__device__ __forceinline__ float xmax(float x) {
  auto a = __builtin_amdgcn_permlane16_swap(__float_as_uint(x), __float_as_uint(x), false, false);
  x = max3(__uint_as_float(a[0]), __uint_as_float(a[1]), __uint_as_float(a[1]));
  auto b = __builtin_amdgcn_permlane32_swap(__float_as_uint(x), __float_as_uint(x), false, false);
  return max3(__uint_as_float(b[0]), __uint_as_float(b[1]), __uint_as_float(b[1]));
}
__device__ __forceinline__ float xsum(float x) {
  auto a = __builtin_amdgcn_permlane16_swap(__float_as_uint(x), __float_as_uint(x), false, false);
  x = __uint_as_float(a[0]) + __uint_as_float(a[1]);
  auto b = __builtin_amdgcn_permlane32_swap(__float_as_uint(x), __float_as_uint(x), false, false);
  return __uint_as_float(b[0]) + __uint_as_float(b[1]);
}

__device__ __forceinline__ unsigned voff_nat(int l, unsigned pitch) { return (l >> 3) * pitch + 16 * ((l & 7) ^ (((l >> 4) & 1) << 1)); }
__device__ __forceinline__ unsigned voff_spl(int l, unsigned pitch) { return (l >> 3) * pitch + 16 * ((l & 7) ^ (((l >> 4) & 3) << 1)); }
__device__ __forceinline__ unsigned voff_k(int l) { return (l >> 3) * 512 + 16 * ((l & 7) ^ ((l >> 4) & 3)); }

struct Params {
  const void *x[2], *k[2], *v[2], *wq[2], *wo[2];
  void* slab;
  unsigned long long* diag;
};

#define STAMP(i)

constexpr unsigned STEP_WQ = 8 * 8192 - 1024, STEP_KV = 8 * 512 - 1024, STEP_WO = 8 * 896 - 1024;

__device__ __forceinline__ void issue_x(const Params& p, int P, int b, int w, unsigned lds0) {
  const int l = lane_id();
  const rsrc_t rx = mk_rsrc(p.x[P], b * X_BYTES, X_BYTES);
  sfor<0, 9>([&](auto I) {
    const unsigned pos = (w * 9 + I) * 64 + l, row = pos / 56, c = pos - row * 56;
    dma1(lds0 + (w * 9 + I) * 1024, rx, row * 896 + 16 * (c ^ ((row >> 1) & 7)));
  });
}

template <int P  , bool LAST, int ST>
__device__ __forceinline__ void run_problem(const Params& p, lds_t lds, unsigned lds0, int b, int j, int w, unsigned (&st)[32]) {
  constexpr bool GQA = (P == 0);
  const int hh = w >> 2, cq = w & 3;
  const unsigned ring = lds0 + LDS_RING + w * 8192;
  f32x4 acc[5][4];
  bf16x8 fa[2][4], fb[2][5];

  const rsrc_t rK = GQA ? mk_rsrc(p.k[P], (b * 8 + j) * KV_HEAD + (hh * 128 + cq * 32) * 512, KV_HEAD - (hh * 128 + cq * 32) * 512)
                        : mk_rsrc(p.k[P], (b * 16 + 2 * j + hh) * KV_HEAD + cq * 32 * 512, KV_HEAD - cq * 32 * 512);
  auto k_soff = [](int g) -> unsigned { return (g >> 2) * (GQA ? 256 : 128) * 512 + (g & 3) * 128; };

  {
    const rsrc_t rW = mk_rsrc(p.wq[P], (512 * j + 64 * w) * 2, W_BYTES - (512 * j + 64 * w) * 2);
    const int l = lane_id(), n = l & 15, g = l >> 4, q = (l >> 2) & 3;
    const unsigned we = voff_nat(l, 8192), wo_ = we ^ 64, ke = voff_k(l), ko = ke ^ 64;
    const unsigned a0 = LDS_RING + w * 8192 + (8 * g + q) * 128 + (((q >> 1) | ((g & 1) << 1)) * 32) + 8 * (l & 3);
    const unsigned x0 = n * 896 + 16 * (g ^ ((n >> 1) & 7));
    auto rd = [&](auto S) {
      constexpr int s = S;
      sfor<0, 4>([&](auto T) { lds_t a = lds + (a0 ^ (T * 32)) + (s & 1) * 4096; fa[s & 1][T] = ldtr(a, a + 512); });
      sfor<0, 5>([&](auto T) { fb[s & 1][T] = ld128(lds + (x0 ^ ((s & 1) * 64)) + T * 14336 + (s >> 1) * 128); });
    };
    auto issue = [&](auto G) {
      constexpr int gi = G;
      if constexpr (gi < 14) dma4<false, STEP_WQ>(ring + (gi & 1) * 4096, rW, we, wo_, gi * 32 * 8192);
      else dma4<true, STEP_KV>(ring + (gi & 1) * 4096, rK, ke, ko, k_soff(gi - 14));
    };
    sfor<0, 5>([&](auto T) { sfor<0, 4>([&](auto U) { acc[T][U] = f32x4{0.f, 0.f, 0.f, 0.f}; }); });
    wait_vm<4>();
    rd(std::integral_constant<int, 0>{});
    need(fa[0]);
    issue(std::integral_constant<int, 2>{});
    sfor<0, 14>([&](auto S) {
      constexpr int s = S;
      SBAR();
      if constexpr (s < 13) { wait_vm<4>(); rd(std::integral_constant<int, s + 1>{}); }
      else bar();
      sfor<0, 5>([&](auto T) { sfor<0, 2>([&](auto U) { acc[T][U] = MFMA(fa[s & 1][U], fb[s & 1][T], acc[T][U]); }); });
      SBAR();
      if constexpr (s < 13) { need(fa[(s + 1) & 1]); issue(std::integral_constant<int, s + 3>{}); }
      sfor<0, 5>([&](auto T) { sfor<2, 4>([&](auto U) { acc[T][U] = MFMA(fa[s & 1][U], fb[s & 1][T], acc[T][U]); }); });
    });
    SBAR();
    STAMP(ST + 1);
    const unsigned qw = hh * 40960 + cq * 10240 + (g >> 1) * 256 + n * 16 + (g & 1) * 8;
    sfor<0, 5>([&](auto T) {
      sfor<0, 4>([&](auto U) {
        *(LAS u32x2*)(lds + qw + (U >> 1) * 5120 + T * 1024 + (U & 1) * 512) = u32x2{pk2(acc[T][U][0], acc[T][U][1]), pk2(acc[T][U][2], acc[T][U][3])};
      });
    });
    bar();
  }
  STAMP(ST + 2);

  const rsrc_t rV = GQA ? mk_rsrc(p.v[P], (b * 8 + j) * KV_HEAD + hh * 16 * 512 + cq * 128, KV_HEAD - (hh * 16 * 512 + cq * 128))
                        : mk_rsrc(p.v[P], (b * 16 + 2 * j + hh) * KV_HEAD + cq * 128, KV_HEAD - cq * 128);
  constexpr int MT = GQA ? 2 : 1;
  constexpr int NG = GQA ? 8 : 16;
  f32x4 sc[4][5][2];
  float mx[5 * MT];
  {
    const int l = lane_id(), n = l & 15, g = l >> 4;
    const unsigned ke = voff_k(l), ko = ke ^ 64, vv = voff_spl(l, 512);
    const unsigned k0 = LDS_RING + w * 8192 + n * 128 + 16 * (g ^ ((n >> 1) & 7));
    const unsigned q0 = (GQA ? 0 : hh * 40960) + l * 16;
    constexpr int NSUB = 2 * MT;
    auto rdk = [&](auto G) {
      constexpr int gi = G;
      sfor<0, 4>([&](auto I) { fa[gi & 1][I] = ld128(lds + (k0 ^ ((I >> 1) * 64)) + (I & 1) * 2048 + (gi & 1) * 4096); });
    };
    auto rdq = [&](auto Z) {
      constexpr int z = Z, gi = z / NSUB, e = (z % NSUB) / MT, h = z % MT;
      sfor<0, 5>([&](auto T) { fb[z & 1][T] = ld128(lds + q0 + h * 40960 + ((2 * (gi & 3) + e) * 5 + T) * 1024); });
    };
    auto issue = [&](auto G) {
      constexpr int gi = G;
      if constexpr (gi < NG) dma4<true, STEP_KV>(ring + (gi & 1) * 4096, rK, ke, ko, k_soff(gi));
      else if constexpr (!GQA) dma4<true, STEP_KV>(ring + (gi & 1) * 4096, rV, vv, vv, (gi - NG) * 32 * 512);
      else {
        dma2<true, STEP_KV>(ring + (gi & 1) * 4096, rV, vv, (2 * (gi - NG)) * 32 * 512);
        dma2<true, STEP_KV>(ring + (gi & 1) * 4096 + 2048, rV, vv, (2 * (gi - NG) + 1) * 32 * 512);
      }
    };
    wait_vm<4>();
    rdk(std::integral_constant<int, 0>{});
    rdq(std::integral_constant<int, 0>{});
    need(fa[0]);
    issue(std::integral_constant<int, 2>{});
    sfor<0, NG * NSUB>([&](auto Z) {
      constexpr int z = Z, gi = z / NSUB, sub = z % NSUB, e = sub / MT, h = sub % MT, un = GQA ? 2 * (gi >> 2) + h : (gi >> 2);
      SBAR();
      if constexpr (sub == 0 && gi + 1 < NG) { wait_vm<4>(); rdk(std::integral_constant<int, gi + 1>{}); }
      if constexpr (sub == 1 && gi + 1 < NG) { need(fa[(gi + 1) & 1]); issue(std::integral_constant<int, gi + 3>{}); }
      if constexpr (z + 1 < NG * NSUB) rdq(std::integral_constant<int, z + 1>{});
      sfor<0, 5>([&](auto T) {
        sfor<0, 2>([&](auto U) {
          if constexpr ((gi & 3) == 0 && e == 0) sc[un][T][U] = MFMA(fa[gi & 1][2 * e + U], fb[z & 1][T], (f32x4{0.f, 0.f, 0.f, 0.f}));
          else sc[un][T][U] = MFMA(fa[gi & 1][2 * e + U], fb[z & 1][T], sc[un][T][U]);
        });
      });
    });
    SBAR();
    STAMP(ST + 3);
    sfor<0, 5 * MT>([&](auto R) {
      constexpr int h = R / 5, t = R % 5;
      float m = -INFINITY;
      sfor<0, 4 / MT>([&](auto G) {
        constexpr int un = GQA ? 2 * G + h : G;
        sfor<0, 2>([&](auto U) { m = max3(max3(m, sc[un][t][U][0], sc[un][t][U][1]), sc[un][t][U][2], sc[un][t][U][3]); });
      });
      m = xmax(m);
      *(LAS float*)(lds + LDS_EXM + ((GQA ? h : hh) * 80 + t * 16 + n) * 48 + (GQA ? w : cq) * 4) = m;
    });
    bar();
    sfor<0, 5 * MT>([&](auto R) {
      constexpr int h = R / 5, t = R % 5;
      lds_t e = lds + LDS_EXM + ((GQA ? h : hh) * 80 + t * 16 + n) * 48;
      f32x4 v = *(LAS const f32x4*)e;
      float m = max3(max3(v[0], v[1], v[2]), v[3], v[3]);
      if constexpr (GQA) { f32x4 v2 = *(LAS const f32x4*)(e + 16); m = max3(max3(m, v2[0], v2[1]), v2[2], v2[3]); }
      mx[R] = -m * SCL;
    });
  }
  STAMP(ST + 4);

  const rsrc_t rO = mk_rsrc(p.wo[P], 512 * j * 896 + (w < 7 ? w : 0) * 128, w < 7 ? W_BYTES - (512 * j * 896 + w * 128) : 0u);
  {
    const int l = lane_id(), g = l >> 4, q = (l >> 2) & 3;
    const unsigned vv = voff_spl(l, 512);
    float ls[5 * MT];
    u32x4 pp[4][5];
    sfor<0, 5 * MT>([&](auto R) { ls[R] = 0.f; });
    auto pexp = [&](auto UN, auto T) {
      constexpr int un = UN, t = T, r = (GQA ? (un & 1) * 5 : 0) + t;
      float e[8];
      sfor<0, 8>([&](auto I) { e[I] = __builtin_amdgcn_exp2f(__builtin_fmaf(sc[un][t][I >> 2][I & 3], SCL, mx[r])); });
      ls[r] += ((e[0] + e[1]) + (e[2] + e[3])) + ((e[4] + e[5]) + (e[6] + e[7]));
      pp[un][t] = u32x4{pk2(e[0], e[1]), pk2(e[2], e[3]), pk2(e[4], e[5]), pk2(e[6], e[7])};
      asm volatile("" : "+v"(ls[r]), "+v"(pp[un][t]));
    };
    auto pwrite = [&](auto UN) {
      constexpr int un = UN;
      const unsigned pw = (GQA ? hh * 40960 : hh * 20480) + cq * 5120 + lane_id() * 16;
      sfor<0, 5>([&](auto T) { *(LAS u32x4*)(lds + pw + (GQA ? (un & 1) * 20480 : (un & 1) * 40960) + T * 1024) = pp[un][T]; });
    };
    sfor<0, 2>([&](auto UN) { sfor<0, 5>([&](auto T) { pexp(UN, T); }); pwrite(UN); });
    const unsigned a0 = LDS_RING + (GQA ? cq : w) * 8192 + (4 * g + q) * 128 + (((q >> 1) | ((g & 1) << 1)) * 32) + 8 * (l & 3);
    const unsigned p0 = hh * 20480 + l * 16;
    auto rda = [&](auto S) {
      constexpr int s = S;
      sfor<0, 4>([&](auto T) {
        lds_t a = lds + (a0 ^ (T * 32)) + (GQA ? (s & 3) * 2048 : (s & 1) * 4096);
        fa[s & 1][T] = ldtr(a, a + (GQA ? 32768 : 2048));
      });
    };
    auto rdb = [&](auto S, auto T) { constexpr int s = S; fb[0][T] = ld128(lds + p0 + ((s >> 2) & 1) * 40960 + ((s & 3) * 5 + T) * 1024); };
    auto issue = [&](auto G) {
      constexpr int gi = G;
      if constexpr (GQA) {
        if constexpr (gi < 16) dma2<true, STEP_KV>(ring + (gi & 3) * 2048, rV, vv, gi * 32 * 512);
        else dma2<false, STEP_WO>(ring + (gi & 3) * 2048, rO, voff_spl(lane_id(), 896), (gi - 16) * 16 * 896);
      } else {
        if constexpr (gi < 16) dma4<true, STEP_KV>(ring + (gi & 1) * 4096, rV, vv, vv, gi * 32 * 512);
        else { const unsigned ov = voff_spl(lane_id(), 896); dma4<false, STEP_WO>(ring + (gi & 1) * 4096, rO, ov, ov, (gi - 16) * 32 * 896); }
      }
    };
    wait_vm_bar<GQA ? 6 : 4>();
    STAMP(ST + 5);
    rda(std::integral_constant<int, 0>{});
    sfor<0, 5>([&](auto T) { rdb(std::integral_constant<int, 0>{}, T); });
    if constexpr (!GQA) { need(fa[0]); issue(std::integral_constant<int, 2>{}); }
    float rl[5];
    sfor<0, 16>([&](auto S) {
      constexpr int s = S;
      SBAR();
      if constexpr (GQA) {
        if constexpr ((s & 3) == 3) wait_vm_bar<4>();
        else { need(fa[s & 1]); wait_vm_bar_raw<4>(); }
        issue(std::integral_constant<int, s + 4>{});
      } else {
        if constexpr (s == 15) bar();
        else if constexpr ((s & 3) == 3) wait_vm_bar<4>();
        else wait_vm<4>();
      }
      if constexpr (s < 15) rda(std::integral_constant<int, s + 1>{});
      sfor<0, 5>([&](auto T) {
        sfor<0, 4>([&](auto U) {
          if constexpr (s == 0) acc[T][U] = MFMA(fa[s & 1][U], fb[0][T], (f32x4{0.f, 0.f, 0.f, 0.f}));
          else acc[T][U] = MFMA(fa[s & 1][U], fb[0][T], acc[T][U]);
        });
        if constexpr (s < 5 && T == 1) pexp(std::integral_constant<int, 2 + ((2 * s) / 5) % 2>{}, std::integral_constant<int, (2 * s) % 5>{});
        if constexpr (s < 5 && T == 3) pexp(std::integral_constant<int, 2 + ((2 * s + 1) / 5) % 2>{}, std::integral_constant<int, (2 * s + 1) % 5>{});
        SBAR();
        if constexpr (s < 15) rdb(std::integral_constant<int, s + 1>{}, T);
        if constexpr (!GQA && s < 15 && T == 1) { need(fa[(s + 1) & 1]); issue(std::integral_constant<int, s + 3>{}); }
      });
      if constexpr (s == 5) {
        if constexpr (GQA) { if (hh == 0) { pwrite(std::integral_constant<int, 2>{}); pwrite(std::integral_constant<int, 3>{}); } }
        else pwrite(std::integral_constant<int, 2>{});
        const int n2 = lane_id() & 15;
        sfor<0, 5 * MT>([&](auto R) {
          constexpr int h = R / 5, t = R % 5;
          *(LAS float*)(lds + LDS_EXL + ((GQA ? h : hh) * 80 + t * 16 + n2) * 48 + (GQA ? w : cq) * 4) = xsum(ls[R]);
        });
      }
      if constexpr (s == 8) {
        if constexpr (GQA) { if (hh == 1) { pwrite(std::integral_constant<int, 2>{}); pwrite(std::integral_constant<int, 3>{}); } }
        else pwrite(std::integral_constant<int, 3>{});
      }
      if constexpr (s == 12) {
        const int n2 = lane_id() & 15;
        sfor<0, 5>([&](auto T) {
          lds_t e = lds + LDS_EXL + (hh * 80 + T * 16 + n2) * 48;
          f32x4 v = *(LAS const f32x4*)e;
          float sum = (v[0] + v[1]) + (v[2] + v[3]);
          if constexpr (GQA) { f32x4 v2 = *(LAS const f32x4*)(e + 16); sum += (v2[0] + v2[1]) + (v2[2] + v2[3]); }
          rl[T] = __builtin_amdgcn_rcpf(sum);
        });
      }
    });
    SBAR();
    STAMP(ST + 6);
    const unsigned ow = hh * 40960 + cq * 10240 + lane_id() * 16;
    sfor<0, 5>([&](auto T) {
      sfor<0, 2>([&](auto H) {
        const f32x4 u = acc[T][2 * H] * rl[T], v = acc[T][2 * H + 1] * rl[T];
        *(LAS u32x4*)(lds + ow + H * 5120 + T * 1024) = u32x4{pk2(u[0], u[1]), pk2(u[2], u[3]), pk2(v[0], v[1]), pk2(v[2], v[3])};
      });
    });
    bar();
  }
  STAMP(ST + 7);

  {
    const rsrc_t rN = mk_rsrc(p.wq[1 - P], (512 * j + 64 * w) * 2, LAST ? 0u : W_BYTES - (512 * j + 64 * w) * 2);
    const int l = lane_id(), g = l >> 4, q = (l >> 2) & 3;
    const unsigned ov = voff_spl(l, 896), we = voff_nat(l, 8192), wo_ = we ^ 64;
    if (w < 7) {
      const unsigned a0 = LDS_RING + w * 8192 + (4 * g + q) * 128 + (((q >> 1) | ((g & 1) << 1)) * 32) + 8 * (l & 3);
      auto rd = [&](auto S) {
        constexpr int s = S;
        sfor<0, 4>([&](auto T) { lds_t a = lds + (a0 ^ (T * 32)) + (s & 1) * 4096; fa[s & 1][T] = ldtr(a, a + 2048); });
        sfor<0, 5>([&](auto T) { fb[s & 1][T] = ld128(lds + l * 16 + (s * 5 + T) * 1024); });
      };
      auto issue = [&](auto G) {
        constexpr int gi = G;
        if constexpr (gi < 16) dma4<false, STEP_WO>(ring + (gi & 1) * 4096, rO, ov, ov, gi * 32 * 896);
        else if constexpr (!LAST) dma4<false, STEP_WQ>(ring + (gi & 1) * 4096, rN, we, wo_, (gi - 16) * 32 * 8192);
      };
      sfor<0, 5>([&](auto T) { sfor<0, 4>([&](auto U) { acc[T][U] = f32x4{0.f, 0.f, 0.f, 0.f}; }); });
      wait_vm<4>();
      rd(std::integral_constant<int, 0>{});
      need(fa[0]);
      issue(std::integral_constant<int, 2>{});
      sfor<0, 16>([&](auto S) {
        constexpr int s = S;
        SBAR();
        if constexpr (s < 15) { wait_vm<(LAST && s == 14) ? 0 : 4>(); rd(std::integral_constant<int, s + 1>{}); }
        else if constexpr (!LAST) { bar(); issue_x(p, 1 - P, b, w, lds0); }
        sfor<0, 5>([&](auto T) { sfor<0, 2>([&](auto U) { acc[T][U] = MFMA(fa[s & 1][U], fb[s & 1][T], acc[T][U]); }); });
        SBAR();
        if constexpr (s < 15) { need(fa[(s + 1) & 1]); issue(std::integral_constant<int, s + 3>{}); }
        sfor<0, 5>([&](auto T) { sfor<2, 4>([&](auto U) { acc[T][U] = MFMA(fa[s & 1][U], fb[s & 1][T], acc[T][U]); }); });
      });
      SBAR();
      STAMP(ST + 8);
      u32x4* slab = (u32x4*)((char*)p.slab + (size_t)((P * 32 + b) * 8 + j) * SLAB + w * 10240 + l * 16);
      sfor<0, 5>([&](auto T) {
        sfor<0, 2>([&](auto H) {
          const f32x4 u = acc[T][2 * H], v = acc[T][2 * H + 1];
          auto r0 = __builtin_amdgcn_permlane16_swap(pk2(u[0], u[1]), pk2(v[0], v[1]), false, false);
          auto r1 = __builtin_amdgcn_permlane16_swap(pk2(u[2], u[3]), pk2(v[2], v[3]), false, false);
          slab[(T * 2 + H) * 64] = u32x4{r0[0], r1[0], r0[1], r1[1]};
        });
      });
      if constexpr (!LAST) wait_vm_bar<10>();
    } else if constexpr (!LAST) {
      wait_vm<0>();
      dma4<false, STEP_WQ>(ring, rN, we, wo_, 0);
      dma4<false, STEP_WQ>(ring + 4096, rN, we, wo_, 32 * 8192);
      bar();
      issue_x(p, 1 - P, b, w, lds0);
      wait_vm_bar<0>();
    }
  }
  STAMP(ST + 9);
}

__global__ __launch_bounds__(512, 2) void fused_attn_kernel(Params p) {
  extern __shared__ __attribute__((aligned(16))) char smem[];
  lds_t lds = (lds_t)smem;
  const unsigned lds0 = (unsigned)(uintptr_t)lds;
  const int w = __builtin_amdgcn_readfirstlane(threadIdx.x >> 6);
  const int j = blockIdx.x & 7, b = blockIdx.x >> 3;
  const int first = b & 1;
  unsigned st[32];
  STAMP(0);
  {
    issue_x(p, first, b, w, lds0);
    const int l = lane_id();
    const rsrc_t rW = mk_rsrc(p.wq[first], (512 * j + 64 * w) * 2, W_BYTES - (512 * j + 64 * w) * 2);
    const unsigned we = voff_nat(l, 8192), ring = lds0 + LDS_RING + w * 8192;
    dma4<false, STEP_WQ>(ring, rW, we, we ^ 64, 0);
    dma4<false, STEP_WQ>(ring + 4096, rW, we, we ^ 64, 32 * 8192);
    wait_vm_bar<8>();
  }
  STAMP(10);
  if (first == 0) {
    run_problem<0, false, 0>(p, lds, lds0, b, j, w, st);
    run_problem<1, true, 10>(p, lds, lds0, b, j, w, st);
  } else {
    run_problem<1, false, 0>(p, lds, lds0, b, j, w, st);
    run_problem<0, true, 10>(p, lds, lds0, b, j, w, st);
  }
}

__global__ __launch_bounds__(256) void reduce_kernel(const u32x4* __restrict__ slab, u32x4* __restrict__ out) {
  const int idx = blockIdx.x * 256 + threadIdx.x;
  const int pb = idx / 4480, r = idx - pb * 4480;
  float s[8];
#pragma unroll
  for (int i = 0; i < 8; ++i) s[i] = 0.f;
  u32x4 v[8];
#pragma unroll
  for (int jj = 0; jj < 8; ++jj) v[jj] = slab[(size_t)(pb * 8 + jj) * 4480 + r];
#pragma unroll
  for (int jj = 0; jj < 8; ++jj)
#pragma unroll
    for (int i = 0; i < 4; ++i) { s[2 * i] += __uint_as_float(v[jj][i] << 16); s[2 * i + 1] += __uint_as_float(v[jj][i] & 0xffff0000u); }
  const int l = r & 63, f = r >> 6, w = f / 10, t = (f - w * 10) >> 1, h = f & 1, n = l & 15, g = l >> 4;
  const int P = pb >> 5, b = pb & 31;
  const int col = P * 448 + 64 * w + 16 * (2 * h + (g & 1)) + 8 * (g >> 1);
  out[((size_t)(b * 80 + t * 16 + n) * 896 + col) >> 3] = u32x4{pk2(s[0], s[1]), pk2(s[2], s[3]), pk2(s[4], s[5]), pk2(s[6], s[7])};
}

extern "C" void kernel_launch(void* const* d_in, const int* in_sizes, int n_in, void* d_out, int out_size, void* d_ws,
                              size_t ws_size, hipStream_t stream) {
  Params p;
  for (int i = 0; i < 2; ++i) {
    p.x[i] = d_in[5 * i + 0]; p.k[i] = d_in[5 * i + 1]; p.v[i] = d_in[5 * i + 2]; p.wq[i] = d_in[5 * i + 3]; p.wo[i] = d_in[5 * i + 4];
  }
  p.slab = d_ws;
  p.diag = (unsigned long long*)((char*)d_ws + (64u << 20));
  (void)hipFuncSetAttribute((const void*)fused_attn_kernel, hipFuncAttributeMaxDynamicSharedMemorySize, LDS_TOTAL);
  fused_attn_kernel<<<256, 512, LDS_TOTAL, stream>>>(p);
  reduce_kernel<<<2 * 32 * 4480 / 256, 256, 0, stream>>>((const u32x4*)d_ws, (u32x4*)d_out);
}
```

```cpp
#include <hip/hip_runtime.h>
#include <hip/hip_bf16.h>
#include <stdint.h>
#include <type_traits>

typedef short bf16x8 __attribute__((ext_vector_type(8)));
typedef short s16x4 __attribute__((ext_vector_type(4)));
typedef float f32x4 __attribute__((ext_vector_type(4)));
typedef float f32x2 __attribute__((ext_vector_type(2)));
typedef __bf16 bf16x2 __attribute__((ext_vector_type(2)));
typedef unsigned u32x4 __attribute__((ext_vector_type(4)));
typedef unsigned u32x2 __attribute__((ext_vector_type(2)));
#define LAS __attribute__((address_space(3)))
typedef __amdgpu_buffer_rsrc_t rsrc_t;
typedef LAS char* lds_t;

#define DIAG 0
#define ABL 0

constexpr int LDS_RING = 81920;
constexpr int LDS_EXM = 147456;
constexpr int LDS_EXL = 151552;
constexpr int LDS_DUMMY = 159232;
constexpr int LDS_TOTAL = 159488;
constexpr unsigned X_BYTES = 80 * 448 * 2;
constexpr unsigned W_BYTES = 448 * 4096 * 2;
constexpr unsigned KV_HEAD = 512 * 256 * 2;
constexpr unsigned SLAB = 80 * 448 * 2;
constexpr float SCL = 0.0625f * 1.4426950408889634f;
constexpr float GROW = 20.f;

template <int I, int N, class F>
__device__ __forceinline__ void sfor(F&& f) {
  if constexpr (I < N) { f(std::integral_constant<int, I>{}); sfor<I + 1, N>(f); }
}
#define SBAR() __builtin_amdgcn_sched_barrier(0)
#define MFMA(a, b, c) __builtin_amdgcn_mfma_f32_16x16x32_bf16(a, b, c, 0, 0, 0)
template <int N> __device__ __forceinline__ void wait_vm() { asm volatile("s_waitcnt vmcnt(%0)" ::"i"(N) : "memory"); }
__device__ __forceinline__ void bar() { asm volatile("s_waitcnt lgkmcnt(0)\n\ts_barrier" ::: "memory"); }
template <int N> __device__ __forceinline__ void wait_vm_bar_raw() { asm volatile("s_waitcnt vmcnt(%0)\n\ts_barrier" ::"i"(N) : "memory"); }
template <int N> __device__ __forceinline__ void wait_vm_bar() { asm volatile("s_waitcnt vmcnt(%0) lgkmcnt(0)\n\ts_barrier" ::"i"(N) : "memory"); }
__device__ __forceinline__ int lane_id() {
  int l; asm volatile("v_mbcnt_lo_u32_b32 %0, -1, 0\n\tv_mbcnt_hi_u32_b32 %0, -1, %0" : "=v"(l)); return l;
}
__device__ __forceinline__ unsigned pk2(float a, float b) {
  return __builtin_bit_cast(unsigned, __builtin_convertvector(f32x2{a, b}, bf16x2));
}
__device__ __forceinline__ rsrc_t mk_rsrc(const void* p, unsigned off, unsigned bytes) {
  return __builtin_amdgcn_make_buffer_rsrc((void*)((const char*)p + off), (short)0, (int)bytes, 0x00020000);
}
__device__ __forceinline__ void need(const bf16x8 (&f)[4]) { asm volatile("" ::"v"(f[0]), "v"(f[1]), "v"(f[2]), "v"(f[3]) : "memory"); }
__device__ __forceinline__ void set_m0(unsigned m0v) { asm volatile("s_mov_b32 m0, %0\n\ts_nop 0" ::"s"(m0v) : "memory"); }
template <bool NT, int I>
__device__ __forceinline__ void dmap(rsrc_t r, unsigned v, unsigned src) {
  if constexpr (NT) asm volatile("buffer_load_dwordx4 %0, %1, %2 offen offset:%3 nt lds" ::"v"(v), "s"(r), "s"(src - 1024u * I), "i"(1024 * I) : "memory");
  else asm volatile("buffer_load_dwordx4 %0, %1, %2 offen offset:%3 lds" ::"v"(v), "s"(r), "s"(src - 1024u * I), "i"(1024 * I) : "memory");
}
__device__ __forceinline__ void dma1(unsigned m0v, rsrc_t r, unsigned v) {
  asm volatile("s_mov_b32 m0, %0\n\ts_nop 0\n\tbuffer_load_dwordx4 %1, %2, 0 offen lds" ::"s"(m0v), "v"(v), "s"(r) : "memory");
}
__device__ __forceinline__ void warm(unsigned lds0, rsrc_t r, unsigned v) {
  asm volatile("s_mov_b32 m0, %0\n\ts_nop 0\n\tbuffer_load_dword %1, %2, 0 offen lds" ::"s"(lds0 + LDS_DUMMY), "v"(v), "s"(r) : "memory");
}
extern "C" __device__ float llvm_fmul_legacy(float, float) __asm("llvm.amdgcn.fmul.legacy");
__device__ __forceinline__ float mul1(float a, float b) { return llvm_fmul_legacy(a, b); }
__device__ __forceinline__ bf16x8 ld128(lds_t p) { return *(LAS const bf16x8*)p; }
__device__ __forceinline__ bf16x8 ldtr(lds_t p0, lds_t p1) {
  s16x4 lo = __builtin_amdgcn_ds_read_tr16_b64_v4i16((LAS s16x4*)p0);
  s16x4 hi = __builtin_amdgcn_ds_read_tr16_b64_v4i16((LAS s16x4*)p1);
  return __builtin_shufflevector(lo, hi, 0, 1, 2, 3, 4, 5, 6, 7);
}
__device__ __forceinline__ float max3(float a, float b, float c) { return __builtin_fmaxf(__builtin_fmaxf(a, b), c); }
struct OpMax { static __device__ __forceinline__ float f(float a, float b) { return max3(a, b, b); } };
struct OpAdd { static __device__ __forceinline__ float f(float a, float b) { return a + b; } };
template <class Op> __device__ __forceinline__ float sw16(float a, float b) {
  auto r = __builtin_amdgcn_permlane16_swap(__float_as_uint(a), __float_as_uint(b), false, false);
  return Op::f(__uint_as_float(r[0]), __uint_as_float(r[1]));
}
template <class Op> __device__ __forceinline__ float sw32(float a, float b) {
  auto r = __builtin_amdgcn_permlane32_swap(__float_as_uint(a), __float_as_uint(b), false, false);
  return Op::f(__uint_as_float(r[0]), __uint_as_float(r[1]));
}
template <class Op> __device__ __forceinline__ float red4(float a, float b, float c, float d) { return sw32<Op>(sw16<Op>(a, b), sw16<Op>(c, d)); }
template <class Op> __device__ __forceinline__ float red2(float a, float b) { const float x = sw16<Op>(a, b); return sw32<Op>(x, x); }

__device__ __forceinline__ unsigned voff_nat(int l, unsigned pitch) { return (l >> 3) * pitch + 16 * ((l & 7) ^ (((l >> 4) & 1) << 1)); }
__device__ __forceinline__ unsigned voff_spl(int l, unsigned pitch) { return (l >> 3) * pitch + 16 * ((l & 7) ^ (((l >> 4) & 3) << 1)); }
__device__ __forceinline__ unsigned voff_k(int l) { return (l >> 3) * 512 + 16 * ((l & 7) ^ ((l >> 4) & 3)); }

struct Params {
  const void *x[2], *k[2], *v[2], *wq[2], *wo[2];
  void* slab;
  unsigned long long* diag;
};

#define STAMP(i)

template <int V> using IC = std::integral_constant<int, V>;
constexpr f32x4 ZERO4 = {0.f, 0.f, 0.f, 0.f};

__device__ __forceinline__ void issue_x(const Params& p, int P, int b, int w, unsigned lds0) {
  const int l = lane_id();
  const rsrc_t rx = mk_rsrc(p.x[P], b * X_BYTES, X_BYTES);
  sfor<0, 9>([&](auto I) {
    const unsigned pos = (w * 9 + I) * 64 + l, row = pos / 56, c = pos - row * 56;
    dma1(lds0 + (w * 9 + I) * 1024, rx, row * 896 + 16 * (c ^ ((row >> 1) & 7)));
  });
}

template <int P  , bool LAST, int ST>
__device__ __forceinline__ void run_problem(const Params& p, lds_t lds, unsigned lds0, int b, int j, int w, unsigned (&st)[32]) {
  constexpr bool GQA = (P == 0);
  const int hh = w >> 2, cq = w & 3;
  const unsigned ring = lds0 + LDS_RING + w * 8192;
  f32x4 acc[5][4];
  bf16x8 fa[2][4], fb[5];

  const unsigned koff = GQA ? (b * 8 + j) * KV_HEAD + w * 16 * 512 : (b * 16 + 2 * j + hh) * KV_HEAD + cq * 32 * 512;
  const rsrc_t rK = mk_rsrc(p.k[P], koff, KV_HEAD - (GQA ? w * 16 * 512 : cq * 32 * 512));
  auto k_src = [](int g, int i) -> unsigned {
    return GQA ? ((g >> 2) * 256 + (i >> 1) * 128 + (i & 1) * 8) * 512 + (g & 3) * 128 : ((g >> 2) * 128 + i * 8) * 512 + (g & 3) * 128;
  };

  {
    const rsrc_t rW = mk_rsrc(p.wq[P], (512 * j + 64 * w) * 2, W_BYTES - (512 * j + 64 * w) * 2);
    const int l = lane_id(), n = l & 15, g = l >> 4, q = (l >> 2) & 3;
    const unsigned we = voff_nat(l, 8192), ke = voff_k(l);
    const unsigned a0 = LDS_RING + w * 8192 + (8 * g + q) * 128 + (((q >> 1) | ((g & 1) << 1)) * 32) + 8 * (l & 3);
    const unsigned x0 = n * 896 + 16 * (g ^ ((n >> 1) & 7));
    const unsigned qw = hh * 40960 + cq * 10240 + (g >> 1) * 256 + n * 16 + (g & 1) * 8;
    auto rda = [&](auto S, auto U) { lds_t a = lds + (a0 ^ (U * 32)) + (S & 1) * 4096; fa[S & 1][U] = ldtr(a, a + 512); };
    auto rdb = [&](auto S, auto T) { fb[T] = ld128(lds + (x0 ^ ((S & 1) * 64)) + T * 14336 + (S >> 1) * 128); };
    auto piece = [&](auto G, auto I) {
      if constexpr (G < 14) dmap<false, I>(rW, we ^ ((I & 1) * 64), (G * 32 + I * 8) * 8192);
      else dmap<true, I>(rK, ke ^ ((I & 1) * 64), k_src(G - 14, I));
    };
    auto qput = [&](auto T, auto U) {
      *(LAS u32x2*)(lds + qw + (U >> 1) * 5120 + T * 1024 + (U & 1) * 512) = u32x2{pk2(acc[T][U][0], acc[T][U][1]), pk2(acc[T][U][2], acc[T][U][3])};
    };
    wait_vm<4>();
    sfor<0, 4>([&](auto U) { rda(IC<0>{}, U); });
    sfor<0, 5>([&](auto T) { rdb(IC<0>{}, T); });
    set_m0(ring);
    need(fa[0]);
    sfor<0, 4>([&](auto I) { piece(IC<2>{}, I); });
    sfor<0, 14>([&](auto S) {
      constexpr int s = S;
      SBAR();
      if constexpr (s < 13) wait_vm<4>();
      else { bar(); *(LAS f32x2*)(lds + LDS_EXM + w * 512 + l * 8) = f32x2{-INFINITY, -INFINITY}; }
      sfor<0, 20>([&](auto I) {
        constexpr int i = I, t = i >> 2, u = i & 3;
        acc[t][u] = MFMA(fa[s & 1][u], fb[t], s == 0 ? ZERO4 : acc[t][u]);
        if constexpr (s < 13) {
          if constexpr (i < 3) rda(IC<s + 1>{}, IC<i>{});
          if constexpr (i == 4) rda(IC<s + 1>{}, IC<3>{});
          if constexpr (u == 3) rdb(IC<s + 1>{}, IC<t>{});
          if constexpr (i == 9) set_m0(ring + ((s + 1) & 1) * 4096);
          if constexpr (i == 10) need(fa[(s + 1) & 1]);
          if constexpr (i >= 10 && i < 18 && (i & 1) == 0) piece(IC<s + 3>{}, IC<(i - 10) / 2>{});
        } else if constexpr (i >= 6) qput(IC<(i - 6) / 4>{}, IC<(i - 6) % 4>{});
        SBAR();
      });
    });
    STAMP(ST + 1);
    sfor<14, 20>([&](auto I) { qput(IC<I / 4>{}, IC<I % 4>{}); });
    bar();
  }
  STAMP(ST + 2);

  const rsrc_t rV = GQA ? mk_rsrc(p.v[P], (b * 8 + j) * KV_HEAD + hh * 16 * 512 + cq * 128, KV_HEAD - (hh * 16 * 512 + cq * 128))
                        : mk_rsrc(p.v[P], (b * 16 + 2 * j + hh) * KV_HEAD + cq * 128, KV_HEAD - cq * 128);
  constexpr int MT = GQA ? 2 : 1;
  constexpr int NG = GQA ? 8 : 16;
  f32x4 sc[4][10];
  float mx[5 * MT], nm[5 * MT], ls[5 * MT], ev[4];
  u32x2 pq[4][10];
  auto mop = [&](auto ID) {
    constexpr int id = ID, kq = id / 90, k = (id / 9) % 10, part = id % 9, r = GQA ? k : (k >> 1);
    if constexpr (part == 0) { ev[0] = __builtin_fmaf(sc[kq][k][0], SCL, nm[r]); ev[1] = __builtin_fmaf(sc[kq][k][1], SCL, nm[r]); }
    if constexpr (part == 1) ev[0] = __builtin_amdgcn_exp2f(ev[0]);
    if constexpr (part == 2) { ev[2] = __builtin_fmaf(sc[kq][k][2], SCL, nm[r]); ev[3] = __builtin_fmaf(sc[kq][k][3], SCL, nm[r]); }
    if constexpr (part == 3) ev[1] = __builtin_amdgcn_exp2f(ev[1]);
    if constexpr (part == 4) ev[2] = __builtin_amdgcn_exp2f(ev[2]);
    if constexpr (part == 5) { if constexpr (kq == 0 && (GQA || (k & 1) == 0)) ls[r] = ev[0] + ev[1]; else ls[r] = (ls[r] + ev[0]) + ev[1]; }
    if constexpr (part == 6) ev[3] = __builtin_amdgcn_exp2f(ev[3]);
    if constexpr (part == 7) { pq[kq][k][0] = pk2(ev[0], ev[1]); ls[r] += ev[2]; }
    if constexpr (part == 8) { pq[kq][k][1] = pk2(ev[2], ev[3]); ls[r] += ev[3]; }
    if constexpr (part == 0) asm volatile("" : "+v"(ev[0]), "+v"(ev[1]));
    if constexpr (part == 1) asm volatile("" : "+v"(ev[0]));
    if constexpr (part == 2) asm volatile("" : "+v"(ev[2]), "+v"(ev[3]));
    if constexpr (part == 3) asm volatile("" : "+v"(ev[1]));
    if constexpr (part == 4) asm volatile("" : "+v"(ev[2]));
    if constexpr (part == 5) asm volatile("" : "+v"(ls[r]));
    if constexpr (part == 6) asm volatile("" : "+v"(ev[3]));
    if constexpr (part == 7) asm volatile("" : "+v"(ls[r]), "+v"(pq[kq][k][0]));
    if constexpr (part == 8) asm volatile("" : "+v"(ls[r]), "+v"(pq[kq][k][1]));
  };
  auto pput = [&](auto KQ, auto K, unsigned pw) {
    constexpr int kq = KQ, k = K;
    if constexpr (GQA) *(LAS u32x2*)(lds + pw + (kq & 1) * 40960 + (k / 5) * 20480 + (k % 5) * 1024) = pq[kq][k];
    else if constexpr (k & 1) *(LAS u32x4*)(lds + pw + (kq & 1) * 40960 + (k / 2) * 1024) = u32x4{pq[kq][k - 1][0], pq[kq][k - 1][1], pq[kq][k][0], pq[kq][k][1]};
  };
  bool grown;
  unsigned pw;
  {
    const int l = lane_id(), n = l & 15, g = l >> 4;
    const unsigned ke = voff_k(l), vv = voff_spl(l, 512);
    const unsigned k0 = LDS_RING + w * 8192 + n * 128 + 16 * (g ^ ((n >> 1) & 7));
    const unsigned q0 = (GQA ? 0 : hh * 40960) + l * 16;
    constexpr int NSUB = 2 * MT;
    auto rdk = [&](auto G, auto I) { fa[G & 1][I] = ld128(lds + (k0 ^ ((I >> 1) * 64)) + (I & 1) * 2048 + (G & 1) * 4096); };
    auto rdq = [&](auto Z, auto T) {
      constexpr int z = Z, gi = z / NSUB, e = (z % NSUB) / MT, h = z % MT;
      fb[T] = ld128(lds + q0 + h * 40960 + ((2 * (gi & 3) + e) * 5 + T) * 1024);
    };
    auto piece = [&](auto G, auto I) {
      if constexpr (G < NG) dmap<true, I>(rK, ke ^ ((I & 1) * 64), k_src(G, I));
      else if constexpr (!GQA) dmap<true, I>(rV, vv, ((G - NG) * 32 + I * 8) * 512);
      else dmap<true, I>(rV, vv, ((2 * (G - NG) + (I >> 1)) * 32 + (I & 1) * 8) * 512);
    };
    auto qmax = [&](auto KQ, auto K) {
      constexpr int kq = KQ, k = K, r = GQA ? k : (k >> 1);
      if constexpr ((kq & 1) == 0 && (GQA || (k & 1) == 0)) mx[r] = max3(max3(sc[kq][k][0], sc[kq][k][1], sc[kq][k][2]), sc[kq][k][3], sc[kq][k][3]);
      else mx[r] = max3(max3(mx[r], sc[kq][k][0], sc[kq][k][1]), sc[kq][k][2], sc[kq][k][3]);
      asm volatile("" : "+v"(mx[r]));
    };
    auto xmax = [&](auto HALF, auto C) {
      const int l2 = lane_id(), g2 = l2 >> 4;
      LAS float* em = (LAS float*)(lds + LDS_EXM + HALF * 1536 + ((GQA ? 0 : hh) * 16 + (l2 & 15)) * 48);
      if constexpr (C == 0) __builtin_amdgcn_ds_fmaxf(em + g2, red4<OpMax>(mx[0], mx[1], mx[2], mx[3]), 0, 0, false);
      if constexpr (C == 1 && GQA) __builtin_amdgcn_ds_fmaxf(em + (g2 & 1) * 192 + 4, red2<OpMax>(mx[4], mx[5 * MT - 1]), 0, 0, false);
      if constexpr (C == 1 && !GQA) __builtin_amdgcn_ds_fmaxf(em + 4, red2<OpMax>(mx[4], mx[4]), 0, 0, false);
      if constexpr (C == 2 && GQA) __builtin_amdgcn_ds_fmaxf(em + 192 + g2, red4<OpMax>(mx[5], mx[6], mx[7], mx[8]), 0, 0, false);
    };
    auto rmax = [&](auto HALF) {
      LAS float* em = (LAS float*)(lds + LDS_EXM + HALF * 1536 + ((GQA ? 0 : hh) * 16 + (lane_id() & 15)) * 48);
      sfor<0, MT>([&](auto H) {
        const f32x4 v = *(LAS const f32x4*)(em + H * 192);
        sfor<0, 4>([&](auto T) { mx[H * 5 + T] = v[T + 0]; });
        mx[H * 5 + 4] = em[H * 192 + 4];
      });
    };
    wait_vm<4>();
    sfor<0, 4>([&](auto I) { rdk(IC<0>{}, I); });
    sfor<0, 5>([&](auto T) { rdq(IC<0>{}, T); });
    set_m0(ring);
    need(fa[0]);
    sfor<0, 4>([&](auto I) { piece(IC<2>{}, I); });
    sfor<0, 32>([&](auto Z) {
      constexpr int z = Z, gi = z / NSUB, sub = z % NSUB, e = sub / MT, h = sub % MT;
      SBAR();
      if constexpr (z == 19) { bar(); rmax(IC<0>{}); }
      if constexpr (sub == 0 && gi + 1 < NG) wait_vm<4>();
      sfor<0, 10>([&](auto I) {
        constexpr int i = I, t = i >> 1, u = i & 1;
        constexpr int kq = GQA ? 2 * (gi >> 2) + u : (gi >> 2), k = GQA ? 5 * h + t : i;
        sc[kq][k] = MFMA(fa[gi & 1][2 * e + u], fb[t], ((gi & 3) == 0 && e == 0) ? ZERO4 : sc[kq][k]);
        if constexpr (u == 1 && z + 1 < 32) rdq(IC<z + 1>{}, IC<t>{});
        if constexpr (gi + 1 < NG) {
          if constexpr (sub == 0 && u == 0 && i < 8) rdk(IC<gi + 1>{}, IC<t>{});
          if constexpr (sub == 1 && i == 0) set_m0(ring + ((gi + 1) & 1) * 4096);
          if constexpr (sub == 1 && i == 2) need(fa[(gi + 1) & 1]);
          if constexpr (sub == 1 && i >= 2 && u == 0) piece(IC<gi + 3>{}, IC<(i - 2) / 2>{});
        }
        if constexpr (!LAST && z == 4 && i == 9)
          warm(lds0, mk_rsrc(p.wo[P], 0, W_BYTES), 512 * j * 896 + (b >> 1) * 28672 + ((w & 3) * 64 + lane_id()) * 128);
        if constexpr (z == 16 || z == 17) qmax(IC<z - 16>{}, I);
        if constexpr (z == 18 && (i == 1 || i == 4 || i == 7)) xmax(IC<0>{}, IC<i / 3>{});
        if constexpr (z == 19 && i >= 5) sfor<0, MT>([&](auto H) { nm[H * 5 + i - 5] = mul1(mx[H * 5 + i - 5], -SCL); });
        if constexpr (z >= 20) { constexpr int x = (z - 20) * 10 + i; sfor<(3 * x + 1) / 2, (3 * x + 4) / 2>(mop); }
        if constexpr (!GQA && z == 26) qmax(IC<2>{}, I);
        SBAR();
      });
    });
    STAMP(ST + 3);
    sfor<4 - MT, 4>([&](auto KQ) { sfor<0, 10>([&](auto K) { qmax(KQ, K); }); });
    sfor<0, 3>([&](auto C) { xmax(IC<1>{}, C); });
    pw = (GQA ? (w >> 1) * 5120 + (w & 1) * 8 : hh * 20480 + cq * 5120) + lane_id() * 16;
    bar();
    rmax(IC<1>{});
    sfor<0, 2>([&](auto KQ) { sfor<0, 10>([&](auto K) { pput(KQ, K, pw); }); });
    bool any = false;
    sfor<0, 5 * MT>([&](auto R) { any |= __builtin_fmaf(mx[R], SCL, nm[R]) > GROW; });
    grown = __builtin_amdgcn_ballot_w64(any) != 0;
    if (grown) {
      sfor<0, 5 * MT>([&](auto R) {
        const float d = __builtin_fmaf(mx[R], SCL, nm[R]);
        if (d > GROW) { ls[R] *= __builtin_amdgcn_exp2f(-d); nm[R] = -SCL * mx[R]; }
      });
    }
  }
  STAMP(ST + 4);

  const rsrc_t rO = mk_rsrc(p.wo[P], 512 * j * 896 + (w < 7 ? w : 0) * 128, w < 7 ? W_BYTES - (512 * j * 896 + w * 128) : 0u);
  {
    const int l = lane_id(), g = l >> 4, q = (l >> 2) & 3;
    const unsigned vv = voff_spl(l, 512);
    const unsigned a0 = LDS_RING + (GQA ? cq : w) * 8192 + (4 * g + q) * 128 + (((q >> 1) | ((g & 1) << 1)) * 32) + 8 * (l & 3);
    const unsigned p0 = hh * 20480 + l * 16;
    auto rda = [&](auto S, auto U) {
      lds_t a = lds + (a0 ^ (U * 32)) + (GQA ? (S & 3) * 2048 : (S & 1) * 4096);
      fa[S & 1][U] = ldtr(a, a + (GQA ? 32768 : 2048));
    };
    auto rdb = [&](auto S, auto T) { fb[T] = ld128(lds + p0 + ((S >> 2) & 1) * 40960 + ((S & 3) * 5 + T) * 1024); };
    unsigned ov = 0, ow = 0;
    auto piece = [&](auto G, auto I) {
      if constexpr (G < 16) dmap<true, I>(rV, vv, (G * 32 + I * 8) * 512);
      else dmap<false, I>(rO, ov, ((G - 16) * (GQA ? 16 : 32) + I * 8) * 896);
    };
    float rl[5];
    f32x4 lv[2];
    auto oput = [&](auto T, auto H) {
      const f32x4 u = acc[T][2 * H], v = acc[T][2 * H + 1];
      const float r = rl[T];
      *(LAS u32x4*)(lds + ow + H * 5120 + T * 1024) = u32x4{pk2(mul1(u[0], r), mul1(u[1], r)), pk2(mul1(u[2], r), mul1(u[3], r)),
                                                            pk2(mul1(v[0], r), mul1(v[1], r)), pk2(mul1(v[2], r), mul1(v[3], r))};
    };
    wait_vm_bar<GQA ? 6 : 4>();
    STAMP(ST + 5);
    sfor<0, 4>([&](auto U) { rda(IC<0>{}, U); });
    sfor<0, 5>([&](auto T) { rdb(IC<0>{}, T); });
    if constexpr (!GQA) { set_m0(ring); need(fa[0]); sfor<0, 4>([&](auto I) { piece(IC<2>{}, I); }); }
    sfor<0, 16>([&](auto S) {
      constexpr int s = S;
      SBAR();
      if constexpr (GQA) {
        if constexpr ((s & 3) == 3) wait_vm_bar<4>();
        else { need(fa[s & 1]); wait_vm_bar_raw<4>(); }
      } else {
        if constexpr (s == 15) bar();
        else if constexpr ((s & 3) == 3) wait_vm_bar<4>();
        else wait_vm<4>();
      }
      if constexpr (s == 8) {
        if (grown) {
          LAS float* em = (LAS float*)(lds + LDS_EXM + (hh * 16 + (lane_id() & 15)) * 48);
          sfor<0, 5>([&](auto T) {
            const float d = __builtin_fmaf(em[384 + T], SCL, mul1(em[T + 0], -SCL));
            const float a = d > GROW ? __builtin_amdgcn_exp2f(-d) : 1.f;
            sfor<0, 4>([&](auto U) { acc[T][U] *= a; });
          });
        }
      }
      if constexpr (s == 11) ov = voff_spl(lane_id(), 896);
      if constexpr (s == 15) ow = hh * 40960 + cq * 10240 + lane_id() * 16;
      sfor<0, 20>([&](auto I) {
        constexpr int i = I, t = i >> 2, u = i & 3;
        acc[t][u] = MFMA(fa[s & 1][u], fb[t], s == 0 ? ZERO4 : acc[t][u]);
        if constexpr (GQA) {
          if constexpr (i == 0) set_m0(ring + (s & 3) * 2048);
          if constexpr (i == 1 || i == 2) piece(IC<s + 4>{}, IC<i - 1>{});
          if constexpr (s < 15 && i >= 4 && i < 7) rda(IC<s + 1>{}, IC<i - 4>{});
          if constexpr (s < 15 && i == 8) rda(IC<s + 1>{}, IC<3>{});
        } else if constexpr (s < 15) {
          if constexpr (i < 3) rda(IC<s + 1>{}, IC<i>{});
          if constexpr (i == 4) rda(IC<s + 1>{}, IC<3>{});
          if constexpr (i == 9) set_m0(ring + ((s + 1) & 1) * 4096);
          if constexpr (i == 10) need(fa[(s + 1) & 1]);
          if constexpr (i >= 10 && i < 18 && (i & 1) == 0) piece(IC<s + 3>{}, IC<(i - 10) / 2>{});
        }
        if constexpr (s < 15 && u == 3) rdb(IC<s + 1>{}, IC<t>{});
        if constexpr (s < 10 && i >= 1 && i < 19) mop(IC<(180 + 18 * s + i - 1) % 360>{});
        if constexpr ((s == 5 || s == 10) && (i & 1) == 0) pput(IC<s == 5 ? 2 : 3>{}, IC<i / 2>{}, pw);
        if constexpr (s == 10 && (i == 3 || i == 9 || i == 15)) {
          const int l2 = lane_id();
          LAS float* el = (LAS float*)(lds + LDS_EXL + ((GQA ? 0 : hh) * 80 + (l2 & 15)) * 48 + (GQA ? w : cq) * 4);
          if constexpr (i == 3) el[(l2 >> 4) * 192] = red4<OpAdd>(ls[0], ls[1], ls[2], ls[3]);
          if constexpr (i == 9 && GQA) el[768 + ((l2 >> 4) & 1) * 960] = red2<OpAdd>(ls[4], ls[5 * MT - 1]);
          if constexpr (i == 9 && !GQA) el[768] = red2<OpAdd>(ls[4], ls[4]);
          if constexpr (i == 15 && GQA) el[960 + (l2 >> 4) * 192] = red4<OpAdd>(ls[5], ls[6], ls[7], ls[8]);
        }
        if constexpr (!LAST && s == 12 && (i == 5 || i == 7))
          warm(lds0, mk_rsrc(p.x[1 - P], b * X_BYTES, X_BYTES), ((i == 5 ? w : 8 + w) * 64 + lane_id()) * 128);
        if constexpr ((s == 12 || s == 13) && (i % 8) == 1 && (s - 12) * 3 + i / 8 < 5) {
          lds_t e = lds + LDS_EXL + (hh * 80 + ((s - 12) * 3 + i / 8) * 16 + (lane_id() & 15)) * 48;
          lv[0] = *(LAS const f32x4*)e;
          if constexpr (GQA) lv[1] = *(LAS const f32x4*)(e + 16);
        }
        if constexpr ((s == 12 || s == 13) && i == (i / 8) * 8 + (i < 16 ? 6 : 3) && (s - 12) * 3 + i / 8 < 5) {
          float sum = ((lv[0][0] + lv[0][1]) + lv[0][2]) + lv[0][3];
          if constexpr (GQA) sum = (((sum + lv[1][0]) + lv[1][1]) + lv[1][2]) + lv[1][3];
          rl[((s - 12) * 3 + i / 8) % 5] = __builtin_amdgcn_rcpf(sum);
        }
        if constexpr (s == 15 && i >= 6 && (i & 1) == 0) oput(IC<(i - 6) / 4>{}, IC<((i - 6) / 2) % 2>{});
        SBAR();
      });
    });
    STAMP(ST + 6);
    sfor<7, 10>([&](auto I) { oput(IC<I / 2>{}, IC<I % 2>{}); });
    bar();
  }
  STAMP(ST + 7);

  {
    const rsrc_t rN = mk_rsrc(p.wq[1 - P], (512 * j + 64 * w) * 2, LAST ? 0u : W_BYTES - (512 * j + 64 * w) * 2);
    const int l = lane_id(), g = l >> 4, q = (l >> 2) & 3;
    const unsigned ov = voff_spl(l, 896), we = voff_nat(l, 8192);
    auto piece = [&](auto G, auto I) {
      if constexpr (G < 16) dmap<false, I>(rO, ov, (G * 32 + I * 8) * 896);
      else if constexpr (!LAST) dmap<false, I>(rN, we ^ ((I & 1) * 64), ((G - 16) * 32 + I * 8) * 8192);
    };
    if (w < 7) {
      const unsigned a0 = LDS_RING + w * 8192 + (4 * g + q) * 128 + (((q >> 1) | ((g & 1) << 1)) * 32) + 8 * (l & 3);
      auto rda = [&](auto S, auto U) { lds_t a = lds + (a0 ^ (U * 32)) + (S & 1) * 4096; fa[S & 1][U] = ldtr(a, a + 2048); };
      auto rdb = [&](auto S, auto T) { fb[T] = ld128(lds + l * 16 + (S * 5 + T) * 1024); };
      u32x4* slab = (u32x4*)((char*)p.slab + (size_t)((P * 32 + b) * 8 + j) * SLAB + w * 10240 + l * 16);
      auto sput = [&](auto T, auto H) {
        const f32x4 u = acc[T][2 * H], v = acc[T][2 * H + 1];
        auto r0 = __builtin_amdgcn_permlane16_swap(pk2(u[0], u[1]), pk2(v[0], v[1]), false, false);
        auto r1 = __builtin_amdgcn_permlane16_swap(pk2(u[2], u[3]), pk2(v[2], v[3]), false, false);
        slab[(T * 2 + H) * 64] = u32x4{r0[0], r1[0], r0[1], r1[1]};
      };
      wait_vm<4>();
      sfor<0, 4>([&](auto U) { rda(IC<0>{}, U); });
      sfor<0, 5>([&](auto T) { rdb(IC<0>{}, T); });
      set_m0(ring);
      need(fa[0]);
      sfor<0, 4>([&](auto I) { piece(IC<2>{}, I); });
      sfor<0, 16>([&](auto S) {
        constexpr int s = S;
        SBAR();
        if constexpr (s < 15) { if constexpr (!(LAST && (ABL & 1))) wait_vm<(LAST && s == 14) ? 0 : 4>(); }
        else if constexpr (!LAST) { bar(); issue_x(p, 1 - P, b, w, lds0); }
        sfor<0, 20>([&](auto I) {
          constexpr int i = I, t = i >> 2, u = i & 3;
          acc[t][u] = MFMA(fa[s & 1][u], fb[t], s == 0 ? ZERO4 : acc[t][u]);
          if constexpr (s < 15) {
            if constexpr (!(LAST && (ABL & 4) && s > 0)) {
            if constexpr (i < 3) rda(IC<s + 1>{}, IC<i>{});
            if constexpr (i == 4) rda(IC<s + 1>{}, IC<3>{}); }
            if constexpr (!(LAST && (ABL & 8)))
            if constexpr (u == 3) rdb(IC<s + 1>{}, IC<t>{});
            if constexpr (!(LAST && (s >= 13 || (ABL & 2)))) {
              if constexpr (i == 9) set_m0(ring + ((s + 1) & 1) * 4096);
              if constexpr (i == 10) need(fa[(s + 1) & 1]);
              if constexpr (i >= 10 && i < 18 && (i & 1) == 0) piece(IC<s + 3>{}, IC<(i - 10) / 2>{});
            }
          } else if constexpr (i >= 6 && (i & 1) == 0) sput(IC<(i - 6) / 4>{}, IC<((i - 6) / 2) % 2>{});
          SBAR();
        });
      });
      STAMP(ST + 8);
      sfor<7, 10>([&](auto I) { sput(IC<I / 2>{}, IC<I % 2>{}); });
      if constexpr (!LAST) wait_vm_bar<10>();
    } else if constexpr (!LAST) {
      wait_vm<0>();
      set_m0(ring);
      sfor<0, 4>([&](auto I) { piece(IC<16>{}, I); });
      set_m0(ring + 4096);
      sfor<0, 4>([&](auto I) { piece(IC<17>{}, I); });
      bar();
      issue_x(p, 1 - P, b, w, lds0);
      wait_vm_bar<0>();
    }
  }
  STAMP(ST + 9);
}

__global__ __launch_bounds__(512, 2) void fused_attn_kernel(Params p) {
  extern __shared__ __attribute__((aligned(16))) char smem[];
  lds_t lds = (lds_t)smem;
  const unsigned lds0 = (unsigned)(uintptr_t)lds;
  const int w = __builtin_amdgcn_readfirstlane(threadIdx.x >> 6);
  const int j = blockIdx.x & 7, b = blockIdx.x >> 3;
  const int first = b & 1;
  unsigned st[32];
  STAMP(0);
  {
    issue_x(p, first, b, w, lds0);
    const int l = lane_id();
    const rsrc_t rW = mk_rsrc(p.wq[first], (512 * j + 64 * w) * 2, W_BYTES - (512 * j + 64 * w) * 2);
    const unsigned we = voff_nat(l, 8192), ring = lds0 + LDS_RING + w * 8192;
    sfor<0, 2>([&](auto G) {
      set_m0(ring + G * 4096);
      sfor<0, 4>([&](auto I) { dmap<false, I>(rW, we ^ ((I & 1) * 64), (G * 32 + I * 8) * 8192); });
    });
    const unsigned id = (w & 3) * 64 + l;
    warm(lds0, mk_rsrc(p.wq[first], 0, W_BYTES), ((b >> 1) * 28 + (id >> 3)) * 8192 + 1024 * j + (id & 7) * 128);
    wait_vm_bar<9>();
  }
  STAMP(10);
  if (first == 0) {
    run_problem<0, false, 0>(p, lds, lds0, b, j, w, st);
    run_problem<1, true, 10>(p, lds, lds0, b, j, w, st);
  } else {
    run_problem<1, false, 0>(p, lds, lds0, b, j, w, st);
    run_problem<0, true, 10>(p, lds, lds0, b, j, w, st);
  }
}

__global__ __launch_bounds__(256) void reduce_kernel(const u32x4* __restrict__ slab, u32x4* __restrict__ out) {
  const int idx = blockIdx.x * 256 + threadIdx.x;
  const int pb = idx / 4480, r = idx - pb * 4480;
  float s[8];
#pragma unroll
  for (int i = 0; i < 8; ++i) s[i] = 0.f;
  u32x4 v[8];
#pragma unroll
  for (int jj = 0; jj < 8; ++jj) v[jj] = slab[(size_t)(pb * 8 + jj) * 4480 + r];
#pragma unroll
  for (int jj = 0; jj < 8; ++jj)
#pragma unroll
    for (int i = 0; i < 4; ++i) { s[2 * i] += __uint_as_float(v[jj][i] << 16); s[2 * i + 1] += __uint_as_float(v[jj][i] & 0xffff0000u); }
  const int l = r & 63, f = r >> 6, w = f / 10, t = (f - w * 10) >> 1, h = f & 1, n = l & 15, g = l >> 4;
  const int P = pb >> 5, b = pb & 31;
  const int col = P * 448 + 64 * w + 16 * (2 * h + (g & 1)) + 8 * (g >> 1);
  out[((size_t)(b * 80 + t * 16 + n) * 896 + col) >> 3] = u32x4{pk2(s[0], s[1]), pk2(s[2], s[3]), pk2(s[4], s[5]), pk2(s[6], s[7])};
}

extern "C" void kernel_launch(void* const* d_in, const int* in_sizes, int n_in, void* d_out, int out_size, void* d_ws,
                              size_t ws_size, hipStream_t stream) {
  Params p;
  for (int i = 0; i < 2; ++i) {
    p.x[i] = d_in[5 * i + 0]; p.k[i] = d_in[5 * i + 1]; p.v[i] = d_in[5 * i + 2]; p.wq[i] = d_in[5 * i + 3]; p.wo[i] = d_in[5 * i + 4];
  }
  p.slab = d_ws;
  p.diag = (unsigned long long*)((char*)d_ws + (64u << 20));
  (void)hipFuncSetAttribute((const void*)fused_attn_kernel, hipFuncAttributeMaxDynamicSharedMemorySize, LDS_TOTAL);
  fused_attn_kernel<<<256, 512, LDS_TOTAL, stream>>>(p);
  reduce_kernel<<<2 * 32 * 4480 / 256, 256, 0, stream>>>((const u32x4*)d_ws, (u32x4*)d_out);
}
```

```cpp
#include <hip/hip_runtime.h>
#include <hip/hip_bf16.h>
#include <stdint.h>
#include <type_traits>

typedef short bf16x8 __attribute__((ext_vector_type(8)));
typedef short s16x4 __attribute__((ext_vector_type(4)));
typedef float f32x4 __attribute__((ext_vector_type(4)));
typedef float f32x2 __attribute__((ext_vector_type(2)));
typedef __bf16 bf16x2 __attribute__((ext_vector_type(2)));
typedef unsigned u32x4 __attribute__((ext_vector_type(4)));
typedef unsigned u32x2 __attribute__((ext_vector_type(2)));
#define LAS __attribute__((address_space(3)))
typedef __amdgpu_buffer_rsrc_t rsrc_t;
typedef LAS char* lds_t;

#define DIAG 0
#define ABL 0
#define WARMNT 1
#define FAKEKV 0

constexpr int LDS_RING = 81920;
constexpr int LDS_EXM = 147456;
constexpr int LDS_EXL = 151552;
constexpr int LDS_DUMMY = 159232;
constexpr int LDS_TOTAL = 159488;
constexpr unsigned X_BYTES = 80 * 448 * 2;
constexpr unsigned W_BYTES = 448 * 4096 * 2;
constexpr unsigned KV_HEAD = 512 * 256 * 2;
constexpr unsigned SLAB = 80 * 448 * 2;
constexpr float SCL = 0.0625f * 1.4426950408889634f;
constexpr float GROW = 20.f;

template <int I, int N, class F>
__device__ __forceinline__ void sfor(F&& f) {
  if constexpr (I < N) { f(std::integral_constant<int, I>{}); sfor<I + 1, N>(f); }
}
#define SBAR() __builtin_amdgcn_sched_barrier(0)
#define MFMA(a, b, c) __builtin_amdgcn_mfma_f32_16x16x32_bf16(a, b, c, 0, 0, 0)
template <int N> __device__ __forceinline__ void wait_vm() { asm volatile("s_waitcnt vmcnt(%0)" ::"i"(N) : "memory"); }
__device__ __forceinline__ void bar() { asm volatile("s_waitcnt lgkmcnt(0)\n\ts_barrier" ::: "memory"); }
template <int N> __device__ __forceinline__ void wait_vm_bar_raw() { asm volatile("s_waitcnt vmcnt(%0)\n\ts_barrier" ::"i"(N) : "memory"); }
template <int N> __device__ __forceinline__ void wait_vm_bar() { asm volatile("s_waitcnt vmcnt(%0) lgkmcnt(0)\n\ts_barrier" ::"i"(N) : "memory"); }
__device__ __forceinline__ int lane_id() {
  int l; asm volatile("v_mbcnt_lo_u32_b32 %0, -1, 0\n\tv_mbcnt_hi_u32_b32 %0, -1, %0" : "=v"(l)); return l;
}
__device__ __forceinline__ unsigned pk2(float a, float b) {
  return __builtin_bit_cast(unsigned, __builtin_convertvector(f32x2{a, b}, bf16x2));
}
__device__ __forceinline__ rsrc_t mk_rsrc(const void* p, unsigned off, unsigned bytes) {
  return __builtin_amdgcn_make_buffer_rsrc((void*)((const char*)p + off), (short)0, (int)bytes, 0x00020000);
}
__device__ __forceinline__ void need(const bf16x8 (&f)[4]) { asm volatile("" ::"v"(f[0]), "v"(f[1]), "v"(f[2]), "v"(f[3]) : "memory"); }
__device__ __forceinline__ void set_m0(unsigned m0v) { asm volatile("s_mov_b32 m0, %0\n\ts_nop 0" ::"s"(m0v) : "memory"); }
template <bool NT, int I>
__device__ __forceinline__ void dmap(rsrc_t r, unsigned v, unsigned src) {
  if constexpr (NT && !FAKEKV) asm volatile("buffer_load_dwordx4 %0, %1, %2 offen offset:%3 nt lds" ::"v"(v), "s"(r), "s"(src - 1024u * I), "i"(1024 * I) : "memory");
  else asm volatile("buffer_load_dwordx4 %0, %1, %2 offen offset:%3 lds" ::"v"(v), "s"(r), "s"(src - 1024u * I), "i"(1024 * I) : "memory");
}
__device__ __forceinline__ void dma1(unsigned m0v, rsrc_t r, unsigned v) {
  asm volatile("s_mov_b32 m0, %0\n\ts_nop 0\n\tbuffer_load_dwordx4 %1, %2, 0 offen lds" ::"s"(m0v), "v"(v), "s"(r) : "memory");
}
template <bool NT = false>
__device__ __forceinline__ void warm(unsigned lds0, rsrc_t r, unsigned v) {
  if constexpr (NT) asm volatile("s_mov_b32 m0, %0\n\ts_nop 0\n\tbuffer_load_dword %1, %2, 0 offen nt lds" ::"s"(lds0 + LDS_DUMMY), "v"(v), "s"(r) : "memory");
  else asm volatile("s_mov_b32 m0, %0\n\ts_nop 0\n\tbuffer_load_dword %1, %2, 0 offen lds" ::"s"(lds0 + LDS_DUMMY), "v"(v), "s"(r) : "memory");
}
extern "C" __device__ float llvm_fmul_legacy(float, float) __asm("llvm.amdgcn.fmul.legacy");
__device__ __forceinline__ float mul1(float a, float b) { return llvm_fmul_legacy(a, b); }
__device__ __forceinline__ bf16x8 ld128(lds_t p) { return *(LAS const bf16x8*)p; }
__device__ __forceinline__ bf16x8 ldtr(lds_t p0, lds_t p1) {
  s16x4 lo = __builtin_amdgcn_ds_read_tr16_b64_v4i16((LAS s16x4*)p0);
  s16x4 hi = __builtin_amdgcn_ds_read_tr16_b64_v4i16((LAS s16x4*)p1);
  return __builtin_shufflevector(lo, hi, 0, 1, 2, 3, 4, 5, 6, 7);
}
__device__ __forceinline__ float max3(float a, float b, float c) { return __builtin_fmaxf(__builtin_fmaxf(a, b), c); }
struct OpMax { static __device__ __forceinline__ float f(float a, float b) { return max3(a, b, b); } };
struct OpAdd { static __device__ __forceinline__ float f(float a, float b) { return a + b; } };
template <class Op> __device__ __forceinline__ float sw16(float a, float b) {
  auto r = __builtin_amdgcn_permlane16_swap(__float_as_uint(a), __float_as_uint(b), false, false);
  return Op::f(__uint_as_float(r[0]), __uint_as_float(r[1]));
}
template <class Op> __device__ __forceinline__ float sw32(float a, float b) {
  auto r = __builtin_amdgcn_permlane32_swap(__float_as_uint(a), __float_as_uint(b), false, false);
  return Op::f(__uint_as_float(r[0]), __uint_as_float(r[1]));
}
template <class Op> __device__ __forceinline__ float red4(float a, float b, float c, float d) { return sw32<Op>(sw16<Op>(a, b), sw16<Op>(c, d)); }
template <class Op> __device__ __forceinline__ float red2(float a, float b) { const float x = sw16<Op>(a, b); return sw32<Op>(x, x); }

__device__ __forceinline__ unsigned voff_nat(int l, unsigned pitch) { return (l >> 3) * pitch + 16 * ((l & 7) ^ (((l >> 4) & 1) << 1)); }
__device__ __forceinline__ unsigned voff_spl(int l, unsigned pitch) { return (l >> 3) * pitch + 16 * ((l & 7) ^ (((l >> 4) & 3) << 1)); }
__device__ __forceinline__ unsigned voff_k(int l) { return (l >> 3) * 512 + 16 * ((l & 7) ^ ((l >> 4) & 3)); }

struct Params {
  const void *x[2], *k[2], *v[2], *wq[2], *wo[2];
  void* slab;
  unsigned* cnt;
  void* out;
  unsigned long long* diag;
};

#define STAMP(i)
#define STAMP2(ph, i)

template <int V> using IC = std::integral_constant<int, V>;
constexpr f32x4 ZERO4 = {0.f, 0.f, 0.f, 0.f};

__device__ __forceinline__ void issue_x(const Params& p, int P, int b, int w, unsigned lds0) {
  const int l = lane_id();
  const rsrc_t rx = mk_rsrc(p.x[P], b * X_BYTES, X_BYTES);
  sfor<0, 9>([&](auto I) {
    const unsigned pos = (w * 9 + I) * 64 + l, row = pos / 56, c = pos - row * 56;
    dma1(lds0 + (w * 9 + I) * 1024, rx, row * 896 + 16 * (c ^ ((row >> 1) & 7)));
  });
}

typedef __attribute__((address_space(1))) unsigned gu32;
__device__ __forceinline__ void arrive(const Params& p, int b, int which) {
  if (lane_id() == 0) __hip_atomic_fetch_add((gu32*)p.cnt + (b * 2 + which) * 32, 1u, __ATOMIC_RELAXED, __HIP_MEMORY_SCOPE_AGENT);
}
__device__ __forceinline__ void wait_all(const Params& p, int b, int which) {
  gu32* cnt = (gu32*)p.cnt + (b * 2 + which) * 32;
  for (unsigned spins = 0; spins < (1u << 22); ++spins) {
    if ((unsigned)__builtin_amdgcn_readfirstlane(__hip_atomic_load(cnt, __ATOMIC_RELAXED, __HIP_MEMORY_SCOPE_AGENT)) >= 8u) break;
    __builtin_amdgcn_s_sleep(4);
  }
  asm volatile("" ::: "memory");
}
template <int N>
__device__ __forceinline__ void reduce_rows(const Params& p, int P, int b, int j, int first, int step) {
  const int l = lane_id(), n = l & 15, g = l >> 4;
  const int r0 = (70 * j) >> 3, nr = ((70 * (j + 1)) >> 3) - r0;
  const rsrc_t rS = mk_rsrc(p.slab, (P * 32 + b) * 8 * SLAB, 8 * SLAB);
  u32x4 v[N][8];
  sfor<0, N>([&](auto K) {
    const int it = first + step * K;
    const unsigned off = it < nr ? (r0 + it) * 1024 + l * 16 : 0x80000000u;
    sfor<0, 8>([&](auto J) { v[K][J] = __builtin_amdgcn_raw_buffer_load_b128(rS, off, J * SLAB, 16  ); });
  });
  sfor<0, N>([&](auto K) {
    const int it = first + step * K, f = r0 + it;
    if (it < nr) {
      float sm[8];
      sfor<0, 4>([&](auto I) { sm[2 * I] = __uint_as_float(v[K][0][I + 0] << 16); sm[2 * I + 1] = __uint_as_float(v[K][0][I + 0] & 0xffff0000u); });
      sfor<1, 8>([&](auto J) {
        sfor<0, 4>([&](auto I) { sm[2 * I] += __uint_as_float(v[K][J][I + 0] << 16); sm[2 * I + 1] += __uint_as_float(v[K][J][I + 0] & 0xffff0000u); });
      });
      const int ww = f / 10, t = (f - ww * 10) >> 1, h = f & 1;
      const int col = P * 448 + 64 * ww + 16 * (2 * h + (g & 1)) + 8 * (g >> 1);
      *(u32x4*)((char*)p.out + ((size_t)(b * 80 + t * 16 + n) * 896 + col) * 2) = u32x4{pk2(sm[0], sm[1]), pk2(sm[2], sm[3]), pk2(sm[4], sm[5]), pk2(sm[6], sm[7])};
    }
  });
}

template <int P  , bool LAST, int ST>
__device__ __forceinline__ void run_problem(const Params& p, lds_t lds, unsigned lds0, int b, int j, int w) {
  constexpr bool GQA = (P == 0);
  const int bk = FAKEKV ? 0 : b;
  const int hh = w >> 2, cq = w & 3;
  const unsigned ring = lds0 + LDS_RING + w * 8192;
  f32x4 acc[5][4];
  bf16x8 fa[2][4], fb[5];

  const unsigned koff = GQA ? (bk * 8 + j) * KV_HEAD + w * 16 * 512 : (bk * 16 + 2 * j + hh) * KV_HEAD + cq * 32 * 512;
  const rsrc_t rK = mk_rsrc(p.k[P], koff, KV_HEAD - (GQA ? w * 16 * 512 : cq * 32 * 512));
  auto k_src = [](int g, int i) -> unsigned {
    return GQA ? ((g >> 2) * 256 + (i >> 1) * 128 + (i & 1) * 8) * 512 + (g & 3) * 128 : ((g >> 2) * 128 + i * 8) * 512 + (g & 3) * 128;
  };

  {
    const rsrc_t rW = mk_rsrc(p.wq[P], (512 * j + 64 * w) * 2, W_BYTES - (512 * j + 64 * w) * 2);
    const int l = lane_id(), n = l & 15, g = l >> 4, q = (l >> 2) & 3;
    const unsigned we = voff_nat(l, 8192), ke = voff_k(l);
    const unsigned a0 = LDS_RING + w * 8192 + (8 * g + q) * 128 + (((q >> 1) | ((g & 1) << 1)) * 32) + 8 * (l & 3);
    const unsigned x0 = n * 896 + 16 * (g ^ ((n >> 1) & 7));
    const unsigned qw = hh * 40960 + cq * 10240 + (g >> 1) * 256 + n * 16 + (g & 1) * 8;
    auto rda = [&](auto S, auto U) { lds_t a = lds + (a0 ^ (U * 32)) + (S & 1) * 4096; fa[S & 1][U] = ldtr(a, a + 512); };
    auto rdb = [&](auto S, auto T) { fb[T] = ld128(lds + (x0 ^ ((S & 1) * 64)) + T * 14336 + (S >> 1) * 128); };
    auto piece = [&](auto G, auto I) {
      if constexpr (G < 14) dmap<false, I>(rW, we ^ ((I & 1) * 64), (G * 32 + I * 8) * 8192);
      else dmap<true, I>(rK, ke ^ ((I & 1) * 64), k_src(G - 14, I));
    };
    auto qput = [&](auto T, auto U) {
      *(LAS u32x2*)(lds + qw + (U >> 1) * 5120 + T * 1024 + (U & 1) * 512) = u32x2{pk2(acc[T][U][0], acc[T][U][1]), pk2(acc[T][U][2], acc[T][U][3])};
    };
    wait_vm<4>();
    sfor<0, 4>([&](auto U) { rda(IC<0>{}, U); });
    sfor<0, 5>([&](auto T) { rdb(IC<0>{}, T); });
    set_m0(ring);
    need(fa[0]);
    sfor<0, 4>([&](auto I) { piece(IC<2>{}, I); });
    sfor<0, 14>([&](auto S) {
      constexpr int s = S;
      SBAR();
      STAMP2(4, s);
      if constexpr (s < 13) wait_vm<4>();
      else { bar(); *(LAS f32x2*)(lds + LDS_EXM + w * 512 + l * 8) = f32x2{-INFINITY, -INFINITY}; }
      sfor<0, 20>([&](auto I) {
        constexpr int i = I, t = i >> 2, u = i & 3;
        acc[t][u] = MFMA(fa[s & 1][u], fb[t], s == 0 ? ZERO4 : acc[t][u]);
        if constexpr (s < 13) {
          if constexpr (i < 3) rda(IC<s + 1>{}, IC<i>{});
          if constexpr (i == 4) rda(IC<s + 1>{}, IC<3>{});
          if constexpr (u == 3) rdb(IC<s + 1>{}, IC<t>{});
          if constexpr (i == 9) set_m0(ring + ((s + 1) & 1) * 4096);
          if constexpr (i == 10) need(fa[(s + 1) & 1]);
          if constexpr (i >= 10 && i < 18 && (i & 1) == 0) piece(IC<s + 3>{}, IC<(i - 10) / 2>{});
        } else if constexpr (i >= 6) qput(IC<(i - 6) / 4>{}, IC<(i - 6) % 4>{});
        SBAR();
      });
    });
    STAMP2(4, 14);
    STAMP(ST + 1);
    sfor<14, 20>([&](auto I) { qput(IC<I / 4>{}, IC<I % 4>{}); });
    bar();
  }
  STAMP(ST + 2);

  const rsrc_t rV = GQA ? mk_rsrc(p.v[P], (bk * 8 + j) * KV_HEAD + hh * 16 * 512 + cq * 128, KV_HEAD - (hh * 16 * 512 + cq * 128))
                        : mk_rsrc(p.v[P], (bk * 16 + 2 * j + hh) * KV_HEAD + cq * 128, KV_HEAD - cq * 128);
  constexpr int MT = GQA ? 2 : 1;
  constexpr int NG = GQA ? 8 : 16;
  f32x4 sc[4][10];
  float mx[5 * MT], nm[5 * MT], ls[5 * MT], ev[4];
  u32x2 pq[4][10];
  auto mop = [&](auto ID) {
    constexpr int id = ID, kq = id / 90, k = (id / 9) % 10, part = id % 9, r = GQA ? k : (k >> 1);
    if constexpr (part == 0) { ev[0] = __builtin_fmaf(sc[kq][k][0], SCL, nm[r]); ev[1] = __builtin_fmaf(sc[kq][k][1], SCL, nm[r]); }
    if constexpr (part == 1) ev[0] = __builtin_amdgcn_exp2f(ev[0]);
    if constexpr (part == 2) { ev[2] = __builtin_fmaf(sc[kq][k][2], SCL, nm[r]); ev[3] = __builtin_fmaf(sc[kq][k][3], SCL, nm[r]); }
    if constexpr (part == 3) ev[1] = __builtin_amdgcn_exp2f(ev[1]);
    if constexpr (part == 4) ev[2] = __builtin_amdgcn_exp2f(ev[2]);
    if constexpr (part == 5) { if constexpr (kq == 0 && (GQA || (k & 1) == 0)) ls[r] = ev[0] + ev[1]; else ls[r] = (ls[r] + ev[0]) + ev[1]; }
    if constexpr (part == 6) ev[3] = __builtin_amdgcn_exp2f(ev[3]);
    if constexpr (part == 7) { pq[kq][k][0] = pk2(ev[0], ev[1]); ls[r] += ev[2]; }
    if constexpr (part == 8) { pq[kq][k][1] = pk2(ev[2], ev[3]); ls[r] += ev[3]; }
    if constexpr (part == 0) asm volatile("" : "+v"(ev[0]), "+v"(ev[1]));
    if constexpr (part == 1) asm volatile("" : "+v"(ev[0]));
    if constexpr (part == 2) asm volatile("" : "+v"(ev[2]), "+v"(ev[3]));
    if constexpr (part == 3) asm volatile("" : "+v"(ev[1]));
    if constexpr (part == 4) asm volatile("" : "+v"(ev[2]));
    if constexpr (part == 5) asm volatile("" : "+v"(ls[r]));
    if constexpr (part == 6) asm volatile("" : "+v"(ev[3]));
    if constexpr (part == 7) asm volatile("" : "+v"(ls[r]), "+v"(pq[kq][k][0]));
    if constexpr (part == 8) asm volatile("" : "+v"(ls[r]), "+v"(pq[kq][k][1]));
  };
  auto pput = [&](auto KQ, auto K, unsigned pw) {
    constexpr int kq = KQ, k = K;
    if constexpr (GQA) *(LAS u32x2*)(lds + pw + (kq & 1) * 40960 + (k / 5) * 20480 + (k % 5) * 1024) = pq[kq][k];
    else if constexpr (k & 1) *(LAS u32x4*)(lds + pw + (kq & 1) * 40960 + (k / 2) * 1024) = u32x4{pq[kq][k - 1][0], pq[kq][k - 1][1], pq[kq][k][0], pq[kq][k][1]};
  };
  bool grown;
  unsigned pw;
  {
    const int l = lane_id(), n = l & 15, g = l >> 4;
    const unsigned ke = voff_k(l), vv = voff_spl(l, 512);
    const unsigned k0 = LDS_RING + w * 8192 + n * 128 + 16 * (g ^ ((n >> 1) & 7));
    const unsigned q0 = (GQA ? 0 : hh * 40960) + l * 16;
    constexpr int NSUB = 2 * MT;
    auto rdk = [&](auto G, auto I) { fa[G & 1][I] = ld128(lds + (k0 ^ ((I >> 1) * 64)) + (I & 1) * 2048 + (G & 1) * 4096); };
    auto rdq = [&](auto Z, auto T) {
      constexpr int z = Z, gi = z / NSUB, e = (z % NSUB) / MT, h = z % MT;
      fb[T] = ld128(lds + q0 + h * 40960 + ((2 * (gi & 3) + e) * 5 + T) * 1024);
    };
    auto piece = [&](auto G, auto I) {
      if constexpr (G < NG) dmap<true, I>(rK, ke ^ ((I & 1) * 64), k_src(G, I));
      else if constexpr (!GQA) dmap<true, I>(rV, vv, ((G - NG) * 32 + I * 8) * 512);
      else dmap<true, I>(rV, vv, ((2 * (G - NG) + (I >> 1)) * 32 + (I & 1) * 8) * 512);
    };
    auto qmax = [&](auto KQ, auto K) {
      constexpr int kq = KQ, k = K, r = GQA ? k : (k >> 1);
      if constexpr ((kq & 1) == 0 && (GQA || (k & 1) == 0)) mx[r] = max3(max3(sc[kq][k][0], sc[kq][k][1], sc[kq][k][2]), sc[kq][k][3], sc[kq][k][3]);
      else mx[r] = max3(max3(mx[r], sc[kq][k][0], sc[kq][k][1]), sc[kq][k][2], sc[kq][k][3]);
      asm volatile("" : "+v"(mx[r]));
    };
    auto xmax = [&](auto HALF, auto C) {
      const int l2 = lane_id(), g2 = l2 >> 4;
      LAS float* em = (LAS float*)(lds + LDS_EXM + HALF * 1536 + ((GQA ? 0 : hh) * 16 + (l2 & 15)) * 48);
      if constexpr (C == 0) __builtin_amdgcn_ds_fmaxf(em + g2, red4<OpMax>(mx[0], mx[1], mx[2], mx[3]), 0, 0, false);
      if constexpr (C == 1 && GQA) __builtin_amdgcn_ds_fmaxf(em + (g2 & 1) * 192 + 4, red2<OpMax>(mx[4], mx[5 * MT - 1]), 0, 0, false);
      if constexpr (C == 1 && !GQA) __builtin_amdgcn_ds_fmaxf(em + 4, red2<OpMax>(mx[4], mx[4]), 0, 0, false);
      if constexpr (C == 2 && GQA) __builtin_amdgcn_ds_fmaxf(em + 192 + g2, red4<OpMax>(mx[5], mx[6], mx[7], mx[8]), 0, 0, false);
    };
    auto rmax = [&](auto HALF) {
      LAS float* em = (LAS float*)(lds + LDS_EXM + HALF * 1536 + ((GQA ? 0 : hh) * 16 + (lane_id() & 15)) * 48);
      sfor<0, MT>([&](auto H) {
        const f32x4 v = *(LAS const f32x4*)(em + H * 192);
        sfor<0, 4>([&](auto T) { mx[H * 5 + T] = v[T + 0]; });
        mx[H * 5 + 4] = em[H * 192 + 4];
      });
    };
    wait_vm<4>();
    sfor<0, 4>([&](auto I) { rdk(IC<0>{}, I); });
    sfor<0, 5>([&](auto T) { rdq(IC<0>{}, T); });
    set_m0(ring);
    need(fa[0]);
    sfor<0, 4>([&](auto I) { piece(IC<2>{}, I); });
    sfor<0, 32>([&](auto Z) {
      constexpr int z = Z, gi = z / NSUB, sub = z % NSUB, e = sub / MT, h = sub % MT;
      SBAR();
      STAMP2(2, z);
      if constexpr (z == 19) { bar(); rmax(IC<0>{}); }
      if constexpr (sub == 0 && gi + 1 < NG) wait_vm<4>();
      sfor<0, 10>([&](auto I) {
        constexpr int i = I, t = i >> 1, u = i & 1;
        constexpr int kq = GQA ? 2 * (gi >> 2) + u : (gi >> 2), k = GQA ? 5 * h + t : i;
        sc[kq][k] = MFMA(fa[gi & 1][2 * e + u], fb[t], ((gi & 3) == 0 && e == 0) ? ZERO4 : sc[kq][k]);
        if constexpr (u == 1 && z + 1 < 32) rdq(IC<z + 1>{}, IC<t>{});
        if constexpr (gi + 1 < NG) {
          if constexpr (sub == 0 && u == 0 && i < 8) rdk(IC<gi + 1>{}, IC<t>{});
          if constexpr (sub == 1 && i == 0) set_m0(ring + ((gi + 1) & 1) * 4096);
          if constexpr (sub == 1 && i == 2) need(fa[(gi + 1) & 1]);
          if constexpr (sub == 1 && i >= 2 && u == 0) piece(IC<gi + 3>{}, IC<(i - 2) / 2>{});
        }
        if constexpr (!LAST && z == 4 && i == 9)
          warm<(WARMNT & 2) != 0>(lds0, mk_rsrc(p.wo[P], 0, W_BYTES), 512 * j * 896 + (b >> 1) * 28672 + ((w & 3) * 64 + lane_id()) * 128);
        if constexpr (z == 16 || z == 17) qmax(IC<z - 16>{}, I);
        if constexpr (z == 18 && (i == 1 || i == 4 || i == 7)) xmax(IC<0>{}, IC<i / 3>{});
        if constexpr (z == 19 && i >= 5) sfor<0, MT>([&](auto H) { nm[H * 5 + i - 5] = mul1(mx[H * 5 + i - 5], -SCL); });
        if constexpr (z >= 20) { constexpr int x = (z - 20) * 10 + i; sfor<(3 * x + 1) / 2, (3 * x + 4) / 2>(mop); }
        if constexpr (!GQA && z == 26) qmax(IC<2>{}, I);
        SBAR();
      });
    });
    STAMP2(2, 32);
    STAMP(ST + 3);
    sfor<4 - MT, 4>([&](auto KQ) { sfor<0, 10>([&](auto K) { qmax(KQ, K); }); });
    sfor<0, 3>([&](auto C) { xmax(IC<1>{}, C); });
    pw = (GQA ? (w >> 1) * 5120 + (w & 1) * 8 : hh * 20480 + cq * 5120) + lane_id() * 16;
    bar();
    rmax(IC<1>{});
    sfor<0, 2>([&](auto KQ) { sfor<0, 10>([&](auto K) { pput(KQ, K, pw); }); });
    bool any = false;
    sfor<0, 5 * MT>([&](auto R) { any |= __builtin_fmaf(mx[R], SCL, nm[R]) > GROW; });
    grown = __builtin_amdgcn_ballot_w64(any) != 0;
    if (grown) {
      sfor<0, 5 * MT>([&](auto R) {
        const float d = __builtin_fmaf(mx[R], SCL, nm[R]);
        if (d > GROW) { ls[R] *= __builtin_amdgcn_exp2f(-d); nm[R] = -SCL * mx[R]; }
      });
    }
  }
  STAMP(ST + 4);

  const rsrc_t rO = mk_rsrc(p.wo[P], 512 * j * 896 + (w < 7 ? w : 0) * 128, w < 7 ? W_BYTES - (512 * j * 896 + w * 128) : 0u);
  {
    const int l = lane_id(), g = l >> 4, q = (l >> 2) & 3;
    const unsigned vv = voff_spl(l, 512);
    const unsigned a0 = LDS_RING + (GQA ? cq : w) * 8192 + (4 * g + q) * 128 + (((q >> 1) | ((g & 1) << 1)) * 32) + 8 * (l & 3);
    const unsigned p0 = hh * 20480 + l * 16;
    auto rda = [&](auto S, auto U) {
      lds_t a = lds + (a0 ^ (U * 32)) + (GQA ? (S & 3) * 2048 : (S & 1) * 4096);
      fa[S & 1][U] = ldtr(a, a + (GQA ? 32768 : 2048));
    };
    auto rdb = [&](auto S, auto T) { fb[T] = ld128(lds + p0 + ((S >> 2) & 1) * 40960 + ((S & 3) * 5 + T) * 1024); };
    unsigned ov = 0, ow = 0;
    auto piece = [&](auto G, auto I) {
      if constexpr (G < 16) dmap<true, I>(rV, vv, (G * 32 + I * 8) * 512);
      else dmap<false, I>(rO, ov, ((G - 16) * (GQA ? 16 : 32) + I * 8) * 896);
    };
    float rl[5];
    f32x4 lv[2];
    auto oput = [&](auto T, auto H) {
      const f32x4 u = acc[T][2 * H], v = acc[T][2 * H + 1];
      const float r = rl[T];
      *(LAS u32x4*)(lds + ow + H * 5120 + T * 1024) = u32x4{pk2(mul1(u[0], r), mul1(u[1], r)), pk2(mul1(u[2], r), mul1(u[3], r)),
                                                            pk2(mul1(v[0], r), mul1(v[1], r)), pk2(mul1(v[2], r), mul1(v[3], r))};
    };
    wait_vm_bar<GQA ? 6 : 4>();
    STAMP(ST + 5);
    sfor<0, 4>([&](auto U) { rda(IC<0>{}, U); });
    sfor<0, 5>([&](auto T) { rdb(IC<0>{}, T); });
    if constexpr (!GQA) { set_m0(ring); need(fa[0]); sfor<0, 4>([&](auto I) { piece(IC<2>{}, I); }); }
    sfor<0, 16>([&](auto S) {
      constexpr int s = S;
      SBAR();
      STAMP2(3, s);
      if constexpr (GQA) {
        if constexpr ((s & 3) == 3) wait_vm_bar<4>();
        else { need(fa[s & 1]); wait_vm_bar_raw<4>(); }
      } else {
        if constexpr (s == 15) bar();
        else if constexpr ((s & 3) == 3) wait_vm_bar<4>();
        else wait_vm<4>();
      }
      if constexpr (s == 8) {
        if (grown) {
          LAS float* em = (LAS float*)(lds + LDS_EXM + (hh * 16 + (lane_id() & 15)) * 48);
          sfor<0, 5>([&](auto T) {
            const float d = __builtin_fmaf(em[384 + T], SCL, mul1(em[T + 0], -SCL));
            const float a = d > GROW ? __builtin_amdgcn_exp2f(-d) : 1.f;
            sfor<0, 4>([&](auto U) { acc[T][U] *= a; });
          });
        }
      }
      if constexpr (s == 11) ov = voff_spl(lane_id(), 896);
      if constexpr (s == 15) ow = hh * 40960 + cq * 10240 + lane_id() * 16;
      sfor<0, 20>([&](auto I) {
        constexpr int i = I, t = i >> 2, u = i & 3;
        acc[t][u] = MFMA(fa[s & 1][u], fb[t], s == 0 ? ZERO4 : acc[t][u]);
        if constexpr (GQA) {
          if constexpr (i == 0) set_m0(ring + (s & 3) * 2048);
          if constexpr (i == 1 || i == 2) piece(IC<s + 4>{}, IC<i - 1>{});
          if constexpr (s < 15 && i >= 4 && i < 7) rda(IC<s + 1>{}, IC<i - 4>{});
          if constexpr (s < 15 && i == 8) rda(IC<s + 1>{}, IC<3>{});
        } else if constexpr (s < 15) {
          if constexpr (i < 3) rda(IC<s + 1>{}, IC<i>{});
          if constexpr (i == 4) rda(IC<s + 1>{}, IC<3>{});
          if constexpr (i == 9) set_m0(ring + ((s + 1) & 1) * 4096);
          if constexpr (i == 10) need(fa[(s + 1) & 1]);
          if constexpr (i >= 10 && i < 18 && (i & 1) == 0) piece(IC<s + 3>{}, IC<(i - 10) / 2>{});
        }
        if constexpr (s < 15 && u == 3) rdb(IC<s + 1>{}, IC<t>{});
        if constexpr (s < 10 && i >= 1 && i < 19) mop(IC<(180 + 18 * s + i - 1) % 360>{});
        if constexpr ((s == 5 || s == 10) && (i & 1) == 0) pput(IC<s == 5 ? 2 : 3>{}, IC<i / 2>{}, pw);
        if constexpr (s == 10 && (i == 3 || i == 9 || i == 15)) {
          const int l2 = lane_id();
          LAS float* el = (LAS float*)(lds + LDS_EXL + ((GQA ? 0 : hh) * 80 + (l2 & 15)) * 48 + (GQA ? w : cq) * 4);
          if constexpr (i == 3) el[(l2 >> 4) * 192] = red4<OpAdd>(ls[0], ls[1], ls[2], ls[3]);
          if constexpr (i == 9 && GQA) el[768 + ((l2 >> 4) & 1) * 960] = red2<OpAdd>(ls[4], ls[5 * MT - 1]);
          if constexpr (i == 9 && !GQA) el[768] = red2<OpAdd>(ls[4], ls[4]);
          if constexpr (i == 15 && GQA) el[960 + (l2 >> 4) * 192] = red4<OpAdd>(ls[5], ls[6], ls[7], ls[8]);
        }
        if constexpr (!LAST && s == 13 && i == 5) {
          const unsigned id = (w & 3) * 64 + lane_id();
          warm(lds0, mk_rsrc(p.wq[1 - P], 0, W_BYTES), ((b >> 1) * 28 + (id >> 3)) * 8192 + 1024 * j + (id & 7) * 128);
        }
        if constexpr (!LAST && s == 12 && (i == 5 || i == 7))
          warm(lds0, mk_rsrc(p.x[1 - P], b * X_BYTES, X_BYTES), ((i == 5 ? w : 8 + w) * 64 + lane_id()) * 128);
        if constexpr ((s == 12 || s == 13) && (i % 8) == 1 && (s - 12) * 3 + i / 8 < 5) {
          lds_t e = lds + LDS_EXL + (hh * 80 + ((s - 12) * 3 + i / 8) * 16 + (lane_id() & 15)) * 48;
          lv[0] = *(LAS const f32x4*)e;
          if constexpr (GQA) lv[1] = *(LAS const f32x4*)(e + 16);
        }
        if constexpr ((s == 12 || s == 13) && i == (i / 8) * 8 + (i < 16 ? 6 : 3) && (s - 12) * 3 + i / 8 < 5) {
          float sum = ((lv[0][0] + lv[0][1]) + lv[0][2]) + lv[0][3];
          if constexpr (GQA) sum = (((sum + lv[1][0]) + lv[1][1]) + lv[1][2]) + lv[1][3];
          rl[((s - 12) * 3 + i / 8) % 5] = __builtin_amdgcn_rcpf(sum);
        }
        if constexpr (s == 15 && i >= 6 && (i & 1) == 0) oput(IC<(i - 6) / 4>{}, IC<((i - 6) / 2) % 2>{});
        SBAR();
      });
    });
    STAMP2(3, 16);
    STAMP(ST + 6);
    sfor<7, 10>([&](auto I) { oput(IC<I / 2>{}, IC<I % 2>{}); });
    if constexpr (LAST) wait_vm_bar<0>();
    else bar();
  }
  STAMP(ST + 7);

  {
    const rsrc_t rN = mk_rsrc(p.wq[1 - P], (512 * j + 64 * w) * 2, LAST ? 0u : W_BYTES - (512 * j + 64 * w) * 2);
    const int l = lane_id(), g = l >> 4, q = (l >> 2) & 3;
    const unsigned ov = voff_spl(l, 896), we = voff_nat(l, 8192);
    auto piece = [&](auto G, auto I) {
      if constexpr (G < 16) dmap<false, I>(rO, ov, (G * 32 + I * 8) * 896);
      else if constexpr (!LAST) dmap<false, I>(rN, we ^ ((I & 1) * 64), ((G - 16) * 32 + I * 8) * 8192);
    };
    if (w < 7) {
      const unsigned a0 = LDS_RING + w * 8192 + (4 * g + q) * 128 + (((q >> 1) | ((g & 1) << 1)) * 32) + 8 * (l & 3);
      auto rda = [&](auto S, auto U) { lds_t a = lds + (a0 ^ (U * 32)) + (S & 1) * 4096; fa[S & 1][U] = ldtr(a, a + 2048); };
      auto rdb = [&](auto S, auto T) { fb[T] = ld128(lds + l * 16 + (S * 5 + T) * 1024); };
      const rsrc_t rS = mk_rsrc(p.slab, ((P * 32 + b) * 8 + j) * SLAB + w * 10240, 10240);
      auto sput = [&](auto T, auto H) {
        const f32x4 u = acc[T][2 * H], v = acc[T][2 * H + 1];
        auto r0 = __builtin_amdgcn_permlane16_swap(pk2(u[0], u[1]), pk2(v[0], v[1]), false, false);
        auto r1 = __builtin_amdgcn_permlane16_swap(pk2(u[2], u[3]), pk2(v[2], v[3]), false, false);
        __builtin_amdgcn_raw_buffer_store_b128(u32x4{r0[0], r1[0], r0[1], r1[1]}, rS, l * 16 + (T * 2 + H) * 1024, 0, 16  );
      };
      wait_vm<4>();
      sfor<0, 4>([&](auto U) { rda(IC<0>{}, U); });
      sfor<0, 5>([&](auto T) { rdb(IC<0>{}, T); });
      set_m0(ring);
      need(fa[0]);
      sfor<0, 4>([&](auto I) { piece(IC<2>{}, I); });
      sfor<0, 16>([&](auto S) {
        constexpr int s = S;
        SBAR();
        if constexpr (s < 15) { if constexpr (!(LAST && (ABL & 1))) wait_vm<(LAST && s == 14) ? 0 : 4>(); }
        else if constexpr (!LAST) { bar(); issue_x(p, 1 - P, b, w, lds0); }
        sfor<0, 20>([&](auto I) {
          constexpr int i = I, t = i >> 2, u = i & 3;
          acc[t][u] = MFMA(fa[s & 1][u], fb[t], s == 0 ? ZERO4 : acc[t][u]);
          if constexpr (s < 15) {
            if constexpr (!(LAST && (ABL & 4) && s > 0)) {
            if constexpr (i < 3) rda(IC<s + 1>{}, IC<i>{});
            if constexpr (i == 4) rda(IC<s + 1>{}, IC<3>{}); }
            if constexpr (!(LAST && (ABL & 8)))
            if constexpr (u == 3) rdb(IC<s + 1>{}, IC<t>{});
            if constexpr (!(LAST && (s >= 13 || (ABL & 2)))) {
              if constexpr (i == 9) set_m0(ring + ((s + 1) & 1) * 4096);
              if constexpr (i == 10) need(fa[(s + 1) & 1]);
              if constexpr (i >= 10 && i < 18 && (i & 1) == 0) piece(IC<s + 3>{}, IC<(i - 10) / 2>{});
            }
          } else if constexpr (i >= 6 && (i & 1) == 0) sput(IC<(i - 6) / 4>{}, IC<((i - 6) / 2) % 2>{});
          SBAR();
        });
      });
      STAMP(ST + 8);
      sfor<7, 10>([&](auto I) { sput(IC<I / 2>{}, IC<I % 2>{}); });
      if constexpr (!LAST) wait_vm_bar<10>();
    } else if constexpr (!LAST) {
      wait_vm<0>();
      set_m0(ring);
      sfor<0, 4>([&](auto I) { piece(IC<16>{}, I); });
      set_m0(ring + 4096);
      sfor<0, 4>([&](auto I) { piece(IC<17>{}, I); });
      bar();
      issue_x(p, 1 - P, b, w, lds0);
      wait_vm_bar<0>();
    } else {
      arrive(p, b, 0);
      wait_all(p, b, 0);
    }
  }
  STAMP(ST + 9);
}

__global__ __launch_bounds__(512, 2) void fused_attn_kernel(Params p) {
  extern __shared__ __attribute__((aligned(16))) char smem[];
  lds_t lds = (lds_t)smem;
  const unsigned lds0 = (unsigned)(uintptr_t)lds;
  const int w = __builtin_amdgcn_readfirstlane(threadIdx.x >> 6);
  const int j = blockIdx.x & 7, b = blockIdx.x >> 3;
  const int first = b & 1;
  STAMP(0);
  {
    issue_x(p, first, b, w, lds0);
    const int l = lane_id();
    const rsrc_t rW = mk_rsrc(p.wq[first], (512 * j + 64 * w) * 2, W_BYTES - (512 * j + 64 * w) * 2);
    const unsigned we = voff_nat(l, 8192), ring = lds0 + LDS_RING + w * 8192;
    sfor<0, 2>([&](auto G) {
      set_m0(ring + G * 4096);
      sfor<0, 4>([&](auto I) { dmap<false, I>(rW, we ^ ((I & 1) * 64), (G * 32 + I * 8) * 8192); });
    });
    const unsigned id = (w & 3) * 64 + l;
    warm<(WARMNT & 1) != 0>(lds0, mk_rsrc(p.wq[first], 0, W_BYTES), ((b >> 1) * 28 + (id >> 3)) * 8192 + 1024 * j + (id & 7) * 128);
    wait_vm_bar<9>();
  }
  STAMP(10);
  if (first == 0) {
    run_problem<0, false, 0>(p, lds, lds0, b, j, w);
    run_problem<1, true, 10>(p, lds, lds0, b, j, w);
  } else {
    run_problem<1, false, 0>(p, lds, lds0, b, j, w);
    run_problem<0, true, 10>(p, lds, lds0, b, j, w);
  }
  wait_vm<0>();
  STAMP(21);
  bar();
  STAMP(22);
  if (w == 7) arrive(p, b, 1);
  reduce_rows<2>(p, first, b, j, w, 8);
  if (w == 7) wait_all(p, b, 1);
  bar();
  STAMP(23);
  reduce_rows<2>(p, 1 - first, b, j, w, 8);
  STAMP(20);
}

extern "C" void kernel_launch(void* const* d_in, const int* in_sizes, int n_in, void* d_out, int out_size, void* d_ws,
                              size_t ws_size, hipStream_t stream) {
  Params p;
  for (int i = 0; i < 2; ++i) {
    p.x[i] = d_in[5 * i + 0]; p.k[i] = d_in[5 * i + 1]; p.v[i] = d_in[5 * i + 2]; p.wq[i] = d_in[5 * i + 3]; p.wo[i] = d_in[5 * i + 4];
  }
  p.cnt = (unsigned*)d_ws;
  p.slab = (char*)d_ws + 65536;
  p.out = d_out;
  p.diag = (unsigned long long*)((char*)d_ws + (64u << 20));
  (void)hipFuncSetAttribute((const void*)fused_attn_kernel, hipFuncAttributeMaxDynamicSharedMemorySize, LDS_TOTAL);
  (void)hipMemsetAsync(d_ws, 0, 8192, stream);
  fused_attn_kernel<<<256, 512, LDS_TOTAL, stream>>>(p);
}
```

```cpp
#include <hip/hip_runtime.h>
#include <hip/hip_bf16.h>
#include <stdint.h>
#include <type_traits>

typedef short bf16x8 __attribute__((ext_vector_type(8)));
typedef short s16x4 __attribute__((ext_vector_type(4)));
typedef float f32x4 __attribute__((ext_vector_type(4)));
typedef float f32x2 __attribute__((ext_vector_type(2)));
typedef __bf16 bf16x2 __attribute__((ext_vector_type(2)));
typedef unsigned u32x4 __attribute__((ext_vector_type(4)));
typedef unsigned u32x2 __attribute__((ext_vector_type(2)));
#define LAS __attribute__((address_space(3)))
typedef __amdgpu_buffer_rsrc_t rsrc_t;
typedef LAS char* lds_t;

#define DIAG 0
#define DLAST 1
#define ABL 0
#define WARMNT 0
#define XW 0
#define WOL 0
#define XNT 0
#define FAKEKV 0

constexpr int LDS_RING = 81920;
constexpr int LDS_EXM = 147456;
constexpr int LDS_EXL = 151552;
constexpr int LDS_DUMMY = 159232;
constexpr int LDS_TOTAL = DIAG ? 163840 : 159488;
constexpr unsigned X_BYTES = 80 * 448 * 2;
constexpr unsigned W_BYTES = 448 * 4096 * 2;
constexpr unsigned KV_HEAD = 512 * 256 * 2;
constexpr unsigned SLAB = 80 * 448 * 2;
constexpr float SCL = 0.0625f * 1.4426950408889634f;
constexpr float GROW = 20.f;

template <int I, int N, class F>
__device__ __forceinline__ void sfor(F&& f) {
  if constexpr (I < N) { f(std::integral_constant<int, I>{}); sfor<I + 1, N>(f); }
}
#define SBAR() __builtin_amdgcn_sched_barrier(0)
#define MFMA(a, b, c) __builtin_amdgcn_mfma_f32_16x16x32_bf16(a, b, c, 0, 0, 0)
template <int N> __device__ __forceinline__ void wait_vm() { asm volatile("s_waitcnt vmcnt(%0)" ::"i"(N) : "memory"); }
__device__ __forceinline__ void bar() { asm volatile("s_waitcnt lgkmcnt(0)\n\ts_barrier" ::: "memory"); }
template <int N> __device__ __forceinline__ void wait_vm_bar_raw() { asm volatile("s_waitcnt vmcnt(%0)\n\ts_barrier" ::"i"(N) : "memory"); }
template <int N> __device__ __forceinline__ void wait_vm_bar() { asm volatile("s_waitcnt vmcnt(%0) lgkmcnt(0)\n\ts_barrier" ::"i"(N) : "memory"); }
__device__ __forceinline__ int lane_id() {
  int l; asm volatile("v_mbcnt_lo_u32_b32 %0, -1, 0\n\tv_mbcnt_hi_u32_b32 %0, -1, %0" : "=v"(l)); return l;
}
__device__ __forceinline__ unsigned pk2(float a, float b) {
  return __builtin_bit_cast(unsigned, __builtin_convertvector(f32x2{a, b}, bf16x2));
}
__device__ __forceinline__ rsrc_t mk_rsrc(const void* p, unsigned off, unsigned bytes) {
  return __builtin_amdgcn_make_buffer_rsrc((void*)((const char*)p + off), (short)0, (int)bytes, 0x00020000);
}
__device__ __forceinline__ void need(const bf16x8 (&f)[4]) { asm volatile("" ::"v"(f[0]), "v"(f[1]), "v"(f[2]), "v"(f[3]) : "memory"); }
__device__ __forceinline__ void set_m0(unsigned m0v) { asm volatile("s_mov_b32 m0, %0\n\ts_nop 0" ::"s"(m0v) : "memory"); }
template <bool NT, int I>
__device__ __forceinline__ void dmap(rsrc_t r, unsigned v, unsigned src) {
  if constexpr (NT && !FAKEKV) asm volatile("buffer_load_dwordx4 %0, %1, %2 offen offset:%3 nt lds" ::"v"(v), "s"(r), "s"(src - 1024u * I), "i"(1024 * I) : "memory");
  else asm volatile("buffer_load_dwordx4 %0, %1, %2 offen offset:%3 lds" ::"v"(v), "s"(r), "s"(src - 1024u * I), "i"(1024 * I) : "memory");
}
template <bool NT = false>
__device__ __forceinline__ void warm(unsigned lds0, rsrc_t r, unsigned v) {
  if constexpr (NT) asm volatile("s_mov_b32 m0, %0\n\ts_nop 0\n\tbuffer_load_dword %1, %2, 0 offen nt lds" ::"s"(lds0 + LDS_DUMMY), "v"(v), "s"(r) : "memory");
  else asm volatile("s_mov_b32 m0, %0\n\ts_nop 0\n\tbuffer_load_dword %1, %2, 0 offen lds" ::"s"(lds0 + LDS_DUMMY), "v"(v), "s"(r) : "memory");
}
extern "C" __device__ float llvm_fmul_legacy(float, float) __asm("llvm.amdgcn.fmul.legacy");
__device__ __forceinline__ float mul1(float a, float b) { return llvm_fmul_legacy(a, b); }
__device__ __forceinline__ bf16x8 ld128(lds_t p) { return *(LAS const bf16x8*)p; }
#define RDB(dst, expr) do { if constexpr (ABL & 256) asm volatile("" : "+v"(dst)); else dst = (expr); } while (0)
#define EXP2(x) ((ABL & 512) ? (x) : __builtin_amdgcn_exp2f(x))
__device__ __forceinline__ bf16x8 ldtr(lds_t p0, lds_t p1) {
  s16x4 lo = __builtin_amdgcn_ds_read_tr16_b64_v4i16((LAS s16x4*)p0);
  s16x4 hi = __builtin_amdgcn_ds_read_tr16_b64_v4i16((LAS s16x4*)p1);
  return __builtin_shufflevector(lo, hi, 0, 1, 2, 3, 4, 5, 6, 7);
}
__device__ __forceinline__ float max3(float a, float b, float c) { return __builtin_fmaxf(__builtin_fmaxf(a, b), c); }
struct OpMax { static __device__ __forceinline__ float f(float a, float b) { return max3(a, b, b); } };
struct OpAdd { static __device__ __forceinline__ float f(float a, float b) { return a + b; } };
template <class Op> __device__ __forceinline__ float sw16(float a, float b) {
  auto r = __builtin_amdgcn_permlane16_swap(__float_as_uint(a), __float_as_uint(b), false, false);
  return Op::f(__uint_as_float(r[0]), __uint_as_float(r[1]));
}
template <class Op> __device__ __forceinline__ float sw32(float a, float b) {
  auto r = __builtin_amdgcn_permlane32_swap(__float_as_uint(a), __float_as_uint(b), false, false);
  return Op::f(__uint_as_float(r[0]), __uint_as_float(r[1]));
}
template <class Op> __device__ __forceinline__ float red4(float a, float b, float c, float d) { return sw32<Op>(sw16<Op>(a, b), sw16<Op>(c, d)); }
template <class Op> __device__ __forceinline__ float red2(float a, float b) { const float x = sw16<Op>(a, b); return sw32<Op>(x, x); }

__device__ __forceinline__ unsigned voff_nat(int l, unsigned pitch) { return (l >> 3) * pitch + 16 * ((l & 7) ^ (((l >> 4) & 1) << 1)); }
__device__ __forceinline__ unsigned voff_spl(int l, unsigned pitch) { return (l >> 3) * pitch + 16 * ((l & 7) ^ (((l >> 4) & 3) << 1)); }
__device__ __forceinline__ unsigned voff_k(int l) { return (l >> 3) * 512 + 16 * ((l & 7) ^ ((l >> 4) & 3)); }

struct Params {
  const void *x[2], *k[2], *v[2], *wq[2], *wo[2];
  void* slab;
  unsigned* cnt;
  void* out;
  unsigned long long* diag;
};

#define STAMP(i)
#define STAMP2(ph, i)

#define DM0 7
template <int V> using IC = std::integral_constant<int, V>;
constexpr f32x4 ZERO4 = {0.f, 0.f, 0.f, 0.f};

__device__ __forceinline__ void issue_x(const Params& p, int P, int b, int w, unsigned lds0) {
  const int l = lane_id();
  const unsigned v = (l >> 3) * 896 + 16 * ((l & 7) ^ ((l >> 4) & 3));
  const rsrc_t rx = mk_rsrc(p.x[P], b * X_BYTES, X_BYTES);
  sfor<0, 9>([&](auto I) {
    const unsigned pc = w * 9 + I, blk = (pc * 205) >> 11, rg = pc - blk * 10;
    set_m0(lds0 + pc * 1024);
    dmap<XNT != 0, 0>(rx, v ^ ((rg & 1) * 64), pc < 70 ? rg * 7168 + blk * 128 : X_BYTES);
  });
}

typedef __attribute__((address_space(1))) unsigned gu32;
__device__ __forceinline__ void arrive(const Params& p, int b, int which) {
  if (lane_id() == 0) __hip_atomic_fetch_add((gu32*)p.cnt + (b * 2 + which) * 32, 1u, __ATOMIC_RELAXED, __HIP_MEMORY_SCOPE_AGENT);
}
__device__ __forceinline__ void wait_all(const Params& p, int b, int which) {
  gu32* cnt = (gu32*)p.cnt + (b * 2 + which) * 32;
  for (unsigned spins = 0; spins < (1u << 22); ++spins) {
    if ((unsigned)__builtin_amdgcn_readfirstlane(__hip_atomic_load(cnt, __ATOMIC_RELAXED, __HIP_MEMORY_SCOPE_AGENT)) >= 8u) break;
    __builtin_amdgcn_s_sleep(4);
  }
  asm volatile("" ::: "memory");
}
template <int N>
__device__ __forceinline__ void reduce_rows(const Params& p, int P, int b, int j, int first, int step) {
  const int l = lane_id(), n = l & 15, g = l >> 4;
  const int r0 = (70 * j) >> 3, nr = ((70 * (j + 1)) >> 3) - r0;
  const rsrc_t rS = mk_rsrc(p.slab, (P * 32 + b) * 8 * SLAB, 8 * SLAB);
  u32x4 v[N][8];
  sfor<0, N>([&](auto K) {
    const int it = first + step * K;
    const unsigned off = it < nr ? (r0 + it) * 1024 + l * 16 : 0x80000000u;
    sfor<0, 8>([&](auto J) { v[K][J] = __builtin_amdgcn_raw_buffer_load_b128(rS, off, J * SLAB, 16  ); });
  });
  sfor<0, N>([&](auto K) {
    const int it = first + step * K, f = r0 + it;
    if (it < nr) {
      float sm[8];
      sfor<0, 4>([&](auto I) { sm[2 * I] = __uint_as_float(v[K][0][I + 0] << 16); sm[2 * I + 1] = __uint_as_float(v[K][0][I + 0] & 0xffff0000u); });
      sfor<1, 8>([&](auto J) {
        sfor<0, 4>([&](auto I) { sm[2 * I] += __uint_as_float(v[K][J][I + 0] << 16); sm[2 * I + 1] += __uint_as_float(v[K][J][I + 0] & 0xffff0000u); });
      });
      const int ww = f / 10, t = (f - ww * 10) >> 1, h = f & 1;
      const int col = P * 448 + 64 * ww + 16 * (2 * h + (g & 1)) + 8 * (g >> 1);
      *(u32x4*)((char*)p.out + ((size_t)(b * 80 + t * 16 + n) * 896 + col) * 2) = u32x4{pk2(sm[0], sm[1]), pk2(sm[2], sm[3]), pk2(sm[4], sm[5]), pk2(sm[6], sm[7])};
    }
  });
}

template <int P  , bool LAST, int ST>
__device__ __forceinline__ void run_problem(const Params& p, lds_t lds, unsigned lds0, int b, int j, int w) {
  constexpr bool GQA = (P == 0);
  const int bk = FAKEKV ? 0 : b;
  const int hh = w >> 2, cq = w & 3;
  const unsigned ring = lds0 + LDS_RING + w * 8192;
  f32x4 acc[5][4];
  bf16x8 fa[2][4], fb[5];

  const unsigned koff = GQA ? (bk * 8 + j) * KV_HEAD + w * 16 * 512 : (bk * 16 + 2 * j + hh) * KV_HEAD + cq * 32 * 512;
  const rsrc_t rK = mk_rsrc(p.k[P], koff, KV_HEAD - (GQA ? w * 16 * 512 : cq * 32 * 512));
  auto k_src = [](int g, int i) -> unsigned {
    return GQA ? ((g >> 2) * 256 + (i >> 1) * 128 + (i & 1) * 8) * 512 + (g & 3) * 128 : ((g >> 2) * 128 + i * 8) * 512 + (g & 3) * 128;
  };

  {
    const rsrc_t rW = mk_rsrc(p.wq[P], (512 * j + 64 * w) * 2, (ABL & 1024) ? 65536u : W_BYTES - (512 * j + 64 * w) * 2);
    const int l = lane_id(), n = l & 15, g = l >> 4, q = (l >> 2) & 3;
    const unsigned we = voff_nat(l, 8192), ke = voff_k(l);
    const unsigned a0 = LDS_RING + w * 8192 + (8 * g + q) * 128 + (((q >> 1) | ((g & 1) << 1)) * 32) + 8 * (l & 3);
    unsigned x0[2][2];
    x0[0][0] = n * 128 + 16 * (g ^ ((n >> 1) & 7)); x0[1][0] = x0[0][0] ^ 64; x0[0][1] = x0[0][0] + 40960; x0[1][1] = x0[1][0] + 40960;
    asm volatile("" : "+v"(x0[0][1]), "+v"(x0[1][1]));
    const unsigned qw = hh * 40960 + cq * 10240 + (g >> 1) * 256 + n * 16 + (g & 1) * 8;
    auto rda = [&](auto S, auto U) { lds_t a = lds + (a0 ^ (U * 32)) + (S & 1) * 4096; fa[S & 1][U] = ldtr(a, a + 512); };
    auto rdb = [&](auto S, auto T) { fb[T] = ld128(lds + x0[S & 1][S >= 8] + ((S >> 1) & 3) * 10240 + T * 2048); };
    auto piece = [&](auto G, auto I) {
      if constexpr (G < 14) dmap<false, I>(rW, we ^ ((I & 1) * 64), (G * 32 + I * 8) * 8192);
      else dmap<true, I>(rK, ke ^ ((I & 1) * 64), k_src(G - 14, I));
    };
    auto qput = [&](auto T, auto U) {
      *(LAS u32x2*)(lds + qw + (U >> 1) * 5120 + T * 1024 + (U & 1) * 512) = u32x2{pk2(acc[T][U][0], acc[T][U][1]), pk2(acc[T][U][2], acc[T][U][3])};
    };
    if constexpr (!LAST) wait_vm<6>();
    sfor<0, 4>([&](auto U) { rda(IC<0>{}, U); });
    sfor<0, 5>([&](auto T) { rdb(IC<0>{}, T); });
    set_m0(ring);
    need(fa[0]);
    sfor<0, 4>([&](auto I) { piece(IC<2>{}, I); });
    sfor<0, 14>([&](auto S) {
      constexpr int s = S;
      SBAR();
      STAMP2(4, s);
      if constexpr (s == 0) { if constexpr (!LAST) wait_vm<6>(); }
      else if constexpr (s < 13) wait_vm<4>();
      else {
        bar();
        *(LAS f32x2*)(lds + LDS_EXM + w * 512 + l * 8) = f32x2{-INFINITY, -INFINITY};
        if constexpr (!LAST && WOL) warm(lds0, mk_rsrc(p.wo[P], 0, W_BYTES), l < 28 ? 512 * j * 896 + (b >> 1) * 28672 + (w * 28 + l) * 128 : W_BYTES);
      }
      sfor<0, 20>([&](auto I) {
        constexpr int i = I, t = i >> 2, u = i & 3;
        acc[t][u] = MFMA(fa[s & 1][u], fb[t], s == 0 ? ZERO4 : acc[t][u]);
        if constexpr (s < 13) {
          if constexpr (i < 4) rda(IC<s + 1>{}, IC<i & 3>{});
          if constexpr (u == 3) rdb(IC<s + 1>{}, IC<t>{});
          if constexpr (i == 5) set_m0(ring + ((s + 1) & 1) * 4096);
          if constexpr (i == DM0) need(fa[(s + 1) & 1]);
          if constexpr (i >= DM0 && i < DM0 + 4) piece(IC<s + 3>{}, IC<(i - DM0) & 3>{});
        } else if constexpr (i >= 6) qput(IC<(i - 6) / 4>{}, IC<(i - 6) % 4>{});
        SBAR();
      });
    });
    STAMP2(4, 14);
    STAMP(ST + 1);
    sfor<14, 20>([&](auto I) { qput(IC<I / 4>{}, IC<I % 4>{}); });
    bar();
  }
  STAMP(ST + 2);

  const rsrc_t rV = GQA ? mk_rsrc(p.v[P], (bk * 8 + j) * KV_HEAD + hh * 16 * 512 + cq * 128, KV_HEAD - (hh * 16 * 512 + cq * 128))
                        : mk_rsrc(p.v[P], (bk * 16 + 2 * j + hh) * KV_HEAD + cq * 128, KV_HEAD - cq * 128);
  constexpr int MT = GQA ? 2 : 1;
  constexpr int NG = GQA ? 8 : 16;
  f32x4 sc[4][10];
  float mx[5 * MT], nm[5 * MT], ls[5 * MT], ev[4];
  u32x2 pq[4][10];
  auto mop = [&](auto ID) {
    constexpr int id = ID, kq = id / 90, k = (id / 9) % 10, part = id % 9, r = GQA ? k : (k >> 1);
    if constexpr (part == 0) { ev[0] = __builtin_fmaf(sc[kq][k][0], SCL, nm[r]); ev[1] = __builtin_fmaf(sc[kq][k][1], SCL, nm[r]); }
    if constexpr (part == 1) ev[0] = EXP2(ev[0]);
    if constexpr (part == 2) { ev[2] = __builtin_fmaf(sc[kq][k][2], SCL, nm[r]); ev[3] = __builtin_fmaf(sc[kq][k][3], SCL, nm[r]); }
    if constexpr (part == 3) ev[1] = EXP2(ev[1]);
    if constexpr (part == 4) ev[2] = EXP2(ev[2]);
    if constexpr (part == 5) { if constexpr (kq == 0 && (GQA || (k & 1) == 0)) ls[r] = ev[0] + ev[1]; else ls[r] = (ls[r] + ev[0]) + ev[1]; }
    if constexpr (part == 6) ev[3] = EXP2(ev[3]);
    if constexpr (part == 7) { pq[kq][k][0] = pk2(ev[0], ev[1]); ls[r] += ev[2]; }
    if constexpr (part == 8) { pq[kq][k][1] = pk2(ev[2], ev[3]); ls[r] += ev[3]; }
    if constexpr (part == 0) asm volatile("" : "+v"(ev[0]), "+v"(ev[1]));
    if constexpr (part == 1) asm volatile("" : "+v"(ev[0]));
    if constexpr (part == 2) asm volatile("" : "+v"(ev[2]), "+v"(ev[3]));
    if constexpr (part == 3) asm volatile("" : "+v"(ev[1]));
    if constexpr (part == 4) asm volatile("" : "+v"(ev[2]));
    if constexpr (part == 5) asm volatile("" : "+v"(ls[r]));
    if constexpr (part == 6) asm volatile("" : "+v"(ev[3]));
    if constexpr (part == 7) asm volatile("" : "+v"(ls[r]), "+v"(pq[kq][k][0]));
    if constexpr (part == 8) asm volatile("" : "+v"(ls[r]), "+v"(pq[kq][k][1]));
  };
  auto pput = [&](auto KQ, auto K, unsigned pw) {
    constexpr int kq = KQ, k = K;
    if constexpr (GQA) *(LAS u32x2*)(lds + pw + (kq & 1) * 40960 + (k / 5) * 20480 + (k % 5) * 1024) = pq[kq][k];
    else if constexpr (k & 1) *(LAS u32x4*)(lds + pw + (kq & 1) * 40960 + (k / 2) * 1024) = u32x4{pq[kq][k - 1][0], pq[kq][k - 1][1], pq[kq][k][0], pq[kq][k][1]};
  };
  bool grown;
  unsigned pw;
  {
    const int l = lane_id(), n = l & 15, g = l >> 4;
    const unsigned ke = voff_k(l), vv = voff_spl(l, 512);
    const unsigned k0 = LDS_RING + w * 8192 + n * 128 + 16 * (g ^ ((n >> 1) & 7));
    const unsigned q0 = (GQA ? 0 : hh * 40960) + l * 16;
    unsigned q1 = q0 + 40960;
    asm volatile("" : "+v"(q1));
    constexpr int NSUB = 2 * MT;
    auto rdk = [&](auto G, auto I) { fa[G & 1][I] = ld128(lds + (k0 ^ ((I >> 1) * 64)) + (I & 1) * 2048 + (G & 1) * 4096); };
    auto rdq = [&](auto Z, auto T) {
      constexpr int z = Z, gi = z / NSUB, e = (z % NSUB) / MT, h = z % MT;
      RDB(fb[T], ld128(lds + (h ? q1 : q0) + ((2 * (gi & 3) + e) * 5 + T) * 1024));
    };
    auto piece = [&](auto G, auto I) {
      if constexpr (G < NG) dmap<true, I>(rK, ke ^ ((I & 1) * 64), k_src(G, I));
      else if constexpr (!GQA) dmap<true, I>(rV, vv, ((G - NG) * 32 + I * 8) * 512);
      else dmap<true, I>(rV, vv, ((2 * (G - NG) + (I >> 1)) * 32 + (I & 1) * 8) * 512);
    };
    auto qmax = [&](auto KQ, auto K) {
      constexpr int kq = KQ, k = K, r = GQA ? k : (k >> 1);
      if constexpr ((kq & 1) == 0 && (GQA || (k & 1) == 0)) mx[r] = max3(max3(sc[kq][k][0], sc[kq][k][1], sc[kq][k][2]), sc[kq][k][3], sc[kq][k][3]);
      else mx[r] = max3(max3(mx[r], sc[kq][k][0], sc[kq][k][1]), sc[kq][k][2], sc[kq][k][3]);
      asm volatile("" : "+v"(mx[r]));
    };
    auto xmax = [&](auto HALF, auto C) {
      const int l2 = lane_id(), g2 = l2 >> 4;
      LAS float* em = (LAS float*)(lds + LDS_EXM + HALF * 1536 + ((GQA ? 0 : hh) * 16 + (l2 & 15)) * 48);
      if constexpr (C == 0) __builtin_amdgcn_ds_fmaxf(em + g2, red4<OpMax>(mx[0], mx[1], mx[2], mx[3]), 0, 0, false);
      if constexpr (C == 1 && GQA) __builtin_amdgcn_ds_fmaxf(em + (g2 & 1) * 192 + 4, red2<OpMax>(mx[4], mx[5 * MT - 1]), 0, 0, false);
      if constexpr (C == 1 && !GQA) __builtin_amdgcn_ds_fmaxf(em + 4, red2<OpMax>(mx[4], mx[4]), 0, 0, false);
      if constexpr (C == 2 && GQA) __builtin_amdgcn_ds_fmaxf(em + 192 + g2, red4<OpMax>(mx[5], mx[6], mx[7], mx[8]), 0, 0, false);
    };
    auto rmax = [&](auto HALF) {
      LAS float* em = (LAS float*)(lds + LDS_EXM + HALF * 1536 + ((GQA ? 0 : hh) * 16 + (lane_id() & 15)) * 48);
      sfor<0, MT>([&](auto H) {
        const f32x4 v = *(LAS const f32x4*)(em + H * 192);
        sfor<0, 4>([&](auto T) { mx[H * 5 + T] = v[T + 0]; });
        mx[H * 5 + 4] = em[H * 192 + 4];
      });
    };
    wait_vm<(!LAST && WOL) ? 5 : 4>();
    sfor<0, 4>([&](auto I) { rdk(IC<0>{}, I); });
    sfor<0, 5>([&](auto T) { rdq(IC<0>{}, T); });
    set_m0(ring);
    need(fa[0]);
    sfor<0, 4>([&](auto I) { piece(IC<2>{}, I); });
    sfor<0, 32>([&](auto Z) {
      constexpr int z = Z, gi = z / NSUB, sub = z % NSUB, e = sub / MT, h = sub % MT;
      SBAR();
      STAMP2(2, z);
      if constexpr (z == 19) { bar(); rmax(IC<0>{}); }
      if constexpr (sub == 0 && gi + 1 < NG) wait_vm<(!LAST && WOL && gi == 0) ? 5 : 4>();
      sfor<0, 10>([&](auto I) {
        constexpr int i = I, t = i >> 1, u = i & 1;
        constexpr int kq = GQA ? 2 * (gi >> 2) + u : (gi >> 2), k = GQA ? 5 * h + t : i;
        sc[kq][k] = MFMA(fa[gi & 1][2 * e + u], fb[t], ((gi & 3) == 0 && e == 0) ? ZERO4 : sc[kq][k]);
        if constexpr (u == 1 && z + 1 < 32) rdq(IC<z + 1>{}, IC<t>{});
        if constexpr (gi + 1 < NG) {
          if constexpr (sub == 0 && i < 4) rdk(IC<gi + 1>{}, IC<i & 3>{});
          if constexpr (sub == 0 && i == 5) set_m0(ring + ((gi + 1) & 1) * 4096);
          if constexpr (sub == 0 && i == 6) need(fa[(gi + 1) & 1]);
          if constexpr (sub == 0 && i >= 6) piece(IC<gi + 3>{}, IC<(i - 6) & 3>{});
        }
        if constexpr (z == 16 || z == 17) qmax(IC<z - 16>{}, I);
        if constexpr (z == 18 && (i == 1 || i == 4 || i == 7)) xmax(IC<0>{}, IC<i / 3>{});
        if constexpr (z == 19 && i >= 5) sfor<0, MT>([&](auto H) { nm[H * 5 + i - 5] = mul1(mx[H * 5 + i - 5], -SCL); });
        if constexpr (z >= 20) { constexpr int x = (z - 20) * 10 + i; sfor<(3 * x + 1) / 2, (3 * x + 4) / 2>(mop); }
        if constexpr (!GQA && z == 26) qmax(IC<2>{}, I);
        SBAR();
      });
    });
    STAMP2(2, 32);
    STAMP(ST + 3);
    sfor<4 - MT, 4>([&](auto KQ) { sfor<0, 10>([&](auto K) { qmax(KQ, K); }); });
    sfor<0, 3>([&](auto C) { xmax(IC<1>{}, C); });
    pw = (GQA ? (w >> 1) * 5120 + (w & 1) * 8 : hh * 20480 + cq * 5120) + lane_id() * 16;
    bar();
    rmax(IC<1>{});
    sfor<0, 2>([&](auto KQ) { sfor<0, 10>([&](auto K) { pput(KQ, K, pw); }); });
    bool any = false;
    sfor<0, 5 * MT>([&](auto R) { any |= __builtin_fmaf(mx[R], SCL, nm[R]) > GROW; });
    grown = __builtin_amdgcn_ballot_w64(any) != 0;
    if (grown) {
      sfor<0, 5 * MT>([&](auto R) {
        const float d = __builtin_fmaf(mx[R], SCL, nm[R]);
        if (d > GROW) { ls[R] *= __builtin_amdgcn_exp2f(-d); nm[R] = -SCL * mx[R]; }
      });
    }
  }
  STAMP(ST + 4);

  const rsrc_t rO = mk_rsrc(p.wo[P], 512 * j * 896 + (w < 7 ? w : 0) * 128, w < 7 ? ((ABL & 1024) ? 8192u : W_BYTES - (512 * j * 896 + w * 128)) : 0u);
  {
    const int l = lane_id(), g = l >> 4, q = (l >> 2) & 3;
    const unsigned vv = voff_spl(l, 512);
    const unsigned a0 = LDS_RING + (GQA ? cq : w) * 8192 + (4 * g + q) * 128 + (((q >> 1) | ((g & 1) << 1)) * 32) + 8 * (l & 3);
    const unsigned p0 = hh * 20480 + l * 16;
    auto rda = [&](auto S, auto U) {
      lds_t a = lds + (a0 ^ (U * 32)) + (GQA ? (S & 3) * 2048 : (S & 1) * 4096);
      fa[S & 1][U] = ldtr(a, a + (GQA ? 32768 : 2048));
    };
    auto rdb = [&](auto S, auto T) { RDB(fb[T], ld128(lds + p0 + ((S >> 2) & 1) * 40960 + ((S & 3) * 5 + T) * 1024)); };
    unsigned ov = 0, ow = 0;
    auto piece = [&](auto G, auto I) {
      if constexpr (G < 16) dmap<true, I>(rV, vv, (G * 32 + I * 8) * 512);
      else dmap<false, I>(rO, ov, ((G - 16) * (GQA ? 16 : 32) + I * 8) * 896);
    };
    float rl[5];
    f32x4 lv[2];
    auto oput = [&](auto T, auto H) {
      const f32x4 u = acc[T][2 * H], v = acc[T][2 * H + 1];
      const float r = rl[T];
      *(LAS u32x4*)(lds + ow + H * 5120 + T * 1024) = u32x4{pk2(mul1(u[0], r), mul1(u[1], r)), pk2(mul1(u[2], r), mul1(u[3], r)),
                                                            pk2(mul1(v[0], r), mul1(v[1], r)), pk2(mul1(v[2], r), mul1(v[3], r))};
    };
    wait_vm_bar<GQA ? 6 : 4>();
    STAMP(ST + 5);
    sfor<0, 4>([&](auto U) { rda(IC<0>{}, U); });
    sfor<0, 5>([&](auto T) { rdb(IC<0>{}, T); });
    if constexpr (!GQA) { set_m0(ring); need(fa[0]); sfor<0, 4>([&](auto I) { piece(IC<2>{}, I); }); }
    sfor<0, 16>([&](auto S) {
      constexpr int s = S;
      SBAR();
      STAMP2(3, s);
      if constexpr (GQA) {
        if constexpr ((s & 3) == 3) wait_vm_bar<4>();
        else { need(fa[s & 1]); wait_vm_bar_raw<4>(); }
      } else {
        if constexpr (s == 15) bar();
        else { if constexpr ((s & 3) == 3) bar(); wait_vm<4>(); }
      }
      if constexpr (s == 8) {
        if (grown) {
          LAS float* em = (LAS float*)(lds + LDS_EXM + (hh * 16 + (lane_id() & 15)) * 48);
          sfor<0, 5>([&](auto T) {
            const float d = __builtin_fmaf(em[384 + T], SCL, mul1(em[T + 0], -SCL));
            const float a = d > GROW ? __builtin_amdgcn_exp2f(-d) : 1.f;
            sfor<0, 4>([&](auto U) { acc[T][U] *= a; });
          });
        }
      }
      if constexpr (s == 11) ov = voff_spl(lane_id(), 896);
      if constexpr (s == 15) ow = hh * 40960 + cq * 10240 + lane_id() * 16;
      sfor<0, 20>([&](auto I) {
        constexpr int i = I, t = i >> 2, u = i & 3;
        acc[t][u] = MFMA(fa[s & 1][u], fb[t], s == 0 ? ZERO4 : acc[t][u]);
        if constexpr (GQA) {
          if constexpr (i == 0) set_m0(ring + (s & 3) * 2048);
          if constexpr (i == 1 || i == 2) piece(IC<s + 4>{}, IC<i - 1>{});
          if constexpr (s < 15 && i >= 4 && i < 7) rda(IC<s + 1>{}, IC<i - 4>{});
          if constexpr (s < 15 && i == 8) rda(IC<s + 1>{}, IC<3>{});
        } else if constexpr (s < 15) {
          if constexpr (i < 4) rda(IC<s + 1>{}, IC<i & 3>{});
          if constexpr (i == 5) set_m0(ring + ((s + 1) & 1) * 4096);
          if constexpr (i == DM0) need(fa[(s + 1) & 1]);
          if constexpr (i >= DM0 && i < DM0 + 4) piece(IC<s + 3>{}, IC<(i - DM0) & 3>{});
        }
        if constexpr (s < 15 && u == 3) rdb(IC<s + 1>{}, IC<t>{});
        if constexpr (s < 10 && i >= 1 && i < 19) mop(IC<(180 + 18 * s + i - 1) % 360>{});
        if constexpr (s >= 3 && s <= 10 && (i == 5 || i == 10 || i == 14)) {
          constexpr int q4 = (s - 3) & 3, k = q4 * 3 - (q4 > 2) + (i == 5 ? 0 : i == 10 ? 1 : 2);
          if constexpr (k < 10 && !(q4 == 2 && i == 14)) pput(IC<s < 7 ? 2 : 3>{}, IC<k % 10>{}, pw);
        }
        if constexpr (s == 10 && (i == 3 || i == 9 || i == 15)) {
          const int l2 = lane_id();
          LAS float* el = (LAS float*)(lds + LDS_EXL + ((GQA ? 0 : hh) * 80 + (l2 & 15)) * 48 + (GQA ? w : cq) * 4);
          if constexpr (i == 3) el[(l2 >> 4) * 192] = red4<OpAdd>(ls[0], ls[1], ls[2], ls[3]);
          if constexpr (i == 9 && GQA) el[768 + ((l2 >> 4) & 1) * 960] = red2<OpAdd>(ls[4], ls[5 * MT - 1]);
          if constexpr (i == 9 && !GQA) el[768] = red2<OpAdd>(ls[4], ls[4]);
          if constexpr (i == 15 && GQA) el[960 + (l2 >> 4) * 192] = red4<OpAdd>(ls[5], ls[6], ls[7], ls[8]);
        }
        if constexpr (WARMNT == 2 && !LAST && s == 12 && i == 14)
          warm<true>(lds0, mk_rsrc(p.wo[P], 0, hh ? 0u : W_BYTES), 512 * j * 896 + (b >> 1) * 28672 + (cq * 64 + lane_id()) * 128);
        if constexpr ((s == 12 || s == 13) && (i % 8) == 1 && (s - 12) * 3 + i / 8 < 5) {
          lds_t e = lds + LDS_EXL + (hh * 80 + ((s - 12) * 3 + i / 8) * 16 + (lane_id() & 15)) * 48;
          lv[0] = *(LAS const f32x4*)e;
          if constexpr (GQA) lv[1] = *(LAS const f32x4*)(e + 16);
        }
        if constexpr ((s == 12 || s == 13) && i == (i / 8) * 8 + (i < 16 ? 6 : 3) && (s - 12) * 3 + i / 8 < 5) {
          float sum = ((lv[0][0] + lv[0][1]) + lv[0][2]) + lv[0][3];
          if constexpr (GQA) sum = (((sum + lv[1][0]) + lv[1][1]) + lv[1][2]) + lv[1][3];
          rl[((s - 12) * 3 + i / 8) % 5] = __builtin_amdgcn_rcpf(sum);
        }
        if constexpr (s == 15 && i >= 6 && (i & 1) == 0) oput(IC<(i - 6) / 4>{}, IC<((i - 6) / 2) % 2>{});
        SBAR();
      });
    });
    STAMP2(3, 16);
    STAMP(ST + 6);
    sfor<7, 10>([&](auto I) { oput(IC<I / 2>{}, IC<I % 2>{}); });
    if constexpr (LAST) wait_vm_bar<0>();
    else bar();
  }
  STAMP(ST + 7);

  {
    const rsrc_t rN = mk_rsrc(p.wq[1 - P], (512 * j + 64 * w) * 2, LAST ? 0u : W_BYTES - (512 * j + 64 * w) * 2);
    const int l = lane_id(), g = l >> 4, q = (l >> 2) & 3;
    const unsigned ov = voff_spl(l, 896), we = voff_nat(l, 8192);
    auto piece = [&](auto G, auto I) {
      if constexpr (G < 16) dmap<false, I>(rO, ov, (G * 32 + I * 8) * 896);
      else if constexpr (!LAST) dmap<false, I>(rN, we ^ ((I & 1) * 64), ((G - 16) * 32 + I * 8) * 8192);
    };
    if (w < 7) {
      const unsigned a0 = LDS_RING + w * 8192 + (4 * g + q) * 128 + (((q >> 1) | ((g & 1) << 1)) * 32) + 8 * (l & 3);
      auto rda = [&](auto S, auto U) { lds_t a = lds + (a0 ^ (U * 32)) + (S & 1) * 4096; fa[S & 1][U] = ldtr(a, a + 2048); };
      unsigned o1 = l * 16 + 40960;
      asm volatile("" : "+v"(o1));
      auto rdb = [&](auto S, auto T) { RDB(fb[T], ld128(lds + (S < 8 ? l * 16 : o1) + ((S & 7) * 5 + T) * 1024)); };
      const rsrc_t rS = mk_rsrc(p.slab, ((P * 32 + b) * 8 + j) * SLAB + w * 10240, 10240);
      auto sput = [&](auto T, auto H) {
        const f32x4 u = acc[T][2 * H], v = acc[T][2 * H + 1];
        auto r0 = __builtin_amdgcn_permlane16_swap(pk2(u[0], u[1]), pk2(v[0], v[1]), false, false);
        auto r1 = __builtin_amdgcn_permlane16_swap(pk2(u[2], u[3]), pk2(v[2], v[3]), false, false);
        __builtin_amdgcn_raw_buffer_store_b128(u32x4{r0[0], r1[0], r0[1], r1[1]}, rS, l * 16 + (T * 2 + H) * 1024, 0, 16  );
      };
      wait_vm<4>();
      sfor<0, 4>([&](auto U) { rda(IC<0>{}, U); });
      sfor<0, 5>([&](auto T) { rdb(IC<0>{}, T); });
      set_m0(ring);
      need(fa[0]);
      sfor<0, 4>([&](auto I) { piece(IC<2>{}, I); });
      sfor<0, 16>([&](auto S) {
        constexpr int s = S;
        SBAR();
        STAMP2(5, s);
        if constexpr (s < 15) { if constexpr (!(LAST && (ABL & 1))) wait_vm<(LAST && s == 14) ? 0 : 4>(); }
        else if constexpr (!LAST) { bar(); issue_x(p, 1 - P, b, w, lds0); }
        sfor<0, 20>([&](auto I) {
          constexpr int i = I, t = i >> 2, u = i & 3;
          acc[t][u] = MFMA(fa[s & 1][u], fb[t], s == 0 ? ZERO4 : acc[t][u]);
          if constexpr (s < 15) {
            if constexpr (!(LAST && (ABL & 4) && s > 0)) {
            if constexpr (i < 4) rda(IC<s + 1>{}, IC<i & 3>{});
            }
            if constexpr (!(LAST && (ABL & 8)))
            if constexpr (u == 3) rdb(IC<s + 1>{}, IC<t>{});
            if constexpr (!(LAST && (s >= 13 || (ABL & 2)))) {
              if constexpr (i == 5) set_m0(ring + ((s + 1) & 1) * 4096);
              if constexpr (i == DM0) need(fa[(s + 1) & 1]);
              if constexpr (i >= DM0 && i < DM0 + 4) piece(IC<s + 3>{}, IC<(i - DM0) & 3>{});
            }
          } else if constexpr (i >= 6 && (i & 1) == 0) sput(IC<(i - 6) / 4>{}, IC<((i - 6) / 2) % 2>{});
          SBAR();
        });
      });
      STAMP2(5, 16);
      STAMP(ST + 8);
      sfor<7, 10>([&](auto I) { sput(IC<I / 2>{}, IC<I % 2>{}); });
      if constexpr (!LAST) wait_vm_bar<10>();
    } else if constexpr (!LAST) {
      wait_vm<0>();
      set_m0(ring);
      sfor<0, 4>([&](auto I) { piece(IC<16>{}, I); });
      set_m0(ring + 4096);
      sfor<0, 4>([&](auto I) { piece(IC<17>{}, I); });
      if constexpr (WARMNT) sfor<0, 4>([&](auto I) { warm<true>(lds0, mk_rsrc(p.wq[1 - P], 0, W_BYTES), ((b >> 1) * 28 + I * 8 + (l >> 3)) * 8192 + 1024 * j + (l & 7) * 128); });
      if constexpr (XW == 1) sfor<0, 9>([&](auto I) { warm(lds0, mk_rsrc(p.x[1 - P], b * X_BYTES, X_BYTES), (I * 64 + l) * 128); });
      bar();
      issue_x(p, 1 - P, b, w, lds0);
      wait_vm_bar<0>();
    } else {
      arrive(p, b, 0);
      wait_all(p, b, 0);
    }
  }
  STAMP(ST + 9);
}

__global__ __launch_bounds__(512, 2) void fused_attn_kernel(Params p) {
  extern __shared__ __attribute__((aligned(16))) char smem[];
  lds_t lds = (lds_t)smem;
  const unsigned lds0 = (unsigned)(uintptr_t)lds;
  const int w = __builtin_amdgcn_readfirstlane(threadIdx.x >> 6);
  const int j = blockIdx.x & 7, b = blockIdx.x >> 3;
  const int first = b & 1;
  STAMP(0);
  {
    issue_x(p, first, b, w, lds0);
    const int l = lane_id();
    const rsrc_t rW = mk_rsrc(p.wq[first], (512 * j + 64 * w) * 2, W_BYTES - (512 * j + 64 * w) * 2);
    const unsigned we = voff_nat(l, 8192), ring = lds0 + LDS_RING + w * 8192;
    sfor<0, 2>([&](auto G) {
      set_m0(ring + G * 4096);
      sfor<0, 4>([&](auto I) { dmap<false, I>(rW, we ^ ((I & 1) * 64), (G * 32 + I * 8) * 8192); });
    });
    const unsigned id = (w & 3) * 64 + l;
    if (w >= 4 || WARMNT == 2) warm<WARMNT != 0>(lds0, mk_rsrc(p.wq[first], 0, W_BYTES), ((b >> 1) * 28 + (id >> 3)) * 8192 + 1024 * j + (id & 7) * 128);
    else warm(lds0, mk_rsrc(p.wo[first], 0, WOL ? 0u : W_BYTES), 512 * j * 896 + (b >> 1) * 28672 + id * 128);
    warm(lds0, mk_rsrc(p.x[1 - first], b * X_BYTES, X_BYTES), (XW == 2 && l < 9) ? (j * 70 + w * 9 + l) * 128 : X_BYTES);
    wait_vm_bar<10>();
  }
  STAMP(10);
  if (first == 0) {
    run_problem<0, false, 0>(p, lds, lds0, b, j, w);
    run_problem<1, true, 10>(p, lds, lds0, b, j, w);
  } else {
    run_problem<1, false, 0>(p, lds, lds0, b, j, w);
    run_problem<0, true, 10>(p, lds, lds0, b, j, w);
  }
  wait_vm<0>();
  STAMP(21);
  bar();
  STAMP(22);
  if (w == 7) arrive(p, b, 1);
  reduce_rows<2>(p, first, b, j, w, 8);
  if (w == 7) wait_all(p, b, 1);
  bar();
  STAMP(23);
  reduce_rows<2>(p, 1 - first, b, j, w, 8);
  STAMP(20);
}

extern "C" void kernel_launch(void* const* d_in, const int* in_sizes, int n_in, void* d_out, int out_size, void* d_ws,
                              size_t ws_size, hipStream_t stream) {
  Params p;
  for (int i = 0; i < 2; ++i) {
    p.x[i] = d_in[5 * i + 0]; p.k[i] = d_in[5 * i + 1]; p.v[i] = d_in[5 * i + 2]; p.wq[i] = d_in[5 * i + 3]; p.wo[i] = d_in[5 * i + 4];
  }
  p.cnt = (unsigned*)d_ws;
  p.slab = (char*)d_ws + 65536;
  p.out = d_out;
  p.diag = (unsigned long long*)((char*)d_ws + (64u << 20));
  (void)hipFuncSetAttribute((const void*)fused_attn_kernel, hipFuncAttributeMaxDynamicSharedMemorySize, LDS_TOTAL);
  (void)hipMemsetAsync(d_ws, 0, 8192, stream);
  fused_attn_kernel<<<256, 512, LDS_TOTAL, stream>>>(p);
}
```

```cpp
#include <hip/hip_runtime.h>
#include <hip/hip_bf16.h>
#include <stdint.h>
#include <type_traits>

typedef short bf16x8 __attribute__((ext_vector_type(8)));
typedef short s16x4 __attribute__((ext_vector_type(4)));
typedef float f32x4 __attribute__((ext_vector_type(4)));
typedef float f32x2 __attribute__((ext_vector_type(2)));
typedef __bf16 bf16x2 __attribute__((ext_vector_type(2)));
typedef unsigned u32x4 __attribute__((ext_vector_type(4)));
typedef unsigned u32x2 __attribute__((ext_vector_type(2)));
#define LAS __attribute__((address_space(3)))
typedef __amdgpu_buffer_rsrc_t rsrc_t;
typedef LAS char* lds_t;

#define DIAG 0
#define DLAST 1
#define ABL 0
#define WARMNT 0
#define XW 0
#define WOL 0
#define PRE1 6
#define XNT 0
#define FAKEKV 0

constexpr int LDS_RING = 81920;
constexpr int LDS_EXM = 147456;
constexpr int LDS_EXL = 151552;
constexpr int LDS_DUMMY = 159232;
constexpr int LDS_OLD = 159488;
constexpr int LDS_TOTAL = DIAG ? 163840 : 159552;
constexpr unsigned X_BYTES = 80 * 448 * 2;
constexpr unsigned W_BYTES = 448 * 4096 * 2;
constexpr unsigned KV_HEAD = 512 * 256 * 2;
constexpr unsigned SLAB = 80 * 448 * 2;
constexpr float SCL = 0.0625f * 1.4426950408889634f;
constexpr float GROW = 20.f;

template <int I, int N, class F>
__device__ __forceinline__ void sfor(F&& f) {
  if constexpr (I < N) { f(std::integral_constant<int, I>{}); sfor<I + 1, N>(f); }
}
#define SBAR() __builtin_amdgcn_sched_barrier(0)
#define MFMA(a, b, c) __builtin_amdgcn_mfma_f32_16x16x32_bf16(a, b, c, 0, 0, 0)
template <int N> __device__ __forceinline__ void wait_vm() { asm volatile("s_waitcnt vmcnt(%0)" ::"i"(N) : "memory"); }
__device__ __forceinline__ void bar() { asm volatile("s_waitcnt lgkmcnt(0)\n\ts_barrier" ::: "memory"); }
template <int N> __device__ __forceinline__ void wait_vm_bar_raw() { asm volatile("s_waitcnt vmcnt(%0)\n\ts_barrier" ::"i"(N) : "memory"); }
template <int N> __device__ __forceinline__ void wait_vm_bar() { asm volatile("s_waitcnt vmcnt(%0) lgkmcnt(0)\n\ts_barrier" ::"i"(N) : "memory"); }
__device__ __forceinline__ int lane_id() {
  int l; asm volatile("v_mbcnt_lo_u32_b32 %0, -1, 0\n\tv_mbcnt_hi_u32_b32 %0, -1, %0" : "=v"(l)); return l;
}
__device__ __forceinline__ unsigned pk2(float a, float b) {
  return __builtin_bit_cast(unsigned, __builtin_convertvector(f32x2{a, b}, bf16x2));
}
__device__ __forceinline__ rsrc_t mk_rsrc(const void* p, unsigned off, unsigned bytes) {
  return __builtin_amdgcn_make_buffer_rsrc((void*)((const char*)p + off), (short)0, (int)bytes, 0x00020000);
}
__device__ __forceinline__ void need(const bf16x8 (&f)[4]) { asm volatile("" ::"v"(f[0]), "v"(f[1]), "v"(f[2]), "v"(f[3]) : "memory"); }
__device__ __forceinline__ void set_m0(unsigned m0v) { asm volatile("s_mov_b32 m0, %0\n\ts_nop 0" ::"s"(m0v) : "memory"); }
template <bool NT, int I>
__device__ __forceinline__ void dmap(rsrc_t r, unsigned v, unsigned src) {
  if constexpr (NT && !FAKEKV) asm volatile("buffer_load_dwordx4 %0, %1, %2 offen offset:%3 nt lds" ::"v"(v), "s"(r), "s"(src - 1024u * I), "i"(1024 * I) : "memory");
  else asm volatile("buffer_load_dwordx4 %0, %1, %2 offen offset:%3 lds" ::"v"(v), "s"(r), "s"(src - 1024u * I), "i"(1024 * I) : "memory");
}
template <bool NT = false>
__device__ __forceinline__ void warm(unsigned lds0, rsrc_t r, unsigned v) {
  if constexpr (NT) asm volatile("s_mov_b32 m0, %0\n\ts_nop 0\n\tbuffer_load_dword %1, %2, 0 offen nt lds" ::"s"(lds0 + LDS_DUMMY), "v"(v), "s"(r) : "memory");
  else asm volatile("s_mov_b32 m0, %0\n\ts_nop 0\n\tbuffer_load_dword %1, %2, 0 offen lds" ::"s"(lds0 + LDS_DUMMY), "v"(v), "s"(r) : "memory");
}
extern "C" __device__ float llvm_fmul_legacy(float, float) __asm("llvm.amdgcn.fmul.legacy");
__device__ __forceinline__ float mul1(float a, float b) { return llvm_fmul_legacy(a, b); }
__device__ __forceinline__ bf16x8 ld128(lds_t p) { return *(LAS const bf16x8*)p; }
#define RDB(dst, expr) do { if constexpr (ABL & 256) asm volatile("" : "+v"(dst)); else dst = (expr); } while (0)
#define EXP2(x) ((ABL & 512) ? (x) : __builtin_amdgcn_exp2f(x))
__device__ __forceinline__ bf16x8 ldtr(lds_t p0, lds_t p1) {
  s16x4 lo = __builtin_amdgcn_ds_read_tr16_b64_v4i16((LAS s16x4*)p0);
  s16x4 hi = __builtin_amdgcn_ds_read_tr16_b64_v4i16((LAS s16x4*)p1);
  return __builtin_shufflevector(lo, hi, 0, 1, 2, 3, 4, 5, 6, 7);
}
__device__ __forceinline__ float max3(float a, float b, float c) { return __builtin_fmaxf(__builtin_fmaxf(a, b), c); }
struct OpMax { static __device__ __forceinline__ float f(float a, float b) { return max3(a, b, b); } };
struct OpAdd { static __device__ __forceinline__ float f(float a, float b) { return a + b; } };
template <class Op> __device__ __forceinline__ float sw16(float a, float b) {
  auto r = __builtin_amdgcn_permlane16_swap(__float_as_uint(a), __float_as_uint(b), false, false);
  return Op::f(__uint_as_float(r[0]), __uint_as_float(r[1]));
}
template <class Op> __device__ __forceinline__ float sw32(float a, float b) {
  auto r = __builtin_amdgcn_permlane32_swap(__float_as_uint(a), __float_as_uint(b), false, false);
  return Op::f(__uint_as_float(r[0]), __uint_as_float(r[1]));
}
template <class Op> __device__ __forceinline__ float red4(float a, float b, float c, float d) { return sw32<Op>(sw16<Op>(a, b), sw16<Op>(c, d)); }
template <class Op> __device__ __forceinline__ float red2(float a, float b) { const float x = sw16<Op>(a, b); return sw32<Op>(x, x); }

__device__ __forceinline__ unsigned voff_nat(int l, unsigned pitch) { return (l >> 3) * pitch + 16 * ((l & 7) ^ (((l >> 4) & 1) << 1)); }
__device__ __forceinline__ unsigned voff_spl(int l, unsigned pitch) { return (l >> 3) * pitch + 16 * ((l & 7) ^ (((l >> 4) & 3) << 1)); }
__device__ __forceinline__ unsigned voff_k(int l) { return (l >> 3) * 512 + 16 * ((l & 7) ^ ((l >> 4) & 3)); }

struct Params {
  const void *x[2], *k[2], *v[2], *wq[2], *wo[2];
  void* slab;
  unsigned* sync;
  void* out;
  unsigned long long* diag;
};

#define STAMP(i)
#define STAMP2(ph, i)

#define DM0 7
template <int V> using IC = std::integral_constant<int, V>;
constexpr f32x4 ZERO4 = {0.f, 0.f, 0.f, 0.f};

__device__ __forceinline__ void issue_x(const Params& p, int P, int b, int w, unsigned lds0) {
  const int l = lane_id();
  const unsigned v = (l >> 3) * 896 + 16 * ((l & 7) ^ ((l >> 4) & 3));
  const rsrc_t rx = mk_rsrc(p.x[P], b * X_BYTES, X_BYTES);
  sfor<0, 9>([&](auto I) {
    const unsigned pc = w * 9 + I, blk = (pc * 205) >> 11, rg = pc - blk * 10;
    set_m0(lds0 + pc * 1024);
    dmap<XNT != 0, 0>(rx, v ^ ((rg & 1) * 64), rg * 7168 + blk * 128);
  });
}

typedef __attribute__((address_space(1))) unsigned gu32;
#define RLX __ATOMIC_RELAXED, __HIP_MEMORY_SCOPE_AGENT
__device__ __forceinline__ gu32* flag_word(const Params& p, int b, int j) { return (gu32*)p.sync + (b * 8 + j) * 32; }
__device__ __forceinline__ gu32* ack_word(const Params& p, int b, int prod, int cons) { return (gu32*)p.sync + 8192 + ((b * 8 + prod) * 8 + cons) * 16; }
template <int STAGE>
__device__ __forceinline__ void arrive(const Params& p, lds_t lds, int b, int j) {
  const unsigned old = *(LAS const unsigned*)(lds + LDS_OLD + j * 4);
  if constexpr (STAGE == 1) {
    gu32* ack = ack_word(p, b, j, lane_id() & 7);
    for (unsigned spins = 0; spins < (1u << 22); ++spins) {
      if (__builtin_amdgcn_ballot_w64(__hip_atomic_load(ack, RLX) != old + 1u) == 0) break;
      __builtin_amdgcn_s_sleep(4);
    }
  }
  if (lane_id() == 0) __hip_atomic_store(flag_word(p, b, j), old + STAGE, RLX);
}
template <int STAGE>
__device__ __forceinline__ void wait_all(const Params& p, lds_t lds, int b) {
  const int c = lane_id() & 7;
  const unsigned old = *(LAS const unsigned*)(lds + LDS_OLD + c * 4);
  gu32* flag = flag_word(p, b, c);
  for (unsigned spins = 0; spins < (1u << 22); ++spins) {
    if (__builtin_amdgcn_ballot_w64(__hip_atomic_load(flag, RLX) - old - STAGE > 2u - STAGE) == 0) break;
    __builtin_amdgcn_s_sleep(4);
  }
  asm volatile("" ::: "memory");
}
template <int N>
__device__ __forceinline__ void reduce_rows(const Params& p, int P, int b, int j, int first, int step) {
  const int l = lane_id(), n = l & 15, g = l >> 4;
  const int r0 = (70 * j) >> 3, nr = ((70 * (j + 1)) >> 3) - r0;
  const rsrc_t rS = mk_rsrc(p.slab, (P * 32 + b) * 8 * SLAB, 8 * SLAB);
  u32x4 v[N][8];
  sfor<0, N>([&](auto K) {
    const int it = first + step * K;
    const unsigned off = it < nr ? (r0 + it) * 1024 + l * 16 : 0x80000000u;
    sfor<0, 8>([&](auto J) { v[K][J] = __builtin_amdgcn_raw_buffer_load_b128(rS, off, J * SLAB, 16  ); });
  });
  sfor<0, N>([&](auto K) {
    const int it = first + step * K, f = r0 + it;
    if (it < nr) {
      float sm[8];
      sfor<0, 4>([&](auto I) { sm[2 * I] = __uint_as_float(v[K][0][I + 0] << 16); sm[2 * I + 1] = __uint_as_float(v[K][0][I + 0] & 0xffff0000u); });
      sfor<1, 8>([&](auto J) {
        sfor<0, 4>([&](auto I) { sm[2 * I] += __uint_as_float(v[K][J][I + 0] << 16); sm[2 * I + 1] += __uint_as_float(v[K][J][I + 0] & 0xffff0000u); });
      });
      const int ww = f / 10, t = (f - ww * 10) >> 1, h = f & 1;
      const int col = P * 448 + 64 * ww + 16 * (2 * h + (g & 1)) + 8 * (g >> 1);
      *(u32x4*)((char*)p.out + ((size_t)(b * 80 + t * 16 + n) * 896 + col) * 2) = u32x4{pk2(sm[0], sm[1]), pk2(sm[2], sm[3]), pk2(sm[4], sm[5]), pk2(sm[6], sm[7])};
    }
  });
}

template <int P  , bool LAST, int ST>
__device__ __forceinline__ void run_problem(const Params& p, lds_t lds, unsigned lds0, int b, int j, int w) {
  constexpr bool GQA = (P == 0);
  const int bk = FAKEKV ? 0 : b;
  const int hh = w >> 2, cq = w & 3;
  const unsigned ring = lds0 + LDS_RING + w * 8192;
  f32x4 acc[5][4];
  bf16x8 fa[2][4], fb[5];

  const unsigned koff = GQA ? (bk * 8 + j) * KV_HEAD + w * 16 * 512 : (bk * 16 + 2 * j + hh) * KV_HEAD + cq * 32 * 512;
  const rsrc_t rK = mk_rsrc(p.k[P], koff, KV_HEAD - (GQA ? w * 16 * 512 : cq * 32 * 512));
  auto k_src = [](int g, int i) -> unsigned {
    return GQA ? ((g >> 2) * 256 + (i >> 1) * 128 + (i & 1) * 8) * 512 + (g & 3) * 128 : ((g >> 2) * 128 + i * 8) * 512 + (g & 3) * 128;
  };

  {
    const rsrc_t rW = mk_rsrc(p.wq[P], (512 * j + 64 * w) * 2, (ABL & 1024) ? 65536u : W_BYTES - (512 * j + 64 * w) * 2);
    const int l = lane_id(), n = l & 15, g = l >> 4, q = (l >> 2) & 3;
    const unsigned we = voff_nat(l, 8192), ke = voff_k(l);
    const unsigned a0 = LDS_RING + w * 8192 + (8 * g + q) * 128 + (((q >> 1) | ((g & 1) << 1)) * 32) + 8 * (l & 3);
    unsigned x0[2][2];
    x0[0][0] = n * 128 + 16 * (g ^ ((n >> 1) & 7)); x0[1][0] = x0[0][0] ^ 64; x0[0][1] = x0[0][0] + 40960; x0[1][1] = x0[1][0] + 40960;
    asm volatile("" : "+v"(x0[0][1]), "+v"(x0[1][1]));
    const unsigned qw = hh * 40960 + cq * 10240 + l * 16;
    auto rda = [&](auto S, auto U) { lds_t a = lds + (a0 ^ (U * 32)) + (S & 1) * 4096; fa[S & 1][U] = ldtr(a, a + 512); };
    auto rdb = [&](auto S, auto T) { fb[T] = ld128(lds + x0[S & 1][S >= 8] + ((S >> 1) & 3) * 10240 + T * 2048); };
    auto piece = [&](auto G, auto I) {
      if constexpr (G < 14) dmap<false, I>(rW, we ^ ((I & 1) * 64), (G * 32 + I * 8) * 8192);
      else dmap<true, I>(rK, ke ^ ((I & 1) * 64), k_src(G - 14, I));
    };
    auto qput = [&](auto T, auto H) {
      const f32x4 u = acc[T][2 * H], v = acc[T][2 * H + 1];
      *(LAS u32x4*)(lds + qw + H * 5120 + T * 1024) = u32x4{pk2(u[0], u[1]), pk2(u[2], u[3]), pk2(v[0], v[1]), pk2(v[2], v[3])};
    };
    if constexpr (!LAST) wait_vm<6>();
    sfor<0, 4>([&](auto U) { rda(IC<0>{}, U); });
    sfor<0, 5>([&](auto T) { rdb(IC<0>{}, T); });
    set_m0(ring);
    need(fa[0]);
    sfor<0, 4>([&](auto I) { piece(IC<2>{}, I); });
    sfor<0, 14>([&](auto S) {
      constexpr int s = S;
      SBAR();
      STAMP2(4, s);
      if constexpr (s == 0) { if constexpr (!LAST) wait_vm<6>(); }
      else if constexpr (s < 13) wait_vm<4>();
      else {
        bar();
        *(LAS f32x2*)(lds + LDS_EXM + w * 512 + l * 8) = f32x2{-INFINITY, -INFINITY};
        if constexpr (!LAST && WOL) warm(lds0, mk_rsrc(p.wo[P], 0, W_BYTES), l < 28 ? 512 * j * 896 + (b >> 1) * 28672 + (w * 28 + l) * 128 : W_BYTES);
      }
      sfor<0, 20>([&](auto I) {
        constexpr int i = I, t = i >> 2, u = i & 3;
        acc[t][u] = MFMA(fa[s & 1][u], fb[t], s == 0 ? ZERO4 : acc[t][u]);
        if constexpr (s < 13) {
          if constexpr (i < 4) rda(IC<s + 1>{}, IC<i & 3>{});
          if constexpr (u == 3) rdb(IC<s + 1>{}, IC<t>{});
          if constexpr (i == 5) set_m0(ring + ((s + 1) & 1) * 4096);
          if constexpr (i == DM0) need(fa[(s + 1) & 1]);
          if constexpr (i >= DM0 && i < DM0 + 4) piece(IC<s + 3>{}, IC<(i - DM0) & 3>{});
        } else if constexpr (i >= 6 && (i & 1) == 0) qput(IC<(i - 6) / 4>{}, IC<((i - 6) / 2) % 2>{});
        SBAR();
      });
    });
    STAMP2(4, 14);
    STAMP(ST + 1);
    sfor<7, 10>([&](auto I) { qput(IC<I / 2>{}, IC<I % 2>{}); });
    bar();
  }
  STAMP(ST + 2);

  const rsrc_t rV = GQA ? mk_rsrc(p.v[P], (bk * 8 + j) * KV_HEAD + hh * 16 * 512 + cq * 128, KV_HEAD - (hh * 16 * 512 + cq * 128))
                        : mk_rsrc(p.v[P], (bk * 16 + 2 * j + hh) * KV_HEAD + cq * 128, KV_HEAD - cq * 128);
  constexpr int MT = GQA ? 2 : 1;
  constexpr int NG = GQA ? 8 : 16;
  f32x4 sc[4][10];
  float mx[5 * MT], nm[5 * MT], ls[5 * MT], ev[4];
  u32x2 pq[4][10];
  auto mop = [&](auto ID) {
    constexpr int id = ID, kq = id / 90, k = (id / 9) % 10, part = id % 9, r = GQA ? k : (k >> 1);
    if constexpr (part == 0) { ev[0] = __builtin_fmaf(sc[kq][k][0], SCL, nm[r]); ev[1] = __builtin_fmaf(sc[kq][k][1], SCL, nm[r]); }
    if constexpr (part == 1) ev[0] = EXP2(ev[0]);
    if constexpr (part == 2) { ev[2] = __builtin_fmaf(sc[kq][k][2], SCL, nm[r]); ev[3] = __builtin_fmaf(sc[kq][k][3], SCL, nm[r]); }
    if constexpr (part == 3) ev[1] = EXP2(ev[1]);
    if constexpr (part == 4) ev[2] = EXP2(ev[2]);
    if constexpr (part == 5) { if constexpr (kq == 0 && (GQA || (k & 1) == 0)) ls[r] = ev[0] + ev[1]; else ls[r] = (ls[r] + ev[0]) + ev[1]; }
    if constexpr (part == 6) ev[3] = EXP2(ev[3]);
    if constexpr (part == 7) { pq[kq][k][0] = pk2(ev[0], ev[1]); ls[r] += ev[2]; }
    if constexpr (part == 8) { pq[kq][k][1] = pk2(ev[2], ev[3]); ls[r] += ev[3]; }
    if constexpr (part == 0) asm volatile("" : "+v"(ev[0]), "+v"(ev[1]));
    if constexpr (part == 1) asm volatile("" : "+v"(ev[0]));
    if constexpr (part == 2) asm volatile("" : "+v"(ev[2]), "+v"(ev[3]));
    if constexpr (part == 3) asm volatile("" : "+v"(ev[1]));
    if constexpr (part == 4) asm volatile("" : "+v"(ev[2]));
    if constexpr (part == 5) asm volatile("" : "+v"(ls[r]));
    if constexpr (part == 6) asm volatile("" : "+v"(ev[3]));
    if constexpr (part == 7) asm volatile("" : "+v"(ls[r]), "+v"(pq[kq][k][0]));
    if constexpr (part == 8) asm volatile("" : "+v"(ls[r]), "+v"(pq[kq][k][1]));
  };
  auto pput = [&](auto KQ, auto K, unsigned pw) {
    constexpr int kq = KQ, k = K;
    if constexpr (GQA) *(LAS u32x2*)(lds + pw + (kq & 1) * 40960 + (k / 5) * 20480 + (k % 5) * 1024) = pq[kq][k];
    else if constexpr (k & 1) *(LAS u32x4*)(lds + pw + (kq & 1) * 40960 + (k / 2) * 1024) = u32x4{pq[kq][k - 1][0], pq[kq][k - 1][1], pq[kq][k][0], pq[kq][k][1]};
  };
  bool grown;
  unsigned pw;
  {
    const int l = lane_id(), n = l & 15, g = l >> 4;
    const unsigned ke = voff_k(l);
    unsigned vv = 0;
    const unsigned k0 = LDS_RING + w * 8192 + n * 128 + 16 * ((g >> 1) ^ ((n >> 1) & 7)) + 8 * (g & 1);
    const unsigned q0 = (GQA ? 0 : hh * 40960) + l * 16;
    unsigned q1 = q0 + 40960;
    asm volatile("" : "+v"(q1));
    constexpr int NSUB = 2 * MT;
    auto rdk = [&](auto G, auto I) {
      lds_t a = lds + (I & 1) * 2048 + (G & 1) * 4096;
      const s16x4 lo = *(LAS const s16x4*)(a + (k0 ^ ((I >> 1) * 64))), hi = *(LAS const s16x4*)(a + (k0 ^ ((I >> 1) * 64 + 32)));
      fa[G & 1][I] = __builtin_shufflevector(lo, hi, 0, 1, 2, 3, 4, 5, 6, 7);
    };
    auto rdq = [&](auto Z, auto T) {
      constexpr int z = Z, gi = z / NSUB, e = (z % NSUB) / MT, h = z % MT;
      RDB(fb[T], ld128(lds + (h ? q1 : q0) + ((2 * (gi & 3) + e) * 5 + T) * 1024));
    };
    auto piece = [&](auto G, auto I) {
      if constexpr (G < NG) dmap<true, I>(rK, ke ^ ((I & 1) * 64), k_src(G, I));
      else if constexpr (!GQA) dmap<true, I>(rV, vv, ((G - NG) * 32 + I * 8) * 512);
      else dmap<true, I>(rV, vv, ((2 * (G - NG) + (I >> 1)) * 32 + (I & 1) * 8) * 512);
    };
    auto qmax = [&](auto KQ, auto K) {
      constexpr int kq = KQ, k = K, r = GQA ? k : (k >> 1);
      if constexpr ((kq & 1) == 0 && (GQA || (k & 1) == 0)) mx[r] = max3(max3(sc[kq][k][0], sc[kq][k][1], sc[kq][k][2]), sc[kq][k][3], sc[kq][k][3]);
      else mx[r] = max3(max3(mx[r], sc[kq][k][0], sc[kq][k][1]), sc[kq][k][2], sc[kq][k][3]);
      asm volatile("" : "+v"(mx[r]));
    };
    auto xmax = [&](auto HALF, auto C) {
      const int l2 = lane_id(), g2 = l2 >> 4;
      LAS float* em = (LAS float*)(lds + LDS_EXM + HALF * 1536 + ((GQA ? 0 : hh) * 16 + (l2 & 15)) * 48);
      if constexpr (C == 0) __builtin_amdgcn_ds_fmaxf(em + g2, red4<OpMax>(mx[0], mx[1], mx[2], mx[3]), 0, 0, false);
      if constexpr (C == 1 && GQA) __builtin_amdgcn_ds_fmaxf(em + (g2 & 1) * 192 + 4, red2<OpMax>(mx[4], mx[5 * MT - 1]), 0, 0, false);
      if constexpr (C == 1 && !GQA) __builtin_amdgcn_ds_fmaxf(em + 4, red2<OpMax>(mx[4], mx[4]), 0, 0, false);
      if constexpr (C == 2 && GQA) __builtin_amdgcn_ds_fmaxf(em + 192 + g2, red4<OpMax>(mx[5], mx[6], mx[7], mx[8]), 0, 0, false);
    };
    auto rmax = [&](auto HALF) {
      LAS float* em = (LAS float*)(lds + LDS_EXM + HALF * 1536 + ((GQA ? 0 : hh) * 16 + (lane_id() & 15)) * 48);
      sfor<0, MT>([&](auto H) {
        const f32x4 v = *(LAS const f32x4*)(em + H * 192);
        sfor<0, 4>([&](auto T) { mx[H * 5 + T] = v[T + 0]; });
        mx[H * 5 + 4] = em[H * 192 + 4];
      });
    };
    wait_vm<(!LAST && WOL) ? 5 : 4>();
    sfor<0, 4>([&](auto I) { rdk(IC<0>{}, I); });
    sfor<0, 5>([&](auto T) { rdq(IC<0>{}, T); });
    set_m0(ring);
    need(fa[0]);
    sfor<0, 4>([&](auto I) { piece(IC<2>{}, I); });
    sfor<0, 32>([&](auto Z) {
      constexpr int z = Z, gi = z / NSUB, sub = z % NSUB, e = sub / MT, h = sub % MT;
      SBAR();
      STAMP2(2, z);
      if constexpr (z == 19) { bar(); rmax(IC<0>{}); }
      if constexpr (z == (NG - 3) * NSUB) vv = voff_spl(lane_id(), 512);
      if constexpr (sub == 0 && gi + 1 < NG) wait_vm<(!LAST && WOL && gi == 0) ? 5 : 4>();
      sfor<0, 10>([&](auto I) {
        constexpr int i = I, t = i >> 1, u = i & 1;
        constexpr int kq = GQA ? 2 * (gi >> 2) + u : (gi >> 2), k = GQA ? 5 * h + t : i;
        sc[kq][k] = MFMA(fa[gi & 1][2 * e + u], fb[t], ((gi & 3) == 0 && e == 0) ? ZERO4 : sc[kq][k]);
        if constexpr (u == 1 && z + 1 < 32) rdq(IC<z + 1>{}, IC<t>{});
        if constexpr (gi + 1 < NG) {
          if constexpr (sub == 0 && i < 4) rdk(IC<gi + 1>{}, IC<i & 3>{});
          if constexpr (sub == 0 && i == 5) set_m0(ring + ((gi + 1) & 1) * 4096);
          if constexpr (sub == 0 && i == 6) need(fa[(gi + 1) & 1]);
          if constexpr (sub == 0 && i >= 6) piece(IC<gi + 3>{}, IC<(i - 6) & 3>{});
        }
        if constexpr (z == 16 || z == 17) qmax(IC<z - 16>{}, I);
        if constexpr (z == 18 && (i == 1 || i == 4 || i == 7)) xmax(IC<0>{}, IC<i / 3>{});
        if constexpr (z == 19 && i >= 5) sfor<0, MT>([&](auto H) { nm[H * 5 + i - 5] = mul1(mx[H * 5 + i - 5], -SCL); });
        if constexpr (z >= 20) { constexpr int x = (z - 20) * 10 + i; sfor<(3 * x + 1) / 2, (3 * x + 4) / 2>(mop); }
        if constexpr (!GQA && z == 26) qmax(IC<2>{}, I);
        SBAR();
      });
    });
    STAMP2(2, 32);
    STAMP(ST + 3);
    sfor<4 - MT, 4>([&](auto KQ) { sfor<0, 10>([&](auto K) { qmax(KQ, K); }); });
    sfor<0, 3>([&](auto C) { xmax(IC<1>{}, C); });
    pw = (GQA ? (w >> 1) * 5120 + (w & 1) * 8 : hh * 20480 + cq * 5120) + lane_id() * 16;
    bar();
    rmax(IC<1>{});
    sfor<0, 10>([&](auto K) { pput(IC<0>{}, K, pw); });
    sfor<0, GQA ? PRE1 : 0>([&](auto K) { pput(IC<1>{}, K, pw); });
    bool any = false;
    sfor<0, 5 * MT>([&](auto R) { any |= __builtin_fmaf(mx[R], SCL, nm[R]) > GROW; });
    grown = __builtin_amdgcn_ballot_w64(any) != 0;
    if (grown) {
      sfor<0, 5 * MT>([&](auto R) {
        const float d = __builtin_fmaf(mx[R], SCL, nm[R]);
        if (d > GROW) { ls[R] *= __builtin_amdgcn_exp2f(-d); nm[R] = -SCL * mx[R]; }
      });
    }
  }
  STAMP(ST + 4);

  const rsrc_t rO = mk_rsrc(p.wo[P], 512 * j * 896 + (w < 7 ? w : 0) * 128, w < 7 ? ((ABL & 1024) ? 8192u : W_BYTES - (512 * j * 896 + w * 128)) : 0u);
  {
    const int l = lane_id(), g = l >> 4, q = (l >> 2) & 3;
    const unsigned vv = voff_spl(l, 512);
    const unsigned a0 = LDS_RING + (GQA ? cq : w) * 8192 + (4 * g + q) * 128 + (((q >> 1) | ((g & 1) << 1)) * 32) + 8 * (l & 3);
    const unsigned p0 = hh * 20480 + l * 16;
    auto rda = [&](auto S, auto U) {
      lds_t a = lds + (a0 ^ (U * 32)) + (GQA ? (S & 3) * 2048 : (S & 1) * 4096);
      fa[S & 1][U] = ldtr(a, a + (GQA ? 32768 : 2048));
    };
    auto rdb = [&](auto S, auto T) { RDB(fb[T], ld128(lds + p0 + ((S >> 2) & 1) * 40960 + ((S & 3) * 5 + T) * 1024)); };
    unsigned ov = 0, ow = 0;
    auto piece = [&](auto G, auto I) {
      if constexpr (G < 16) dmap<true, I>(rV, vv, (G * 32 + I * 8) * 512);
      else dmap<false, I>(rO, ov, ((G - 16) * (GQA ? 16 : 32) + I * 8) * 896);
    };
    float rl[5];
    f32x4 lv[2];
    auto oput = [&](auto T, auto H) {
      const f32x4 u = acc[T][2 * H], v = acc[T][2 * H + 1];
      const float r = rl[T];
      *(LAS u32x4*)(lds + ow + H * 5120 + T * 1024) = u32x4{pk2(mul1(u[0], r), mul1(u[1], r)), pk2(mul1(u[2], r), mul1(u[3], r)),
                                                            pk2(mul1(v[0], r), mul1(v[1], r)), pk2(mul1(v[2], r), mul1(v[3], r))};
    };
    wait_vm_bar<GQA ? 6 : 4>();
    STAMP(ST + 5);
    sfor<0, 4>([&](auto U) { rda(IC<0>{}, U); });
    sfor<0, 5>([&](auto T) { rdb(IC<0>{}, T); });
    if constexpr (!GQA) { set_m0(ring); need(fa[0]); sfor<0, 4>([&](auto I) { piece(IC<2>{}, I); }); }
    sfor<0, 16>([&](auto S) {
      constexpr int s = S;
      SBAR();
      STAMP2(3, s);
      if constexpr (GQA) {
        if constexpr ((s & 3) == 3) wait_vm_bar<4>();
        else { need(fa[s & 1]); wait_vm_bar_raw<4>(); }
      } else {
        if constexpr (s == 15) bar();
        else { if constexpr ((s & 3) == 3) bar(); wait_vm<4>(); }
      }
      if constexpr (s == 8) {
        if (grown) {
          LAS float* em = (LAS float*)(lds + LDS_EXM + (hh * 16 + (lane_id() & 15)) * 48);
          sfor<0, 5>([&](auto T) {
            const float d = __builtin_fmaf(em[384 + T], SCL, mul1(em[T + 0], -SCL));
            const float a = d > GROW ? __builtin_amdgcn_exp2f(-d) : 1.f;
            sfor<0, 4>([&](auto U) { acc[T][U] *= a; });
          });
        }
      }
      if constexpr (s == 11) ov = voff_spl(lane_id(), 896);
      if constexpr (s == 15) ow = hh * 40960 + cq * 10240 + lane_id() * 16;
      sfor<0, 20>([&](auto I) {
        constexpr int i = I, t = i >> 2, u = i & 3;
        acc[t][u] = MFMA(fa[s & 1][u], fb[t], s == 0 ? ZERO4 : acc[t][u]);
        if constexpr (GQA) {
          if constexpr (i == 0) set_m0(ring + (s & 3) * 2048);
          if constexpr (i == 1 || i == 2) piece(IC<s + 4>{}, IC<i - 1>{});
          if constexpr (s < 15 && i >= 4 && i < 7) rda(IC<s + 1>{}, IC<i - 4>{});
          if constexpr (s < 15 && i == 8) rda(IC<s + 1>{}, IC<3>{});
        } else if constexpr (s < 15) {
          if constexpr (i < 4) rda(IC<s + 1>{}, IC<i & 3>{});
          if constexpr (i == 5) set_m0(ring + ((s + 1) & 1) * 4096);
          if constexpr (i == DM0) need(fa[(s + 1) & 1]);
          if constexpr (i >= DM0 && i < DM0 + 4) piece(IC<s + 3>{}, IC<(i - DM0) & 3>{});
        }
        if constexpr (s < 15 && u == 3) rdb(IC<s + 1>{}, IC<t>{});
        if constexpr (s < 10 && i >= 1 && i < 19) mop(IC<(180 + 18 * s + i - 1) % 360>{});
        if constexpr (s == 0 && i + (GQA ? PRE1 : 0) < 10) pput(IC<1>{}, IC<(i + (GQA ? PRE1 : 0)) % 10>{}, pw);
        if constexpr (s >= 3 && s <= 10 && (i == 5 || i == 10 || i == 14)) {
          constexpr int q4 = (s - 3) & 3, k = q4 * 3 - (q4 > 2) + (i == 5 ? 0 : i == 10 ? 1 : 2);
          if constexpr (k < 10 && !(q4 == 2 && i == 14)) pput(IC<s < 7 ? 2 : 3>{}, IC<k % 10>{}, pw);
        }
        if constexpr (s == 10 && (i == 3 || i == 9 || i == 15)) {
          const int l2 = lane_id();
          LAS float* el = (LAS float*)(lds + LDS_EXL + ((GQA ? 0 : hh) * 80 + (l2 & 15)) * 48 + (GQA ? w : cq) * 4);
          if constexpr (i == 3) el[(l2 >> 4) * 192] = red4<OpAdd>(ls[0], ls[1], ls[2], ls[3]);
          if constexpr (i == 9 && GQA) el[768 + ((l2 >> 4) & 1) * 960] = red2<OpAdd>(ls[4], ls[5 * MT - 1]);
          if constexpr (i == 9 && !GQA) el[768] = red2<OpAdd>(ls[4], ls[4]);
          if constexpr (i == 15 && GQA) el[960 + (l2 >> 4) * 192] = red4<OpAdd>(ls[5], ls[6], ls[7], ls[8]);
        }
        if constexpr (WARMNT == 2 && !LAST && s == 12 && i == 14)
          warm<true>(lds0, mk_rsrc(p.wo[P], 0, hh ? 0u : W_BYTES), 512 * j * 896 + (b >> 1) * 28672 + (cq * 64 + lane_id()) * 128);
        if constexpr ((s == 12 || s == 13) && (i % 8) == 1 && (s - 12) * 3 + i / 8 < 5) {
          lds_t e = lds + LDS_EXL + (hh * 80 + ((s - 12) * 3 + i / 8) * 16 + (lane_id() & 15)) * 48;
          lv[0] = *(LAS const f32x4*)e;
          if constexpr (GQA) lv[1] = *(LAS const f32x4*)(e + 16);
        }
        if constexpr ((s == 12 || s == 13) && i == (i / 8) * 8 + (i < 16 ? 6 : 3) && (s - 12) * 3 + i / 8 < 5) {
          float sum = ((lv[0][0] + lv[0][1]) + lv[0][2]) + lv[0][3];
          if constexpr (GQA) sum = (((sum + lv[1][0]) + lv[1][1]) + lv[1][2]) + lv[1][3];
          rl[((s - 12) * 3 + i / 8) % 5] = __builtin_amdgcn_rcpf(sum);
        }
        if constexpr (s == 15 && i >= 6 && (i & 1) == 0) oput(IC<(i - 6) / 4>{}, IC<((i - 6) / 2) % 2>{});
        SBAR();
      });
    });
    STAMP2(3, 16);
    STAMP(ST + 6);
    sfor<7, 10>([&](auto I) { oput(IC<I / 2>{}, IC<I % 2>{}); });
    if constexpr (LAST) wait_vm_bar<0>();
    else bar();
  }
  STAMP(ST + 7);

  {
    const rsrc_t rN = mk_rsrc(p.wq[1 - P], (512 * j + 64 * w) * 2, LAST ? 0u : W_BYTES - (512 * j + 64 * w) * 2);
    const int l = lane_id(), g = l >> 4, q = (l >> 2) & 3;
    const unsigned ov = voff_spl(l, 896), we = voff_nat(l, 8192);
    auto piece = [&](auto G, auto I) {
      if constexpr (G < 16) dmap<false, I>(rO, ov, (G * 32 + I * 8) * 896);
      else if constexpr (!LAST) dmap<false, I>(rN, we ^ ((I & 1) * 64), ((G - 16) * 32 + I * 8) * 8192);
    };
    if (w < 7) {
      const unsigned a0 = LDS_RING + w * 8192 + (4 * g + q) * 128 + (((q >> 1) | ((g & 1) << 1)) * 32) + 8 * (l & 3);
      auto rda = [&](auto S, auto U) { lds_t a = lds + (a0 ^ (U * 32)) + (S & 1) * 4096; fa[S & 1][U] = ldtr(a, a + 2048); };
      unsigned o1 = l * 16 + 40960;
      asm volatile("" : "+v"(o1));
      auto rdb = [&](auto S, auto T) { RDB(fb[T], ld128(lds + (S < 8 ? l * 16 : o1) + ((S & 7) * 5 + T) * 1024)); };
      const rsrc_t rS = mk_rsrc(p.slab, ((P * 32 + b) * 8 + j) * SLAB + w * 10240, 10240);
      auto sput = [&](auto T, auto H) {
        const f32x4 u = acc[T][2 * H], v = acc[T][2 * H + 1];
        auto r0 = __builtin_amdgcn_permlane16_swap(pk2(u[0], u[1]), pk2(v[0], v[1]), false, false);
        auto r1 = __builtin_amdgcn_permlane16_swap(pk2(u[2], u[3]), pk2(v[2], v[3]), false, false);
        __builtin_amdgcn_raw_buffer_store_b128(u32x4{r0[0], r1[0], r0[1], r1[1]}, rS, l * 16 + (T * 2 + H) * 1024, 0, 16  );
      };
      wait_vm<4>();
      sfor<0, 4>([&](auto U) { rda(IC<0>{}, U); });
      sfor<0, 5>([&](auto T) { rdb(IC<0>{}, T); });
      set_m0(ring);
      need(fa[0]);
      sfor<0, 4>([&](auto I) { piece(IC<2>{}, I); });
      sfor<0, 16>([&](auto S) {
        constexpr int s = S;
        SBAR();
        STAMP2(5, s);
        if constexpr (s < 15) { if constexpr (!(LAST && (ABL & 1))) wait_vm<(LAST && s == 14) ? 0 : 4>(); }
        else if constexpr (!LAST) { bar(); issue_x(p, 1 - P, b, w, lds0); }
        sfor<0, 20>([&](auto I) {
          constexpr int i = I, t = i >> 2, u = i & 3;
          acc[t][u] = MFMA(fa[s & 1][u], fb[t], s == 0 ? ZERO4 : acc[t][u]);
          if constexpr (s < 15) {
            if constexpr (!(LAST && (ABL & 4) && s > 0)) {
            if constexpr (i < 4) rda(IC<s + 1>{}, IC<i & 3>{});
            }
            if constexpr (!(LAST && (ABL & 8)))
            if constexpr (u == 3) rdb(IC<s + 1>{}, IC<t>{});
            if constexpr (!(LAST && (s >= 13 || (ABL & 2)))) {
              if constexpr (i == 5) set_m0(ring + ((s + 1) & 1) * 4096);
              if constexpr (i == DM0) need(fa[(s + 1) & 1]);
              if constexpr (i >= DM0 && i < DM0 + 4) piece(IC<s + 3>{}, IC<(i - DM0) & 3>{});
            }
          } else if constexpr (i >= 6 && (i & 1) == 0) sput(IC<(i - 6) / 4>{}, IC<((i - 6) / 2) % 2>{});
          SBAR();
        });
      });
      STAMP2(5, 16);
      STAMP(ST + 8);
      sfor<7, 10>([&](auto I) { sput(IC<I / 2>{}, IC<I % 2>{}); });
      if constexpr (!LAST) wait_vm_bar<10>();
    } else if constexpr (!LAST) {
      wait_vm<0>();
      set_m0(ring);
      sfor<0, 4>([&](auto I) { piece(IC<16>{}, I); });
      set_m0(ring + 4096);
      sfor<0, 4>([&](auto I) { piece(IC<17>{}, I); });
      if constexpr (WARMNT) sfor<0, 4>([&](auto I) { warm<true>(lds0, mk_rsrc(p.wq[1 - P], 0, W_BYTES), ((b >> 1) * 28 + I * 8 + (l >> 3)) * 8192 + 1024 * j + (l & 7) * 128); });
      if constexpr (XW == 1) sfor<0, 9>([&](auto I) { warm(lds0, mk_rsrc(p.x[1 - P], b * X_BYTES, X_BYTES), (I * 64 + l) * 128); });
      bar();
      issue_x(p, 1 - P, b, w, lds0);
      wait_vm_bar<0>();
    } else {
      arrive<1>(p, lds, b, j);
      wait_all<1>(p, lds, b);
    }
  }
  STAMP(ST + 9);
}

__global__ __launch_bounds__(512, 2) void fused_attn_kernel(Params p) {
  extern __shared__ __attribute__((aligned(16))) char smem[];
  lds_t lds = (lds_t)smem;
  const unsigned lds0 = (unsigned)(uintptr_t)lds;
  const int w = __builtin_amdgcn_readfirstlane(threadIdx.x >> 6);
  const int j = blockIdx.x & 7, b = blockIdx.x >> 3;
  const int first = b & 1;
  STAMP(0);
  {
    unsigned found = 0;
    if (w == 7) found = __hip_atomic_load(flag_word(p, b, lane_id() & 7), RLX);
    asm volatile("" ::: "memory");
    issue_x(p, first, b, w, lds0);
    const int l = lane_id();
    const rsrc_t rW = mk_rsrc(p.wq[first], (512 * j + 64 * w) * 2, W_BYTES - (512 * j + 64 * w) * 2);
    const unsigned we = voff_nat(l, 8192), ring = lds0 + LDS_RING + w * 8192;
    sfor<0, 2>([&](auto G) {
      set_m0(ring + G * 4096);
      sfor<0, 4>([&](auto I) { dmap<false, I>(rW, we ^ ((I & 1) * 64), (G * 32 + I * 8) * 8192); });
    });
    const unsigned id = (w & 3) * 64 + l;
    if (w >= 4 || WARMNT == 2) warm<WARMNT != 0>(lds0, mk_rsrc(p.wq[first], 0, W_BYTES), ((b >> 1) * 28 + (id >> 3)) * 8192 + 1024 * j + (id & 7) * 128);
    else warm(lds0, mk_rsrc(p.wo[first], 0, WOL ? 0u : W_BYTES), 512 * j * 896 + (b >> 1) * 28672 + id * 128);
    warm(lds0, mk_rsrc(p.x[1 - first], b * X_BYTES, X_BYTES), (XW == 2 && l < 9) ? (j * 70 + w * 9 + l) * 128 : X_BYTES);
    wait_vm_bar<10>();
    if (w == 7) {
      *(LAS unsigned*)(lds + LDS_OLD + (l & 7) * 4) = found;
      if (l < 8) __hip_atomic_store(ack_word(p, b, l, j), found + 1u, RLX);
    }
  }
  STAMP(10);
  if (first == 0) {
    run_problem<0, false, 0>(p, lds, lds0, b, j, w);
    run_problem<1, true, 10>(p, lds, lds0, b, j, w);
  } else {
    run_problem<1, false, 0>(p, lds, lds0, b, j, w);
    run_problem<0, true, 10>(p, lds, lds0, b, j, w);
  }
  wait_vm<0>();
  STAMP(21);
  bar();
  STAMP(22);
  if (w == 7) arrive<2>(p, lds, b, j);
  reduce_rows<2>(p, first, b, j, w, 8);
  if (w == 7) wait_all<2>(p, lds, b);
  bar();
  STAMP(23);
  reduce_rows<2>(p, 1 - first, b, j, w, 8);
  STAMP(20);
}

extern "C" void kernel_launch(void* const* d_in, const int* in_sizes, int n_in, void* d_out, int out_size, void* d_ws,
                              size_t ws_size, hipStream_t stream) {
  Params p;
  for (int i = 0; i < 2; ++i) {
    p.x[i] = d_in[5 * i + 0]; p.k[i] = d_in[5 * i + 1]; p.v[i] = d_in[5 * i + 2]; p.wq[i] = d_in[5 * i + 3]; p.wo[i] = d_in[5 * i + 4];
  }
  p.sync = (unsigned*)d_ws;
  p.slab = (char*)d_ws + 262144;
  p.out = d_out;
  p.diag = (unsigned long long*)((char*)d_ws + (64u << 20));
  (void)hipFuncSetAttribute((const void*)fused_attn_kernel, hipFuncAttributeMaxDynamicSharedMemorySize, LDS_TOTAL);
  fused_attn_kernel<<<256, 512, LDS_TOTAL, stream>>>(p);
}
```

```cpp
#include <hip/hip_runtime.h>
#include <hip/hip_bf16.h>
#include <stdint.h>
#include <type_traits>

typedef short bf16x8 __attribute__((ext_vector_type(8)));
typedef short s16x4 __attribute__((ext_vector_type(4)));
typedef float f32x4 __attribute__((ext_vector_type(4)));
typedef float f32x2 __attribute__((ext_vector_type(2)));
typedef __bf16 bf16x2 __attribute__((ext_vector_type(2)));
typedef unsigned u32x4 __attribute__((ext_vector_type(4)));
typedef unsigned u32x2 __attribute__((ext_vector_type(2)));
#define LAS __attribute__((address_space(3)))
typedef __amdgpu_buffer_rsrc_t rsrc_t;
typedef LAS char* lds_t;

#define DIAG 0
#define DLAST 1
#define PO 12
#define FAKEKV 0

constexpr int LDS_RING = 81920;
constexpr int LDS_EXM = 147456;
constexpr int LDS_EXL = 151552;
constexpr int LDS_DUMMY = 159232;
constexpr int LDS_OLD = 159488;
constexpr int LDS_TOTAL = DIAG ? 163840 : 159552;
constexpr unsigned X_BYTES = 80 * 448 * 2;
constexpr unsigned W_BYTES = 448 * 4096 * 2;
constexpr unsigned KV_HEAD = 512 * 256 * 2;
constexpr unsigned SLAB = 72 * 1024;
constexpr float SCL = 0.0625f * 1.4426950408889634f;
constexpr float GROW = 20.f;

template <int I, int N, class F>
__device__ __forceinline__ void sfor(F&& f) {
  if constexpr (I < N) { f(std::integral_constant<int, I>{}); sfor<I + 1, N>(f); }
}
#define SBAR() __builtin_amdgcn_sched_barrier(0)
#define MFMA(a, b, c) __builtin_amdgcn_mfma_f32_16x16x32_bf16(a, b, c, 0, 0, 0)
template <int N> __device__ __forceinline__ void wait_vm() { asm volatile("s_waitcnt vmcnt(%0)" ::"i"(N) : "memory"); }
__device__ __forceinline__ void bar() { asm volatile("s_waitcnt lgkmcnt(0)\n\ts_barrier" ::: "memory"); }
template <int N> __device__ __forceinline__ void wait_vm_bar_raw() { asm volatile("s_waitcnt vmcnt(%0)\n\ts_barrier" ::"i"(N) : "memory"); }
template <int N> __device__ __forceinline__ void wait_vm_bar() { asm volatile("s_waitcnt vmcnt(%0) lgkmcnt(0)\n\ts_barrier" ::"i"(N) : "memory"); }
__device__ __forceinline__ void prio_reset() { asm volatile("s_setprio 1"); }
__device__ __forceinline__ void prio_yield(int hh) { asm volatile("s_cmp_lg_u32 %0, 0\n\ts_cbranch_scc1 1f\n\ts_setprio 0\n1:" ::"s"(hh) : "scc"); }
__device__ __forceinline__ int lane_id() {
  int l; asm volatile("v_mbcnt_lo_u32_b32 %0, -1, 0\n\tv_mbcnt_hi_u32_b32 %0, -1, %0" : "=v"(l)); return l;
}
__device__ __forceinline__ int fresh(int x) { asm volatile("" : "+s"(x)); return x; }
__device__ __forceinline__ unsigned pk2(float a, float b) {
  return __builtin_bit_cast(unsigned, __builtin_convertvector(f32x2{a, b}, bf16x2));
}
__device__ __forceinline__ rsrc_t mk_rsrc(const void* p, unsigned off, unsigned bytes) {
  return __builtin_amdgcn_make_buffer_rsrc((void*)((const char*)p + off), (short)0, (int)bytes, 0x00020000);
}
__device__ __forceinline__ void need(const bf16x8 (&f)[4]) { asm volatile("" ::"v"(f[0]), "v"(f[1]), "v"(f[2]), "v"(f[3]) : "memory"); }
__device__ __forceinline__ void need3(const bf16x8 (&f)[4]) { asm volatile("" ::"v"(f[0]), "v"(f[1]), "v"(f[2]) : "memory"); }
__device__ __forceinline__ void set_m0(unsigned m0v) { asm volatile("s_mov_b32 m0, %0\n\ts_nop 0" ::"s"(m0v) : "memory"); }
template <bool NT, int I>
__device__ __forceinline__ void dmap(rsrc_t r, unsigned v, unsigned src) {
  if constexpr (NT && !FAKEKV) asm volatile("buffer_load_dwordx4 %0, %1, %2 offen offset:%3 nt lds" ::"v"(v), "s"(r), "s"(src - 1024u * I), "i"(1024 * I) : "memory");
  else asm volatile("buffer_load_dwordx4 %0, %1, %2 offen offset:%3 lds" ::"v"(v), "s"(r), "s"(src - 1024u * I), "i"(1024 * I) : "memory");
}
__device__ __forceinline__ void warm(unsigned lds0, rsrc_t r, unsigned v) {
  asm volatile("s_mov_b32 m0, %0\n\ts_nop 0\n\tbuffer_load_dword %1, %2, 0 offen lds" ::"s"(lds0 + LDS_DUMMY), "v"(v), "s"(r) : "memory");
}
extern "C" __device__ float llvm_fmul_legacy(float, float) __asm("llvm.amdgcn.fmul.legacy");
__device__ __forceinline__ float mul1(float a, float b) { return llvm_fmul_legacy(a, b); }
__device__ __forceinline__ bf16x8 ld128(lds_t p) { return *(LAS const bf16x8*)p; }
__device__ __forceinline__ bf16x8 ldtr(lds_t p0, lds_t p1) {
  s16x4 lo = __builtin_amdgcn_ds_read_tr16_b64_v4i16((LAS s16x4*)p0);
  s16x4 hi = __builtin_amdgcn_ds_read_tr16_b64_v4i16((LAS s16x4*)p1);
  return __builtin_shufflevector(lo, hi, 0, 1, 2, 3, 4, 5, 6, 7);
}
__device__ __forceinline__ float max3(float a, float b, float c) { return __builtin_fmaxf(__builtin_fmaxf(a, b), c); }
struct OpMax { static __device__ __forceinline__ float f(float a, float b) { return max3(a, b, b); } };
struct OpAdd { static __device__ __forceinline__ float f(float a, float b) { return a + b; } };
template <class Op> __device__ __forceinline__ float sw16(float a, float b) {
  auto r = __builtin_amdgcn_permlane16_swap(__float_as_uint(a), __float_as_uint(b), false, false);
  return Op::f(__uint_as_float(r[0]), __uint_as_float(r[1]));
}
template <class Op> __device__ __forceinline__ float sw32(float a, float b) {
  auto r = __builtin_amdgcn_permlane32_swap(__float_as_uint(a), __float_as_uint(b), false, false);
  return Op::f(__uint_as_float(r[0]), __uint_as_float(r[1]));
}
template <class Op> __device__ __forceinline__ float red4(float a, float b, float c, float d) { return sw32<Op>(sw16<Op>(a, b), sw16<Op>(c, d)); }
template <class Op> __device__ __forceinline__ float red2(float a, float b) { const float x = sw16<Op>(a, b); return sw32<Op>(x, x); }

__device__ __forceinline__ unsigned voff_nat(int l, unsigned pitch) { return (l >> 3) * pitch + 16 * ((l & 7) ^ (((l >> 4) & 1) << 1)); }
__device__ __forceinline__ unsigned voff_spl(int l, unsigned pitch) { return (l >> 3) * pitch + 16 * ((l & 7) ^ (((l >> 4) & 3) << 1)); }
__device__ __forceinline__ unsigned voff_k(int l) { return (l >> 3) * 512 + 16 * ((l & 7) ^ ((l >> 4) & 3)); }

struct Params {
  const void *x[2], *k[2], *v[2], *wq[2], *wo[2];
  void* slab;
  unsigned* sync;
  void* out;
  unsigned long long* diag;
};

#define STAMP(i)
#define STAMP2(ph, i)

constexpr int DM0 = 7;
template <int V> using IC = std::integral_constant<int, V>;
constexpr f32x4 ZERO4 = {0.f, 0.f, 0.f, 0.f};

__device__ __forceinline__ void issue_x(const Params& p, int P, int b, int w, unsigned lds0) {
  w = fresh(w);
  const int l = lane_id();
  const unsigned v = (l >> 3) * 896 + 16 * ((l & 7) ^ ((l >> 4) & 3));
  const rsrc_t rx = mk_rsrc(p.x[P], b * X_BYTES, X_BYTES);
  sfor<0, 9>([&](auto I) {
    const unsigned pc = w * 9 + I, blk = (pc * 205) >> 11, rg = pc - blk * 10;
    set_m0(lds0 + pc * 1024);
    dmap<false, 0>(rx, v ^ ((rg & 1) * 64), rg * 7168 + blk * 128);
  });
}

typedef __attribute__((address_space(1))) unsigned gu32;
#define RLX __ATOMIC_RELAXED, __HIP_MEMORY_SCOPE_AGENT
__device__ __forceinline__ gu32* flag_word(const Params& p, int b, int j) { return (gu32*)p.sync + (b * 8 + j) * 32; }
__device__ __forceinline__ gu32* ack_word(const Params& p, int b, int prod, int cons) { return (gu32*)p.sync + 8192 + ((b * 8 + prod) * 8 + cons) * 16; }
template <int STAGE>
__device__ __forceinline__ void arrive(const Params& p, lds_t lds, int b, int j) {
  const unsigned old = *(LAS const unsigned*)(lds + LDS_OLD + j * 4);
  if (lane_id() == 0) __hip_atomic_store(flag_word(p, b, j), old + STAGE, RLX);
}
template <int STAGE> __device__ __forceinline__ gu32* sync_word(const Params& p, int b, int j) {
  const int c = lane_id() & 7;
  return STAGE ? flag_word(p, b, c) : ack_word(p, b, j, c);
}
template <int STAGE>
__device__ __forceinline__ void settle(const Params& p, lds_t lds, int b, int j, unsigned seen) {
  const unsigned old = *(LAS const unsigned*)(lds + LDS_OLD + (STAGE ? lane_id() & 7 : j) * 4);
  for (unsigned spins = 0; spins < (1u << 22); ++spins) {
    if (__builtin_amdgcn_ballot_w64(STAGE ? seen - old - STAGE > 2u - STAGE : seen != old + 1u) == 0) break;
    __builtin_amdgcn_s_sleep(2);
    seen = __hip_atomic_load(sync_word<STAGE>(p, b, j), RLX);
  }
  asm volatile("" ::: "memory");
}
struct RedRows { u32x4 v[2][8]; };
template <int K, int J>
__device__ __forceinline__ void red_load(RedRows& r, const Params& p, int P, int b, int j, int w) {
  const rsrc_t rS = mk_rsrc(p.slab, (P * 32 + b) * 8 * SLAB, 8 * SLAB);
  r.v[K][J] = __builtin_amdgcn_raw_buffer_load_b128(rS, lane_id() * 16, J * SLAB + (9 * j + w + K) * 1024, 16  );
}
__device__ __forceinline__ void red_store(const RedRows& r, const Params& p, int P, int b, int j, int w) {
  const int l = lane_id(), n = l & 15, g = l >> 4;
  sfor<0, 2>([&](auto K) {
    if (K == 0 || w == 7) {
      float sm[8];
      sfor<0, 4>([&](auto I) { sm[2 * I] = __uint_as_float(r.v[K][0][I + 0] << 16); sm[2 * I + 1] = __uint_as_float(r.v[K][0][I + 0] & 0xffff0000u); });
      sfor<1, 8>([&](auto J) {
        sfor<0, 4>([&](auto I) { sm[2 * I] += __uint_as_float(r.v[K][J][I + 0] << 16); sm[2 * I + 1] += __uint_as_float(r.v[K][J][I + 0] & 0xffff0000u); });
      });
      const int f = 9 * j + w + K;
      int t, col;
      if (f < 40) { const int ww = f / 10; t = (f - ww * 10) >> 1; col = 64 * ww + 32 * (f & 1) + 16 * (g & 1); }
      else { const int rr = f & 7, c0 = 256 + 48 * ((f - 40) >> 3); t = rr < 5 ? rr : 2 * (rr - 5) + (g & 1); col = c0 + (rr < 5 ? 16 * (g & 1) : 32); }
      if (t < 5)
        *(u32x4*)((char*)p.out + ((size_t)(b * 80 + t * 16 + n) * 896 + P * 448 + col + 8 * (g >> 1)) * 2) =
            u32x4{pk2(sm[0], sm[1]), pk2(sm[2], sm[3]), pk2(sm[4], sm[5]), pk2(sm[6], sm[7])};
    }
  });
}

template <int P  , bool LAST, int ST>
__device__ __forceinline__ void run_problem(const Params& p, lds_t lds, unsigned lds0, int b, int j, int w) {
  constexpr bool GQA = (P == 0);
  const int bk = FAKEKV ? 0 : b;
  const int hh = w >> 2, cq = w & 3;
  const unsigned ring = lds0 + LDS_RING + w * 8192;
  f32x4 acc[5][4];
  bf16x8 fa[2][4], fb[5];

  const unsigned koff = GQA ? (bk * 8 + j) * KV_HEAD + w * 16 * 512 : (bk * 16 + 2 * j + hh) * KV_HEAD + cq * 32 * 512;
  const rsrc_t rK = mk_rsrc(p.k[P], koff, KV_HEAD - (GQA ? w * 16 * 512 : cq * 32 * 512));
  auto k_src = [](int g, int i) -> unsigned {
    return GQA ? ((g >> 2) * 256 + (i >> 1) * 128 + (i & 1) * 8) * 512 + (g & 3) * 128 : ((g >> 2) * 128 + i * 8) * 512 + (g & 3) * 128;
  };

  {
    const rsrc_t rW = mk_rsrc(p.wq[P], (512 * j + 64 * w) * 2, W_BYTES - (512 * j + 64 * w) * 2);
    const int l = lane_id(), n = l & 15, g = l >> 4, q = (l >> 2) & 3;
    const unsigned we = voff_nat(l, 8192), ke = voff_k(l);
    const unsigned a0 = LDS_RING + w * 8192 + (8 * g + q) * 128 + (((q >> 1) | ((g & 1) << 1)) * 32) + 8 * (l & 3);
    unsigned x0[2][2];
    x0[0][0] = n * 128 + 16 * (g ^ ((n >> 1) & 7)); x0[1][0] = x0[0][0] ^ 64; x0[0][1] = x0[0][0] + 40960; x0[1][1] = x0[1][0] + 40960;
    asm volatile("" : "+v"(x0[0][1]), "+v"(x0[1][1]));
    const unsigned qw = hh * 40960 + cq * 10240 + l * 16;
    auto rda = [&](auto S, auto U) { lds_t a = lds + (a0 ^ (U * 32)) + (S & 1) * 4096; fa[S & 1][U] = ldtr(a, a + 512); };
    auto rdb = [&](auto S, auto T) { fb[T] = ld128(lds + x0[S & 1][S >= 8] + ((S >> 1) & 3) * 10240 + T * 2048); };
    auto piece = [&](auto G, auto I) {
      if constexpr (G < 14) dmap<false, I>(rW, we ^ ((I & 1) * 64), (G * 32 + I * 8) * 8192);
      else dmap<true, I>(rK, ke ^ ((I & 1) * 64), k_src(G - 14, I));
    };
    auto qput = [&](auto T, auto H) {
      const f32x4 u = acc[T][2 * H], v = acc[T][2 * H + 1];
      *(LAS u32x4*)(lds + qw + H * 5120 + T * 1024) = u32x4{pk2(u[0], u[1]), pk2(u[2], u[3]), pk2(v[0], v[1]), pk2(v[2], v[3])};
    };
    if constexpr (!LAST) wait_vm<5>();
    sfor<0, 4>([&](auto U) { rda(IC<0>{}, U); });
    sfor<0, 5>([&](auto T) { rdb(IC<0>{}, T); });
    set_m0(ring);
    need(fa[0]);
    sfor<0, 4>([&](auto I) { piece(IC<2>{}, I); });
    sfor<0, 14>([&](auto S) {
      constexpr int s = S;
      SBAR();
      STAMP2(4, s);
      if constexpr (s == 0) prio_reset();
      if constexpr (s == 9) prio_yield(hh);
      if constexpr (s == 0) { if constexpr (!LAST) wait_vm<5>(); }
      else if constexpr (s < 13) wait_vm<(LAST && s == 6) ? 0 : 4>();
      else {
        bar();
        *(LAS f32x2*)(lds + LDS_EXM + w * 512 + l * 8) = f32x2{-INFINITY, -INFINITY};
        if constexpr (LAST) { if (w == 0) arrive<1>(p, lds, b, j); }
      }
      sfor<0, 20>([&](auto I) {
        constexpr int i = I, t = i >> 2, u = i & 3;
        acc[t][u] = MFMA(fa[s & 1][u], fb[t], s == 0 ? ZERO4 : acc[t][u]);
        if constexpr (s < 13) {
          if constexpr (i < 4) rda(IC<s + 1>{}, IC<i & 3>{});
          if constexpr (u == 3) rdb(IC<s + 1>{}, IC<t>{});
          if constexpr (i == 5) set_m0(ring + ((s + 1) & 1) * 4096);
          if constexpr (i == DM0) need(fa[(s + 1) & 1]);
          if constexpr (i >= DM0 && i < DM0 + 4) piece(IC<s + 3>{}, IC<(i - DM0) & 3>{});
        } else if constexpr (i >= 6 && (i & 1) == 0) qput(IC<(i - 6) / 4>{}, IC<((i - 6) / 2) % 2>{});
        SBAR();
      });
    });
    STAMP2(4, 14);
    STAMP(ST + 1);
    sfor<7, 10>([&](auto I) { qput(IC<I / 2>{}, IC<I % 2>{}); });
    bar();
  }
  STAMP(ST + 2);

  const rsrc_t rV = GQA ? mk_rsrc(p.v[P], (bk * 8 + j) * KV_HEAD + hh * 16 * 512 + cq * 128, KV_HEAD - (hh * 16 * 512 + cq * 128))
                        : mk_rsrc(p.v[P], (bk * 16 + 2 * j + hh) * KV_HEAD + cq * 128, KV_HEAD - cq * 128);
  constexpr int MT = GQA ? 2 : 1;
  constexpr int NG = GQA ? 8 : 16;
  f32x4 sc[4][10];
  float mx[5 * MT], nm[5 * MT], ls[5 * MT], ev[4];
  u32x2 pq[4][10];
  auto mop = [&](auto ID) {
    constexpr int id = ID, kq = id / 90, k = (id / 9) % 10, part = id % 9, r = GQA ? k : (k >> 1);
    if constexpr (part == 0) { ev[0] = __builtin_fmaf(sc[kq][k][0], SCL, nm[r]); ev[1] = __builtin_fmaf(sc[kq][k][1], SCL, nm[r]); }
    if constexpr (part == 1) ev[0] = __builtin_amdgcn_exp2f(ev[0]);
    if constexpr (part == 2) { ev[2] = __builtin_fmaf(sc[kq][k][2], SCL, nm[r]); ev[3] = __builtin_fmaf(sc[kq][k][3], SCL, nm[r]); }
    if constexpr (part == 3) ev[1] = __builtin_amdgcn_exp2f(ev[1]);
    if constexpr (part == 4) ev[2] = __builtin_amdgcn_exp2f(ev[2]);
    if constexpr (part == 5) { if constexpr (kq == 0 && (GQA || (k & 1) == 0)) ls[r] = ev[0] + ev[1]; else ls[r] = (ls[r] + ev[0]) + ev[1]; }
    if constexpr (part == 6) ev[3] = __builtin_amdgcn_exp2f(ev[3]);
    if constexpr (part == 7) { pq[kq][k][0] = pk2(ev[0], ev[1]); ls[r] += ev[2]; }
    if constexpr (part == 8) { pq[kq][k][1] = pk2(ev[2], ev[3]); ls[r] += ev[3]; }
    if constexpr (part == 0) asm volatile("" : "+v"(ev[0]), "+v"(ev[1]));
    if constexpr (part == 1) asm volatile("" : "+v"(ev[0]));
    if constexpr (part == 2) asm volatile("" : "+v"(ev[2]), "+v"(ev[3]));
    if constexpr (part == 3) asm volatile("" : "+v"(ev[1]));
    if constexpr (part == 4) asm volatile("" : "+v"(ev[2]));
    if constexpr (part == 5) asm volatile("" : "+v"(ls[r]));
    if constexpr (part == 6) asm volatile("" : "+v"(ev[3]));
    if constexpr (part == 7) asm volatile("" : "+v"(ls[r]), "+v"(pq[kq][k][0]));
    if constexpr (part == 8) asm volatile("" : "+v"(ls[r]), "+v"(pq[kq][k][1]));
  };
  auto pput = [&](auto KQ, auto K, unsigned pw) {
    constexpr int kq = KQ, k = K;
    if constexpr (GQA) *(LAS u32x2*)(lds + pw + (kq & 1) * 40960 + (k / 5) * 20480 + (k % 5) * 1024) = pq[kq][k];
    else if constexpr (k & 1) *(LAS u32x4*)(lds + pw + (kq & 1) * 40960 + (k / 2) * 1024) = u32x4{pq[kq][k - 1][0], pq[kq][k - 1][1], pq[kq][k][0], pq[kq][k][1]};
  };
  bool grown;
  unsigned pw;
  {
    const int l = lane_id(), n = l & 15, g = l >> 4;
    const unsigned ke = voff_k(l);
    unsigned vv = 0;
    const unsigned k0 = LDS_RING + w * 8192 + n * 128 + 16 * ((g >> 1) ^ ((n >> 1) & 7)) + 8 * (g & 1);
    const unsigned q0 = (GQA ? 0 : hh * 40960) + l * 16;
    unsigned q1 = q0 + 40960;
    asm volatile("" : "+v"(q1));
    constexpr int NSUB = 2 * MT;
    auto rdk = [&](auto G, auto I) {
      lds_t a = lds + (I & 1) * 2048 + (G & 1) * 4096;
      const s16x4 lo = *(LAS const s16x4*)(a + (k0 ^ ((I >> 1) * 64))), hi = *(LAS const s16x4*)(a + (k0 ^ ((I >> 1) * 64 + 32)));
      fa[G & 1][I] = __builtin_shufflevector(lo, hi, 0, 1, 2, 3, 4, 5, 6, 7);
    };
    auto rdq = [&](auto Z, auto T) {
      constexpr int z = Z, gi = z / NSUB, e = (z % NSUB) / MT, h = z % MT;
      fb[T] = ld128(lds + (h ? q1 : q0) + ((2 * (gi & 3) + e) * 5 + T) * 1024);
    };
    auto piece = [&](auto G, auto I) {
      if constexpr (G < NG) dmap<true, I>(rK, ke ^ ((I & 1) * 64), k_src(G, I));
      else if constexpr (!GQA) dmap<true, I>(rV, vv, ((G - NG) * 32 + I * 8) * 512);
      else dmap<true, I>(rV, vv, ((2 * (G - NG) + (I >> 1)) * 32 + (I & 1) * 8) * 512);
    };
    auto qmax = [&](auto KQ, auto K) {
      constexpr int kq = KQ, k = K, r = GQA ? k : (k >> 1);
      if constexpr ((kq & 1) == 0 && (GQA || (k & 1) == 0)) mx[r] = max3(max3(sc[kq][k][0], sc[kq][k][1], sc[kq][k][2]), sc[kq][k][3], sc[kq][k][3]);
      else mx[r] = max3(max3(mx[r], sc[kq][k][0], sc[kq][k][1]), sc[kq][k][2], sc[kq][k][3]);
      asm volatile("" : "+v"(mx[r]));
    };
    auto xmax = [&](auto HALF, auto C) {
      const int l2 = lane_id(), g2 = l2 >> 4;
      LAS float* em = (LAS float*)(lds + LDS_EXM + HALF * 1536 + ((GQA ? 0 : hh) * 16 + (l2 & 15)) * 48);
      if constexpr (C == 0) __builtin_amdgcn_ds_fmaxf(em + g2, red4<OpMax>(mx[0], mx[1], mx[2], mx[3]), 0, 0, false);
      if constexpr (C == 1 && GQA) __builtin_amdgcn_ds_fmaxf(em + (g2 & 1) * 192 + 4, red2<OpMax>(mx[4], mx[5 * MT - 1]), 0, 0, false);
      if constexpr (C == 1 && !GQA) __builtin_amdgcn_ds_fmaxf(em + 4, red2<OpMax>(mx[4], mx[4]), 0, 0, false);
      if constexpr (C == 2 && GQA) __builtin_amdgcn_ds_fmaxf(em + 192 + g2, red4<OpMax>(mx[5], mx[6], mx[7], mx[8]), 0, 0, false);
    };
    auto rmax = [&](auto HALF) {
      LAS float* em = (LAS float*)(lds + LDS_EXM + HALF * 1536 + ((GQA ? 0 : hh) * 16 + (lane_id() & 15)) * 48);
      sfor<0, MT>([&](auto H) {
        const f32x4 v = *(LAS const f32x4*)(em + H * 192);
        sfor<0, 4>([&](auto T) { mx[H * 5 + T] = v[T + 0]; });
        mx[H * 5 + 4] = em[H * 192 + 4];
      });
    };
    wait_vm<4>();
    sfor<0, 4>([&](auto I) { rdk(IC<0>{}, I); });
    sfor<0, 5>([&](auto T) { rdq(IC<0>{}, T); });
    set_m0(ring);
    need(fa[0]);
    sfor<0, 4>([&](auto I) { piece(IC<2>{}, I); });
    sfor<0, 32>([&](auto Z) {
      constexpr int z = Z, gi = z / NSUB, sub = z % NSUB, e = sub / MT, h = sub % MT;
      SBAR();
      STAMP2(2, z);
      if constexpr (z == 0 || z == 20) prio_reset();
      if constexpr (z == 13 || z == 28) prio_yield(hh);
      if constexpr (z == 19) { bar(); rmax(IC<0>{}); }
      if constexpr (z == (NG - 3) * NSUB) vv = voff_spl(lane_id(), 512);
      if constexpr (sub == 0 && gi + 1 < NG) wait_vm<4>();
      sfor<0, 10>([&](auto I) {
        constexpr int i = I, t = i >> 1, u = i & 1;
        constexpr int kq = GQA ? 2 * (gi >> 2) + u : (gi >> 2), k = GQA ? 5 * h + t : i;
        sc[kq][k] = MFMA(fa[gi & 1][2 * e + u], fb[t], ((gi & 3) == 0 && e == 0) ? ZERO4 : sc[kq][k]);
        if constexpr (u == 1 && z + 1 < 32) rdq(IC<z + 1>{}, IC<t>{});
        if constexpr (gi + 1 < NG) {
          if constexpr (sub == 0 && i < 4) rdk(IC<gi + 1>{}, IC<i & 3>{});
          if constexpr (sub == 0 && i == 5) set_m0(ring + ((gi + 1) & 1) * 4096);
          if constexpr (sub == 0 && i == 6) need(fa[(gi + 1) & 1]);
          if constexpr (sub == 0 && i >= 6) piece(IC<gi + 3>{}, IC<(i - 6) & 3>{});
        }
        if constexpr (z == 16 || z == 17) qmax(IC<z - 16>{}, I);
        if constexpr (z == 18 && (i == 1 || i == 4 || i == 7)) xmax(IC<0>{}, IC<i / 3>{});
        if constexpr (z == 19 && i >= 5) sfor<0, MT>([&](auto H) { nm[H * 5 + i - 5] = mul1(mx[H * 5 + i - 5], -SCL); });
        if constexpr (z >= 20) { constexpr int x = (z - 20) * 10 + i; sfor<(3 * x + 1) / 2, (3 * x + 4) / 2>(mop); }
        if constexpr (!GQA && z == 26) qmax(IC<2>{}, I);
        SBAR();
      });
    });
    STAMP2(2, 32);
    STAMP(ST + 3);
    sfor<4 - MT, 4>([&](auto KQ) { sfor<0, 10>([&](auto K) { qmax(KQ, K); }); });
    sfor<0, 3>([&](auto C) { xmax(IC<1>{}, C); });
    pw = (GQA ? (w >> 1) * 5120 + (w & 1) * 8 : hh * 20480 + cq * 5120) + lane_id() * 16;
    bar();
    rmax(IC<1>{});
    sfor<0, 10>([&](auto K) { pput(IC<0>{}, K, pw); });
    sfor<0, GQA ? 6 : 0>([&](auto K) { pput(IC<1>{}, K, pw); });
    bool any = false;
    sfor<0, 5 * MT>([&](auto R) { any |= __builtin_fmaf(mx[R], SCL, nm[R]) > GROW; });
    grown = __builtin_amdgcn_ballot_w64(any) != 0;
    if (grown) {
      sfor<0, 5 * MT>([&](auto R) {
        const float d = __builtin_fmaf(mx[R], SCL, nm[R]);
        if (d > GROW) { ls[R] *= __builtin_amdgcn_exp2f(-d); nm[R] = -SCL * mx[R]; }
      });
    }
  }
  STAMP(ST + 4);

  const unsigned wwin = hh ? 512 + (cq * 96 & ~63) : w * 128;
  const rsrc_t rO = mk_rsrc(p.wo[P], 512 * j * 896 + wwin, W_BYTES - (512 * j * 896 + wwin));
  {
    const int l = lane_id(), g = l >> 4, q = (l >> 2) & 3;
    const unsigned vv = voff_spl(l, 512);
    const unsigned a0 = LDS_RING + (GQA ? cq : w) * 8192 + (4 * g + q) * 128 + (((q >> 1) | ((g & 1) << 1)) * 32) + 8 * (l & 3);
    const unsigned p0 = hh * 20480 + l * 16;
    auto rda = [&](auto S, auto U) {
      lds_t a = lds + (a0 ^ (U * 32)) + (GQA ? (S & 3) * 2048 : (S & 1) * 4096);
      fa[S & 1][U] = ldtr(a, a + (GQA ? 32768 : 2048));
    };
    auto rdb = [&](auto S, auto T) { fb[T] = ld128(lds + p0 + ((S >> 2) & 1) * 40960 + ((S & 3) * 5 + T) * 1024); };
    unsigned ov = 0, ow = 0;
    auto piece = [&](auto G, auto I) {
      if constexpr (G < 16) dmap<true, I>(rV, vv, (G * 32 + I * 8) * 512);
      else dmap<false, I>(rO, ov, ((G - 16) * (GQA ? 16 : 32) + I * 8) * 896);
    };
    constexpr auto TW = [](int s) -> int { return s == 12; };
    unsigned seen = 0;
    float rl[5];
    f32x4 lv[2];
    auto oput = [&](auto T, auto H) {
      const f32x4 u = acc[T][2 * H], v = acc[T][2 * H + 1];
      const float r = rl[T];
      *(LAS u32x4*)(lds + ow + H * 5120 + T * 1024) = u32x4{pk2(mul1(u[0], r), mul1(u[1], r)), pk2(mul1(u[2], r), mul1(u[3], r)),
                                                            pk2(mul1(v[0], r), mul1(v[1], r)), pk2(mul1(v[2], r), mul1(v[3], r))};
    };
    wait_vm_bar<GQA ? 6 : 4>();
    STAMP(ST + 5);
    sfor<0, 4>([&](auto U) { rda(IC<0>{}, U); });
    sfor<0, 5>([&](auto T) { rdb(IC<0>{}, T); });
    if constexpr (!GQA) { set_m0(ring); need(fa[0]); sfor<0, 4>([&](auto I) { piece(IC<2>{}, I); }); }
    sfor<0, 16>([&](auto S) {
      constexpr int s = S;
      SBAR();
      STAMP2(3, s);
      if constexpr (s == 0) prio_reset();
      if constexpr (GQA) {
        constexpr int nv = 4 + TW(s - 1) + TW(s - 2) + TW(s - 3);
        if constexpr ((s & 3) == 3) wait_vm_bar<nv>();
        else { need(fa[s & 1]); wait_vm_bar_raw<nv>(); }
      } else {
        if constexpr (s == 15) bar();
        else { if constexpr ((s & 3) == 3) bar(); wait_vm<4 + TW(s - 1) + TW(s - 2)>(); }
      }
      if constexpr (s == 8) {
        if (grown) {
          LAS float* em = (LAS float*)(lds + LDS_EXM + (hh * 16 + (lane_id() & 15)) * 48);
          sfor<0, 5>([&](auto T) {
            const float d = __builtin_fmaf(em[384 + T], SCL, mul1(em[T + 0], -SCL));
            const float a = d > GROW ? __builtin_amdgcn_exp2f(-d) : 1.f;
            sfor<0, 4>([&](auto U) { acc[T][U] *= a; });
          });
        }
      }
      if constexpr (s == 11) ov = voff_spl(lane_id(), 896);
      if constexpr (s == 15) ow = hh * 40960 + cq * 10240 + lane_id() * 16;
      sfor<0, 20>([&](auto I) {
        constexpr int i = I, t = i >> 2, u = i & 3;
        acc[t][u] = MFMA(fa[s & 1][u], fb[t], s == 0 ? ZERO4 : acc[t][u]);
        if constexpr (GQA) {
          if constexpr (i == 0) set_m0(ring + (s & 3) * 2048);
          if constexpr (i == 1 || i == 2) piece(IC<s + 4>{}, IC<i - 1>{});
          if constexpr (s < 15 && i >= 4 && i < 7) rda(IC<s + 1>{}, IC<i - 4>{});
          if constexpr (s < 15 && i == 8) rda(IC<s + 1>{}, IC<3>{});
        } else if constexpr (s < 15) {
          if constexpr (i < 4) rda(IC<s + 1>{}, IC<i & 3>{});
          if constexpr (i == 5) set_m0(ring + ((s + 1) & 1) * 4096);
          if constexpr (i == DM0) need(fa[(s + 1) & 1]);
          if constexpr (i >= DM0 && i < DM0 + 4) piece(IC<s + 3>{}, IC<(i - DM0) & 3>{});
        }
        if constexpr (s < 15 && u == 3) rdb(IC<s + 1>{}, IC<t>{});
        if constexpr (s < 10 && i >= 1 && i < 19) mop(IC<(180 + 18 * s + i - 1) % 360>{});
        if constexpr (s == 0 && i + (GQA ? 6 : 0) < 10) pput(IC<1>{}, IC<(i + (GQA ? 6 : 0)) % 10>{}, pw);
        if constexpr (s >= 3 && s <= 10 && (i == 5 || i == 10 || i == 14)) {
          constexpr int q4 = (s - 3) & 3, k = q4 * 3 - (q4 > 2) + (i == 5 ? 0 : i == 10 ? 1 : 2);
          if constexpr (k < 10 && !(q4 == 2 && i == 14)) pput(IC<s < 7 ? 2 : 3>{}, IC<k % 10>{}, pw);
        }
        if constexpr (s == 10 && (i == 3 || i == 9 || i == 15)) {
          const int l2 = lane_id();
          LAS float* el = (LAS float*)(lds + LDS_EXL + ((GQA ? 0 : hh) * 80 + (l2 & 15)) * 48 + (GQA ? w : cq) * 4);
          if constexpr (i == 3) el[(l2 >> 4) * 192] = red4<OpAdd>(ls[0], ls[1], ls[2], ls[3]);
          if constexpr (i == 9 && GQA) el[768 + ((l2 >> 4) & 1) * 960] = red2<OpAdd>(ls[4], ls[5 * MT - 1]);
          if constexpr (i == 9 && !GQA) el[768] = red2<OpAdd>(ls[4], ls[4]);
          if constexpr (i == 15 && GQA) el[960 + (l2 >> 4) * 192] = red4<OpAdd>(ls[5], ls[6], ls[7], ls[8]);
        }
        if constexpr (TW(s) && i == 14) { seen = __hip_atomic_load(sync_word<LAST ? 1 : 0>(p, b, j), RLX); asm volatile("" ::: "memory"); }
        if constexpr ((s == 12 || s == 13) && (i % 8) == 1 && (s - 12) * 3 + i / 8 < 5) {
          lds_t e = lds + LDS_EXL + (hh * 80 + ((s - 12) * 3 + i / 8) * 16 + (lane_id() & 15)) * 48;
          lv[0] = *(LAS const f32x4*)e;
          if constexpr (GQA) lv[1] = *(LAS const f32x4*)(e + 16);
        }
        if constexpr ((s == 12 || s == 13) && i == (i / 8) * 8 + (i < 16 ? 6 : 3) && (s - 12) * 3 + i / 8 < 5) {
          float sum = ((lv[0][0] + lv[0][1]) + lv[0][2]) + lv[0][3];
          if constexpr (GQA) sum = (((sum + lv[1][0]) + lv[1][1]) + lv[1][2]) + lv[1][3];
          rl[((s - 12) * 3 + i / 8) % 5] = __builtin_amdgcn_rcpf(sum);
        }
        if constexpr (s == 15 && i >= 6 && (i & 1) == 0) oput(IC<(i - 6) / 4>{}, IC<((i - 6) / 2) % 2>{});
        SBAR();
      });
    });
    STAMP2(3, 16);
    STAMP(ST + 6);
    sfor<7, 10>([&](auto I) { oput(IC<I / 2>{}, IC<I % 2>{}); });
    settle<LAST ? 1 : 0>(p, lds, b, j, seen);
    bar();
  }
  STAMP(ST + 7);

  RedRows red;
  auto oproj = [&](auto NTILE) {
    constexpr int NT = NTILE, NS = 5 * NT, D0 = NT == 4 ? DM0 : 5;
    const rsrc_t rN = mk_rsrc(p.wq[1 - P], (512 * j + 64 * w) * 2, W_BYTES - (512 * j + 64 * w) * 2);
    const int l = lane_id(), g = l >> 4, q = (l >> 2) & 3;
    const unsigned ov = voff_spl(l, 896), we = voff_nat(l, 8192);
    auto piece = [&](auto G, auto I) {
      if constexpr (G < 16) dmap<false, I>(rO, ov, (G * 32 + I * 8) * 896);
      else dmap<false, I>(rN, we ^ ((I & 1) * 64), ((G - 16) * 32 + I * 8) * 8192);
    };
    unsigned a0[NT], o1 = l * 16 + 40960;
    sfor<0, NT>([&](auto U) {
      a0[U] = LDS_RING + w * 8192 + (4 * g + q) * 128 + ((((q >> 1) | ((g & 1) << 1)) ^ (U + (NT == 3 ? cq & 1 : 0))) * 32) + 8 * (l & 3);
    });
    asm volatile("" : "+v"(o1));
    auto rda = [&](auto S, auto U) { lds_t a = lds + a0[U] + (S & 1) * 4096; fa[S & 1][U] = ldtr(a, a + 2048); };
    auto rdb = [&](auto S, auto T) { fb[T] = ld128(lds + (S < 8 ? l * 16 : o1) + ((S & 7) * 5 + T) * 1024); };
    auto needa = [&](auto X) {
      if constexpr (NT == 4) need(fa[X]);
      else need3(fa[X]);
    };
    const rsrc_t rS = mk_rsrc(p.slab, ((P * 32 + b) * 8 + j) * SLAB + (NT == 4 ? w * 10240 : 40960 + cq * 8192), 10240);
    auto sput = [&](auto R) {
      constexpr int r = R;
      constexpr int tu = NT == 4 ? r / 2 : r < 5 ? r : 2 * (r - 5), cu = NT == 4 ? 2 * (r & 1) : r < 5 ? 0 : 2;
      constexpr int tv = NT == 4 ? r / 2 : r < 5 ? r : r == 7 ? 4 : 2 * (r - 5) + 1, cv = NT == 4 ? 2 * (r & 1) + 1 : r < 5 ? 1 : 2;
      const f32x4 u = acc[tu][cu], v = acc[tv][cv];
      auto r0 = __builtin_amdgcn_permlane16_swap(pk2(u[0], u[1]), pk2(v[0], v[1]), false, false);
      auto r1 = __builtin_amdgcn_permlane16_swap(pk2(u[2], u[3]), pk2(v[2], v[3]), false, false);
      __builtin_amdgcn_raw_buffer_store_b128(u32x4{r0[0], r1[0], r0[1], r1[1]}, rS, l * 16 + r * 1024, 0, 16  );
    };
    wait_vm<4>();
    sfor<0, NT>([&](auto U) { rda(IC<0>{}, U); });
    sfor<0, 5>([&](auto T) { rdb(IC<0>{}, T); });
    set_m0(ring);
    needa(IC<0>{});
    sfor<0, 4>([&](auto I) { piece(IC<2>{}, I); });
    sfor<0, 16>([&](auto S) {
      constexpr int s = S;
      SBAR();
      STAMP2(5, s);
      if constexpr (s == 0) prio_reset();
      if constexpr (s == PO) prio_yield(hh);
      if constexpr (s < 15) wait_vm<4>();
      else if constexpr (!LAST) { bar(); issue_x(p, 1 - P, b, w, lds0); }
      sfor<0, NS>([&](auto I) {
        constexpr int i = I, t = i / NT, u = i % NT;
        acc[t][u] = MFMA(fa[s & 1][u], fb[t], s == 0 ? ZERO4 : acc[t][u]);
        if constexpr (s < 15) {
          if constexpr (i < NT) rda(IC<s + 1>{}, IC<i % NT>{});
          if constexpr (u == NT - 1) rdb(IC<s + 1>{}, IC<t>{});
          if constexpr (!(LAST && s >= 13)) {
            if constexpr (i == D0 - 2) set_m0(ring + ((s + 1) & 1) * 4096);
            if constexpr (i == D0) needa(IC<(s + 1) & 1>{});
            if constexpr (i >= D0 && i < D0 + 4) piece(IC<s + 3>{}, IC<(i - D0) & 3>{});
          }
          if constexpr (LAST && s >= 13 && i >= 3 && i < 15 && i % 3 == 0) red_load<0, (4 * (s - 13) + i / 3 - 1) & 7>(red, p, 1 - P, b, j, w);
        } else if constexpr (NT == 4) {
          if constexpr (i >= 6 && (i & 1) == 0) sput(IC<(i - 6) / 2>{});
        } else {
          if constexpr (i == 5 || i == 8 || i == 11 || i == 14) sput(IC<(i - 5) / 3>{});
          if constexpr (i == 10) sput(IC<5>{});
        }
        SBAR();
      });
    });
    STAMP2(5, 16);
    STAMP(ST + 8);
    if constexpr (NT == 4) sfor<7, 10>(sput);
    else { sput(IC<6>{}); sput(IC<4>{}); sput(IC<7>{}); }
    if constexpr (!LAST) wait_vm_bar<NT == 4 ? 10 : 8>();
  };
  if (hh == 0) oproj(IC<4>{});
  else oproj(IC<3>{});
  STAMP(ST + 9);
  if constexpr (LAST) {
    wait_vm<0>();
    STAMP(21);
    bar();
    STAMP(22);
    if (w == 0) arrive<2>(p, lds, b, j);
    if (w == 7) sfor<0, 8>([&](auto J) { red_load<1, J>(red, p, 1 - P, b, j, w); });
    red_store(red, p, 1 - P, b, j, w);
    if (w == 0) settle<2>(p, lds, b, j, __hip_atomic_load(sync_word<2>(p, b, j), RLX));
    bar();
    STAMP(23);
    sfor<0, 8>([&](auto J) { red_load<0, J>(red, p, P, b, j, w); });
    if (w == 7) sfor<0, 8>([&](auto J) { red_load<1, J>(red, p, P, b, j, w); });
    red_store(red, p, P, b, j, w);
  }
}

__global__ __launch_bounds__(512, 2) void fused_attn_kernel(Params p) {
  extern __shared__ __attribute__((aligned(16))) char smem[];
  lds_t lds = (lds_t)smem;
  const unsigned lds0 = (unsigned)(uintptr_t)lds;
  const int w = __builtin_amdgcn_readfirstlane(threadIdx.x >> 6);
  const int j = blockIdx.x & 7, b = blockIdx.x >> 3;
  const int first = b & 1;
  STAMP(0);
  {
    unsigned found = 0;
    if (w == 7) found = __hip_atomic_load(flag_word(p, b, lane_id() & 7), RLX);
    asm volatile("" ::: "memory");
    issue_x(p, first, b, w, lds0);
    asm volatile("s_barrier" ::: "memory");
    const int l = lane_id();
    const rsrc_t rW = mk_rsrc(p.wq[first], (512 * j + 64 * w) * 2, W_BYTES - (512 * j + 64 * w) * 2);
    const unsigned we = voff_nat(l, 8192), ring = lds0 + LDS_RING + w * 8192;
    sfor<0, 2>([&](auto G) {
      set_m0(ring + G * 4096);
      sfor<0, 4>([&](auto I) { dmap<false, I>(rW, we ^ ((I & 1) * 64), (G * 32 + I * 8) * 8192); });
    });
    const unsigned id = (w & 3) * 64 + l;
    if (w < 4) warm(lds0, mk_rsrc(p.wq[first], 0, W_BYTES), ((b >> 1) * 28 + (id >> 3)) * 8192 + 1024 * j + (id & 7) * 128);
    else warm(lds0, mk_rsrc(p.wo[first], 0, W_BYTES), 512 * j * 896 + (b >> 1) * 28672 + id * 128);
    wait_vm_bar<9>();
    if (w == 7) {
      *(LAS unsigned*)(lds + LDS_OLD + (l & 7) * 4) = found;
      if (l < 8) __hip_atomic_store(ack_word(p, b, l, j), found + 1u, RLX);
    }
  }
  STAMP(10);
  if (first == 0) {
    run_problem<0, false, 0>(p, lds, lds0, fresh(b), fresh(j), fresh(w));
    run_problem<1, true, 10>(p, lds, lds0, fresh(b), fresh(j), fresh(w));
  } else {
    run_problem<1, false, 0>(p, lds, lds0, fresh(b), fresh(j), fresh(w));
    run_problem<0, true, 10>(p, lds, lds0, fresh(b), fresh(j), fresh(w));
  }
  STAMP(20);
}

extern "C" void kernel_launch(void* const* d_in, const int* in_sizes, int n_in, void* d_out, int out_size, void* d_ws,
                              size_t ws_size, hipStream_t stream) {
  Params p;
  for (int i = 0; i < 2; ++i) {
    p.x[i] = d_in[5 * i + 0]; p.k[i] = d_in[5 * i + 1]; p.v[i] = d_in[5 * i + 2]; p.wq[i] = d_in[5 * i + 3]; p.wo[i] = d_in[5 * i + 4];
  }
  p.sync = (unsigned*)d_ws;
  p.slab = (char*)d_ws + 262144;
  p.out = d_out;
  p.diag = (unsigned long long*)((char*)d_ws + (64u << 20));
  (void)hipFuncSetAttribute((const void*)fused_attn_kernel, hipFuncAttributeMaxDynamicSharedMemorySize, LDS_TOTAL);
  fused_attn_kernel<<<256, 512, LDS_TOTAL, stream>>>(p);
}
```

```cpp
#include <hip/hip_runtime.h>
#include <hip/hip_bf16.h>
#include <stdint.h>
#include <type_traits>

typedef short bf16x8 __attribute__((ext_vector_type(8)));
typedef short s16x4 __attribute__((ext_vector_type(4)));
typedef float f32x4 __attribute__((ext_vector_type(4)));
typedef float f32x2 __attribute__((ext_vector_type(2)));
typedef __bf16 bf16x2 __attribute__((ext_vector_type(2)));
typedef unsigned u32x4 __attribute__((ext_vector_type(4)));
typedef unsigned u32x2 __attribute__((ext_vector_type(2)));
#define LAS __attribute__((address_space(3)))
typedef __amdgpu_buffer_rsrc_t rsrc_t;
typedef LAS char* lds_t;

#define DIAG 0
#define DLAST 1
#define PO 12
#define FORCE_SAFE 0
#define PRE1F 0
#define PSF 22
#define ABL 0
#define SAUX 16
#define LAUX 16
#define XW 0
#define ONT 0
#define KVPOL "nt"
#define WPOL ""
#define X2NT 0
#define DEFER 0
#define WSTAIL 0
#define CODEW 1
#define FAKEKV 0

constexpr int LDS_RING = 81920;
constexpr int LDS_EXM = 147456;
constexpr int LDS_EXL = 151552;
constexpr int LDS_DUMMY = 159232;
constexpr int LDS_OLD = 159488;
constexpr int LDS_BAD = LDS_OLD + 32;
constexpr int LDS_TOTAL = DIAG ? 163840 : 159552;
constexpr unsigned X_BYTES = 80 * 448 * 2;
constexpr unsigned W_BYTES = 448 * 4096 * 2;
constexpr unsigned KV_HEAD = 512 * 256 * 2;
constexpr unsigned SLAB = 72 * 1024;
constexpr float SCL = 0.0625f * 1.4426950408889634f;
constexpr float SUM_LO = 0x1p-60f, SUM_HI = 0x1p60f;
constexpr float GROW = 20.f;

template <int I, int N, class F>
__device__ __forceinline__ void sfor(F&& f) {
  if constexpr (I < N) { f(std::integral_constant<int, I>{}); sfor<I + 1, N>(f); }
}
#define SBAR() __builtin_amdgcn_sched_barrier(0)
__device__ __forceinline__ f32x4 MFMA(bf16x8 a, bf16x8 b, f32x4 c) {
  f32x4 d = __builtin_amdgcn_mfma_f32_16x16x32_bf16(a, b, c, 0, 0, 0);
  asm volatile("" : "+v"(d));
  return d;
}
template <int N> __device__ __forceinline__ void wait_vm() { asm volatile("s_waitcnt vmcnt(%0)" ::"i"(N) : "memory"); }
__device__ __forceinline__ void bar() { asm volatile("s_waitcnt lgkmcnt(0)\n\ts_barrier" ::: "memory"); }
template <int N> __device__ __forceinline__ void wait_vm_bar_raw() { asm volatile("s_waitcnt vmcnt(%0)\n\ts_barrier" ::"i"(N) : "memory"); }
template <int N> __device__ __forceinline__ void wait_vm_bar() { asm volatile("s_waitcnt vmcnt(%0) lgkmcnt(0)\n\ts_barrier" ::"i"(N) : "memory"); }
__device__ __forceinline__ void prio_reset() { asm volatile("s_setprio 1"); }
__device__ __forceinline__ void prio_yield(int hh) { asm volatile("s_cmp_lg_u32 %0, 0\n\ts_cbranch_scc1 1f\n\ts_setprio 0\n1:" ::"s"(hh) : "scc"); }
__device__ __forceinline__ int lane_id() {
  int l; asm volatile("v_mbcnt_lo_u32_b32 %0, -1, 0\n\tv_mbcnt_hi_u32_b32 %0, -1, %0" : "=v"(l)); return l;
}
__device__ __forceinline__ int fresh(int x) { asm volatile("" : "+s"(x)); return x; }
__device__ __forceinline__ unsigned pk2(float a, float b) {
  return __builtin_bit_cast(unsigned, __builtin_convertvector(f32x2{a, b}, bf16x2));
}
__device__ __forceinline__ rsrc_t mk_rsrc(const void* p, unsigned off, unsigned bytes) {
  return __builtin_amdgcn_make_buffer_rsrc((void*)((const char*)p + off), (short)0, (int)bytes, 0x00020000);
}
__device__ __forceinline__ void need(const bf16x8 (&f)[4]) { asm volatile("" ::"v"(f[0]), "v"(f[1]), "v"(f[2]), "v"(f[3]) : "memory"); }
__device__ __forceinline__ void need3(const bf16x8 (&f)[4]) { asm volatile("" ::"v"(f[0]), "v"(f[1]), "v"(f[2]) : "memory"); }
__device__ __forceinline__ void set_m0(unsigned m0v) { asm volatile("s_mov_b32 m0, %0\n\ts_nop 0" ::"s"(m0v) : "memory"); }
template <bool NT, int I>
__device__ __forceinline__ void dmap(rsrc_t r, unsigned v, unsigned src) {
  if constexpr (NT && !FAKEKV) asm volatile("buffer_load_dwordx4 %0, %1, %2 offen offset:%3 " KVPOL " lds" ::"v"(v), "s"(r), "s"(src - 1024u * I), "i"(1024 * I) : "memory");
  else asm volatile("buffer_load_dwordx4 %0, %1, %2 offen offset:%3 lds" ::"v"(v), "s"(r), "s"(src - 1024u * I), "i"(1024 * I) : "memory");
}
__device__ __forceinline__ void warm(unsigned lds0, rsrc_t r, unsigned v) {
  asm volatile("s_mov_b32 m0, %0\n\ts_nop 0\n\tbuffer_load_dword %1, %2, 0 offen " WPOL " lds" ::"s"(lds0 + LDS_DUMMY), "v"(v), "s"(r) : "memory");
}
extern "C" __device__ float llvm_fmul_legacy(float, float) __asm("llvm.amdgcn.fmul.legacy");
__device__ __forceinline__ float mul1(float a, float b) { return llvm_fmul_legacy(a, b); }
__device__ __forceinline__ bf16x8 ld128(lds_t p) { return *(LAS const bf16x8*)p; }
__device__ __forceinline__ bf16x8 ldtr(lds_t p0, lds_t p1) {
  s16x4 lo = __builtin_amdgcn_ds_read_tr16_b64_v4i16((LAS s16x4*)p0);
  s16x4 hi = __builtin_amdgcn_ds_read_tr16_b64_v4i16((LAS s16x4*)p1);
  return __builtin_shufflevector(lo, hi, 0, 1, 2, 3, 4, 5, 6, 7);
}
__device__ __forceinline__ float max3(float a, float b, float c) { return __builtin_fmaxf(__builtin_fmaxf(a, b), c); }
struct OpMax { static __device__ __forceinline__ float f(float a, float b) { return max3(a, b, b); } };
struct OpAdd { static __device__ __forceinline__ float f(float a, float b) { return a + b; } };
template <class Op> __device__ __forceinline__ float sw16(float a, float b) {
  auto r = __builtin_amdgcn_permlane16_swap(__float_as_uint(a), __float_as_uint(b), false, false);
  return Op::f(__uint_as_float(r[0]), __uint_as_float(r[1]));
}
template <class Op> __device__ __forceinline__ float sw32(float a, float b) {
  auto r = __builtin_amdgcn_permlane32_swap(__float_as_uint(a), __float_as_uint(b), false, false);
  return Op::f(__uint_as_float(r[0]), __uint_as_float(r[1]));
}
template <class Op> __device__ __forceinline__ float red4(float a, float b, float c, float d) { return sw32<Op>(sw16<Op>(a, b), sw16<Op>(c, d)); }
template <class Op> __device__ __forceinline__ float red2(float a, float b) { const float x = sw16<Op>(a, b); return sw32<Op>(x, x); }

__device__ __forceinline__ unsigned voff_nat(int l, unsigned pitch) { return (l >> 3) * pitch + 16 * ((l & 7) ^ (((l >> 4) & 1) << 1)); }
__device__ __forceinline__ unsigned voff_spl(int l, unsigned pitch) { return (l >> 3) * pitch + 16 * ((l & 7) ^ (((l >> 4) & 3) << 1)); }
__device__ __forceinline__ unsigned voff_k(int l) { return (l >> 3) * 512 + 16 * ((l & 7) ^ ((l >> 4) & 3)); }

struct Params {
  const void *x[2], *k[2], *v[2], *wq[2], *wo[2];
  void* slab;
  unsigned* sync;
  void* out;
  unsigned long long* diag;
};

#define STAMP(i)
#define STAMP2(ph, i)

constexpr int DM0 = 7;
template <int V> using IC = std::integral_constant<int, V>;
constexpr f32x4 ZERO4 = {0.f, 0.f, 0.f, 0.f};

template <bool NT = false>
__device__ __forceinline__ void issue_x(const Params& p, int P, int b, int w, unsigned lds0) {
  w = fresh(w);
  const int l = lane_id();
  const unsigned v = (l >> 3) * 896 + 16 * ((l & 7) ^ ((l >> 4) & 3));
  const rsrc_t rx = mk_rsrc(p.x[P], b * X_BYTES, X_BYTES);
  sfor<0, 9>([&](auto I) {
    const unsigned pc = w * 9 + I, blk = (pc * 205) >> 11, rg = pc - blk * 10;
    set_m0(lds0 + pc * 1024);
    dmap<NT, 0>(rx, v ^ ((rg & 1) * 64), rg * 7168 + blk * 128);
  });
}

typedef __attribute__((address_space(1))) unsigned gu32;
#define RLX __ATOMIC_RELAXED, __HIP_MEMORY_SCOPE_AGENT
__device__ __forceinline__ gu32* flag_word(const Params& p, int b, int j) { return (gu32*)p.sync + (b * 8 + j) * 32; }
__device__ __forceinline__ gu32* ack_word(const Params& p, int b, int prod, int cons) { return (gu32*)p.sync + 8192 + ((b * 8 + prod) * 8 + cons) * 16; }
template <int STAGE>
__device__ __forceinline__ void arrive(const Params& p, lds_t lds, int b, int j) {
  const unsigned old = *(LAS const unsigned*)(lds + LDS_OLD + j * 4);
  if (lane_id() == 0) __hip_atomic_store(flag_word(p, b, j), old + STAGE, RLX);
}
template <int STAGE> __device__ __forceinline__ gu32* sync_word(const Params& p, int b, int j) {
  const int c = lane_id() & 7;
  return STAGE ? flag_word(p, b, c) : ack_word(p, b, j, c);
}
template <int STAGE>
__device__ __forceinline__ void settle(const Params& p, lds_t lds, int b, int j, unsigned seen) {
  const unsigned old = *(LAS const unsigned*)(lds + LDS_OLD + (STAGE ? lane_id() & 7 : j) * 4);
  for (unsigned spins = 0; spins < (1u << 22); ++spins) {
    if (__builtin_amdgcn_ballot_w64(STAGE ? seen - old - STAGE > 2u - STAGE : seen != old + 1u) == 0) break;
    __builtin_amdgcn_s_sleep(2);
    seen = __hip_atomic_load(sync_word<STAGE>(p, b, j), RLX);
  }
  asm volatile("" ::: "memory");
}
struct RedRows { u32x4 v[2][8]; };
template <int K, int J>
__device__ __forceinline__ void red_load(RedRows& r, const Params& p, int P, int b, int j, int w) {
  const rsrc_t rS = mk_rsrc(p.slab, (P * 32 + b) * 8 * SLAB, 8 * SLAB);
  r.v[K][J] = __builtin_amdgcn_raw_buffer_load_b128(rS, lane_id() * 16, J * SLAB + (9 * j + w + K) * 1024, LAUX  );
}
__device__ __forceinline__ void red_store(const RedRows& r, const Params& p, int P, int b, int j, int w) {
  const int l = lane_id(), n = l & 15, g = l >> 4;
  sfor<0, 2>([&](auto K) {
    if (K == 0 || w == 7) {
      float sm[8];
      sfor<0, 4>([&](auto I) { sm[2 * I] = __uint_as_float(r.v[K][0][I + 0] << 16); sm[2 * I + 1] = __uint_as_float(r.v[K][0][I + 0] & 0xffff0000u); });
      sfor<1, 8>([&](auto J) {
        sfor<0, 4>([&](auto I) { sm[2 * I] += __uint_as_float(r.v[K][J][I + 0] << 16); sm[2 * I + 1] += __uint_as_float(r.v[K][J][I + 0] & 0xffff0000u); });
      });
      const int f = 9 * j + w + K;
      int t, col;
      if (f < 40) { const int ww = f / 10; t = (f - ww * 10) >> 1; col = 64 * ww + 32 * (f & 1) + 16 * (g & 1); }
      else { const int rr = f & 7, c0 = 256 + 48 * ((f - 40) >> 3); t = rr < 5 ? rr : 2 * (rr - 5) + (g & 1); col = c0 + (rr < 5 ? 16 * (g & 1) : 32); }
      if (t < 5)
      {
        u32x4* dst = (u32x4*)((char*)p.out + ((size_t)(b * 80 + t * 16 + n) * 896 + P * 448 + col + 8 * (g >> 1)) * 2);
        const u32x4 val = u32x4{pk2(sm[0], sm[1]), pk2(sm[2], sm[3]), pk2(sm[4], sm[5]), pk2(sm[6], sm[7])};
        if constexpr (ONT) __builtin_nontemporal_store(val, dst); else *dst = val;
      }
    }
  });
}

template <int P  , bool LAST, int ST, bool SAFE>
__device__ __forceinline__ bool front(const Params& p, lds_t lds, unsigned lds0, int b, int j, int w, const u32x4 (&held)[10]) {
  constexpr bool GQA = (P == 0), HELD = DEFER && LAST && !SAFE;
  const int bk = FAKEKV ? 0 : b;
  const int hh = w >> 2, cq = w & 3;
  const unsigned ring = lds0 + LDS_RING + w * 8192;
  f32x4 acc[5][4];
  bf16x8 fa[2][4], fb[5];

  const unsigned koff = GQA ? (bk * 8 + j) * KV_HEAD + w * 16 * 512 : (bk * 16 + 2 * j + hh) * KV_HEAD + cq * 32 * 512;
  const rsrc_t rK = mk_rsrc(p.k[P], koff, KV_HEAD - (GQA ? w * 16 * 512 : cq * 32 * 512));
  auto k_src = [](int g, int i) -> unsigned {
    return GQA ? ((g >> 2) * 256 + (i >> 1) * 128 + (i & 1) * 8) * 512 + (g & 3) * 128 : ((g >> 2) * 128 + i * 8) * 512 + (g & 3) * 128;
  };

  {
    const rsrc_t rW = mk_rsrc(p.wq[P], (512 * j + 64 * w) * 2, W_BYTES - (512 * j + 64 * w) * 2);
    const int l = lane_id(), n = l & 15, g = l >> 4, q = (l >> 2) & 3;
    const unsigned we = voff_nat(l, 8192), ke = voff_k(l);
    const unsigned a0 = LDS_RING + w * 8192 + (8 * g + q) * 128 + (((q >> 1) | ((g & 1) << 1)) * 32) + 8 * (l & 3);
    unsigned x0[2][2];
    x0[0][0] = n * 128 + 16 * (g ^ ((n >> 1) & 7)); x0[1][0] = x0[0][0] ^ 64; x0[0][1] = x0[0][0] + 40960; x0[1][1] = x0[1][0] + 40960;
    asm volatile("" : "+v"(x0[0][1]), "+v"(x0[1][1]));
    const unsigned qw = hh * 40960 + cq * 10240 + l * 16;
    const rsrc_t rH = mk_rsrc(p.slab, (((1 - P) * 32 + b) * 8 + j) * SLAB + (hh ? 40960 + cq * 8192 : w * 10240), hh ? 8192 : 10240);
    float scl = SCL;
    asm volatile("" : "+s"(scl));
    auto rda = [&](auto S, auto U) { lds_t a = lds + (a0 ^ (U * 32)) + (S & 1) * 4096; fa[S & 1][U] = ldtr(a, a + 512); };
    auto rdb = [&](auto S, auto T) { fb[T] = ld128(lds + x0[S & 1][S >= 8] + ((S >> 1) & 3) * 10240 + T * 2048); };
    auto piece = [&](auto G, auto I) {
      if constexpr (G < 14) dmap<false, I>(rW, we ^ ((I & 1) * 64), (G * 32 + I * 8) * 8192);
      else dmap<true, I>(rK, ke ^ ((I & 1) * 64), k_src(G - 14, I));
    };
    auto qput = [&](auto T, auto H) {
      const f32x4 u = acc[T][2 * H], v = acc[T][2 * H + 1];
      if constexpr (SAFE) *(LAS u32x4*)(lds + qw + H * 5120 + T * 1024) = u32x4{pk2(u[0], u[1]), pk2(u[2], u[3]), pk2(v[0], v[1]), pk2(v[2], v[3])};
      else *(LAS u32x4*)(lds + qw + H * 5120 + T * 1024) = u32x4{pk2(mul1(u[0], scl), mul1(u[1], scl)), pk2(mul1(u[2], scl), mul1(u[3], scl)),
                                                                 pk2(mul1(v[0], scl), mul1(v[1], scl)), pk2(mul1(v[2], scl), mul1(v[3], scl))};
    };
    if constexpr (!LAST && !SAFE) wait_vm<5 + XW>();
    sfor<0, 4>([&](auto U) { rda(IC<0>{}, U); });
    sfor<0, 5>([&](auto T) { rdb(IC<0>{}, T); });
    set_m0(ring);
    need(fa[0]);
    sfor<0, 4>([&](auto I) { piece(IC<2>{}, I); });
    sfor<0, 14>([&](auto S) {
      constexpr int s = S;
      SBAR();
      STAMP2(4, s);
      if constexpr (s == 0) prio_reset();
      if constexpr (s == 9) prio_yield(hh);
      if constexpr (s == 0) { if constexpr (!LAST && !SAFE) wait_vm<5 + XW>(); }
      else if constexpr (s < 13) wait_vm<(LAST && !DEFER && s == 6) ? 0 : 4>();
      else {
        bar();
        if constexpr (SAFE) *(LAS f32x2*)(lds + LDS_EXM + w * 512 + l * 8) = f32x2{-INFINITY, -INFINITY};
        else *(LAS unsigned*)(lds + LDS_BAD) = 0u;
        if constexpr (LAST && !DEFER) { if (w == 0) arrive<1>(p, lds, b, j); }
      }
      sfor<0, 20>([&](auto I) {
        constexpr int i = I, t = i >> 2, u = i & 3;
        acc[t][u] = MFMA(fa[s & 1][u], fb[t], s == 0 ? ZERO4 : acc[t][u]);
        if constexpr (s < 13) {
          if constexpr (i < 4) rda(IC<s + 1>{}, IC<i & 3>{});
          if constexpr (u == 3) rdb(IC<s + 1>{}, IC<t>{});
          if constexpr (i == 5) set_m0(ring + ((s + 1) & 1) * 4096);
          if constexpr (i == DM0) need(fa[(s + 1) & 1]);
          if constexpr (i >= DM0 && i < DM0 + 4) piece(IC<s + 3>{}, IC<(i - DM0) & 3>{});
        } else {
          if constexpr (i >= 6 && (i & 1) == 0) qput(IC<(i - 6) / 4>{}, IC<((i - 6) / 2) % 2>{});
          if constexpr (HELD && i < 10) __builtin_amdgcn_raw_buffer_store_b128(held[i % 10], rH, l * 16 + i * 1024, 0, SAUX);
        }
        SBAR();
      });
    });
    STAMP2(4, 14);
    STAMP(ST + 1);
    sfor<7, 10>([&](auto I) { qput(IC<I / 2>{}, IC<I % 2>{}); });
    bar();
  }
  STAMP(ST + 2);

  const rsrc_t rV = GQA ? mk_rsrc(p.v[P], (bk * 8 + j) * KV_HEAD + hh * 16 * 512 + cq * 128, KV_HEAD - (hh * 16 * 512 + cq * 128))
                        : mk_rsrc(p.v[P], (bk * 16 + 2 * j + hh) * KV_HEAD + cq * 128, KV_HEAD - cq * 128);
  constexpr int MT = GQA ? 2 : 1;
  constexpr int NG = GQA ? 8 : 16;
  f32x4 sc[4][10];
  float mx[5 * MT], nm[5 * MT], ls[5 * MT], ev[4];
  u32x2 pq[4][10];
  auto mop = [&](auto ID) {
    constexpr int id = ID, kq = id / 90, k = (id / 9) % 10, part = id % 9, r = GQA ? k : (k >> 1);
    if constexpr (part == 0 || part == 2) asm volatile("" : "+v"(nm[r]));
    if constexpr (part == 0) { ev[0] = __builtin_fmaf(sc[kq][k][0], SCL, nm[r]); ev[1] = __builtin_fmaf(sc[kq][k][1], SCL, nm[r]); }
    if constexpr (part == 1) ev[0] = __builtin_amdgcn_exp2f(ev[0]);
    if constexpr (part == 2) { ev[2] = __builtin_fmaf(sc[kq][k][2], SCL, nm[r]); ev[3] = __builtin_fmaf(sc[kq][k][3], SCL, nm[r]); }
    if constexpr (part == 3) ev[1] = __builtin_amdgcn_exp2f(ev[1]);
    if constexpr (part == 4) ev[2] = __builtin_amdgcn_exp2f(ev[2]);
    if constexpr (part == 5) { if constexpr (kq == 0 && (GQA || (k & 1) == 0)) ls[r] = ev[0] + ev[1]; else ls[r] = (ls[r] + ev[0]) + ev[1]; }
    if constexpr (part == 6) ev[3] = __builtin_amdgcn_exp2f(ev[3]);
    if constexpr (part == 7) { pq[kq][k][0] = pk2(ev[0], ev[1]); ls[r] += ev[2]; }
    if constexpr (part == 8) { pq[kq][k][1] = pk2(ev[2], ev[3]); ls[r] += ev[3]; }
    if constexpr (part == 0) asm volatile("" : "+v"(ev[0]), "+v"(ev[1]));
    if constexpr (part == 1) asm volatile("" : "+v"(ev[0]));
    if constexpr (part == 2) asm volatile("" : "+v"(ev[2]), "+v"(ev[3]));
    if constexpr (part == 3) asm volatile("" : "+v"(ev[1]));
    if constexpr (part == 4) asm volatile("" : "+v"(ev[2]));
    if constexpr (part == 5) asm volatile("" : "+v"(ls[r]));
    if constexpr (part == 6) asm volatile("" : "+v"(ev[3]));
    if constexpr (part == 7) asm volatile("" : "+v"(ls[r]), "+v"(pq[kq][k][0]));
    if constexpr (part == 8) asm volatile("" : "+v"(ls[r]), "+v"(pq[kq][k][1]));
  };
  auto fop = [&](auto ID) {
    constexpr int id = ID, kq = id / 70, k = (id / 7) % 10, part = id % 7, r = GQA ? k : (k >> 1);
    if constexpr (part == 0) asm volatile("" : "+v"(sc[kq][k]));
    if constexpr (part == 0) { ev[0] = __builtin_amdgcn_exp2f(sc[kq][k][0]); asm volatile("" : "+v"(ev[0])); }
    if constexpr (part == 1) { ev[1] = __builtin_amdgcn_exp2f(sc[kq][k][1]); asm volatile("" : "+v"(ev[1])); }
    if constexpr (part == 2) { ev[2] = __builtin_amdgcn_exp2f(sc[kq][k][2]); asm volatile("" : "+v"(ev[2])); }
    if constexpr (part == 3) {
      if constexpr (kq == 0 && (GQA || (k & 1) == 0)) ls[r] = ev[0] + ev[1]; else ls[r] = (ls[r] + ev[0]) + ev[1];
      asm volatile("" : "+v"(ls[r]));
    }
    if constexpr (part == 4) { ev[3] = __builtin_amdgcn_exp2f(sc[kq][k][3]); asm volatile("" : "+v"(ev[3])); }
    if constexpr (part == 5) { pq[kq][k][0] = pk2(ev[0], ev[1]); ls[r] += ev[2]; asm volatile("" : "+v"(ls[r]), "+v"(pq[kq][k][0])); }
    if constexpr (part == 6) { pq[kq][k][1] = pk2(ev[2], ev[3]); ls[r] += ev[3]; asm volatile("" : "+v"(ls[r]), "+v"(pq[kq][k][1])); }
  };
  auto pput = [&](auto KQ, auto K, unsigned pw) {
    constexpr int kq = KQ, k = K;
    if constexpr (GQA) *(LAS u32x2*)(lds + pw + (kq & 1) * 40960 + (k / 5) * 20480 + (k % 5) * 1024) = pq[kq][k];
    else if constexpr (k & 1) *(LAS u32x4*)(lds + pw + (kq & 1) * 40960 + (k / 2) * 1024) = u32x4{pq[kq][k - 1][0], pq[kq][k - 1][1], pq[kq][k][0], pq[kq][k][1]};
  };
  bool grown = false;
  unsigned pw;
  constexpr int PRE1 = SAFE ? 10 : GQA ? PRE1F : 0;
  {
    const int l = lane_id(), n = l & 15, g = l >> 4;
    const unsigned ke = voff_k(l);
    unsigned vv = 0;
    const unsigned k0 = LDS_RING + w * 8192 + n * 128 + 16 * ((g >> 1) ^ ((n >> 1) & 7)) + 8 * (g & 1);
    const unsigned q0 = (GQA ? 0 : hh * 40960) + l * 16;
    unsigned q1 = q0 + 40960;
    asm volatile("" : "+v"(q1));
    constexpr int NSUB = 2 * MT;
    auto rdk = [&](auto G, auto I) {
      lds_t a = lds + (I & 1) * 2048 + (G & 1) * 4096;
      const s16x4 lo = *(LAS const s16x4*)(a + (k0 ^ ((I >> 1) * 64))), hi = *(LAS const s16x4*)(a + (k0 ^ ((I >> 1) * 64 + 32)));
      fa[G & 1][I] = __builtin_shufflevector(lo, hi, 0, 1, 2, 3, 4, 5, 6, 7);
    };
    auto rdq = [&](auto Z, auto T) {
      constexpr int z = Z, gi = z / NSUB, e = (z % NSUB) / MT, h = z % MT;
      fb[T] = ld128(lds + (h ? q1 : q0) + ((2 * (gi & 3) + e) * 5 + T) * 1024);
    };
    auto piece = [&](auto G, auto I) {
      if constexpr (G < NG) dmap<true, I>(rK, ke ^ ((I & 1) * 64), k_src(G, I));
      else if constexpr (!GQA) dmap<true, I>(rV, vv, ((G - NG) * 32 + I * 8) * 512);
      else dmap<true, I>(rV, vv, ((2 * (G - NG) + (I >> 1)) * 32 + (I & 1) * 8) * 512);
    };
    auto qmax = [&](auto KQ, auto K) {
      constexpr int kq = KQ, k = K, r = GQA ? k : (k >> 1);
      if constexpr ((kq & 1) == 0 && (GQA || (k & 1) == 0)) mx[r] = max3(max3(sc[kq][k][0], sc[kq][k][1], sc[kq][k][2]), sc[kq][k][3], sc[kq][k][3]);
      else mx[r] = max3(max3(mx[r], sc[kq][k][0], sc[kq][k][1]), sc[kq][k][2], sc[kq][k][3]);
      asm volatile("" : "+v"(mx[r]));
    };
    auto xmax = [&](auto HALF, auto C) {
      const int l2 = lane_id(), g2 = l2 >> 4;
      LAS float* em = (LAS float*)(lds + LDS_EXM + HALF * 1536 + ((GQA ? 0 : hh) * 16 + (l2 & 15)) * 48);
      if constexpr (C == 0) __builtin_amdgcn_ds_fmaxf(em + g2, red4<OpMax>(mx[0], mx[1], mx[2], mx[3]), 0, 0, false);
      if constexpr (C == 1 && GQA) __builtin_amdgcn_ds_fmaxf(em + (g2 & 1) * 192 + 4, red2<OpMax>(mx[4], mx[5 * MT - 1]), 0, 0, false);
      if constexpr (C == 1 && !GQA) __builtin_amdgcn_ds_fmaxf(em + 4, red2<OpMax>(mx[4], mx[4]), 0, 0, false);
      if constexpr (C == 2 && GQA) __builtin_amdgcn_ds_fmaxf(em + 192 + g2, red4<OpMax>(mx[5], mx[6], mx[7], mx[8]), 0, 0, false);
    };
    auto rmax = [&](auto HALF) {
      LAS float* em = (LAS float*)(lds + LDS_EXM + HALF * 1536 + ((GQA ? 0 : hh) * 16 + (lane_id() & 15)) * 48);
      sfor<0, MT>([&](auto H) {
        const f32x4 v = *(LAS const f32x4*)(em + H * 192);
        sfor<0, 4>([&](auto T) { mx[H * 5 + T] = v[T + 0]; });
        mx[H * 5 + 4] = em[H * 192 + 4];
      });
    };
    wait_vm<HELD ? 14 : 4>();
    sfor<0, 4>([&](auto I) { rdk(IC<0>{}, I); });
    sfor<0, 5>([&](auto T) { rdq(IC<0>{}, T); });
    if constexpr (!SAFE) {
      set_m0(ring);
      need(fa[0]);
      sfor<0, 4>([&](auto I) { piece(IC<2>{}, I); });
    }
    sfor<0, 32>([&](auto Z) {
      constexpr int z = Z, gi = z / NSUB, sub = z % NSUB, e = sub / MT, h = sub % MT;
      SBAR();
      STAMP2(2, z);
      if constexpr (z == 0 || (SAFE && z == 20)) prio_reset();
      if constexpr (SAFE ? (z == 13 || z == 28) : z == PSF) prio_yield(hh);
      if constexpr (SAFE && z == 19) { bar(); rmax(IC<0>{}); }
      if constexpr (z == (NG - (SAFE ? 2 : 3)) * NSUB) vv = voff_spl(lane_id(), 512);
      if constexpr (sub == (SAFE ? NSUB - 1 : 0) && gi + 1 < NG) wait_vm<(HELD && gi == 0) ? 14 : 4>();
      sfor<0, 10>([&](auto I) {
        constexpr int i = I, t = i >> 1, u = i & 1;
        constexpr int kq = GQA ? 2 * (gi >> 2) + u : (gi >> 2), k = GQA ? 5 * h + t : i;
        sc[kq][k] = MFMA(fa[gi & 1][2 * e + u], fb[t], ((gi & 3) == 0 && e == 0) ? ZERO4 : sc[kq][k]);
        if constexpr (u == 1 && z + 1 < 32) rdq(IC<z + 1>{}, IC<t>{});
        if constexpr (SAFE) {
          if constexpr (sub == 0 && i == 1) { set_m0(ring + (gi & 1) * 4096); need(fa[gi & 1]); }
          if constexpr (sub == 0 && i >= 2 && i < 6) piece(IC<gi + 2>{}, IC<(i - 2) & 3>{});
          if constexpr (sub == NSUB - 1 && i >= 6 && gi + 1 < NG) rdk(IC<gi + 1>{}, IC<(i - 6) & 3>{});
        } else if constexpr (gi + 1 < NG) {
          if constexpr (sub == 0 && i < 4) rdk(IC<gi + 1>{}, IC<i & 3>{});
          if constexpr (sub == 0 && i == 5) set_m0(ring + ((gi + 1) & 1) * 4096);
          if constexpr (sub == 0 && i == 6) need(fa[(gi + 1) & 1]);
          if constexpr (sub == 0 && i >= 6) piece(IC<gi + 3>{}, IC<(i - 6) & 3>{});
        }
        if constexpr (SAFE) {
          if constexpr (z == 16 || z == 17) qmax(IC<(z - 16) & 1>{}, I);
          if constexpr (z == 18 && (i == 1 || i == 4 || i == 7)) xmax(IC<0>{}, IC<i / 3>{});
          if constexpr (z == 19 && i >= 5) sfor<0, MT>([&](auto H) { nm[H * 5 + i - 5] = mul1(mx[H * 5 + i - 5], -SCL); });
          if constexpr (z >= 20) { constexpr int x = (z - 20) * 10 + i; sfor<(3 * x + 1) / 2, (3 * x + 4) / 2>(mop); }
          if constexpr (!GQA && z == 26) qmax(IC<2>{}, I);
        } else if constexpr (GQA) {
          if constexpr (z >= 17 && z < 31) fop(IC<(z >= 17 ? z - 17 : 0) * 10 + i>{});
        } else {
          if constexpr (z >= 9 && (z & 7) >= 1) fop(IC<(z >= 9 ? (z / 8 - 1) * 70 + ((z & 7) - 1) * 10 + i : 0)>{});
        }
        SBAR();
      });
    });
    STAMP2(2, 32);
    STAMP(ST + 3);
    if constexpr (SAFE) {
      sfor<4 - MT, 4>([&](auto KQ) { sfor<0, 10>([&](auto K) { qmax(KQ, K); }); });
      sfor<0, 3>([&](auto C) { xmax(IC<1>{}, C); });
    }
    pw = (GQA ? (w >> 1) * 5120 + (w & 1) * 8 : hh * 20480 + cq * 5120) + lane_id() * 16;
    bar();
    if constexpr (HELD) { if (w == 0) arrive<1>(p, lds, b, j); }
    if constexpr (SAFE) rmax(IC<1>{});
    sfor<0, 10>([&](auto K) { pput(IC<0>{}, K, pw); });
    sfor<0, PRE1>([&](auto K) { pput(IC<1>{}, K, pw); });
    if constexpr (SAFE) {
      bool any = false;
      sfor<0, 5 * MT>([&](auto R) { any |= __builtin_fmaf(mx[R], SCL, nm[R]) > GROW; });
      grown = __builtin_amdgcn_ballot_w64(any) != 0;
      if (grown) {
        sfor<0, 5 * MT>([&](auto R) {
          const float d = __builtin_fmaf(mx[R], SCL, nm[R]);
          if (d > GROW) { ls[R] *= __builtin_amdgcn_exp2f(-d); nm[R] = -SCL * mx[R]; }
        });
      }
      sfor<180, 216>(mop);
    }
  }
  STAMP(ST + 4);

  unsigned redo = 0;
  const unsigned wwin = hh ? 512 + (cq * 96 & ~63) : w * 128;
  const rsrc_t rO = mk_rsrc(p.wo[P], 512 * j * 896 + wwin, W_BYTES - (512 * j * 896 + wwin));
  {
    const int l = lane_id(), g = l >> 4, q = (l >> 2) & 3;
    const unsigned vv = voff_spl(l, 512);
    const unsigned a0 = LDS_RING + (GQA ? cq : w) * 8192 + (4 * g + q) * 128 + (((q >> 1) | ((g & 1) << 1)) * 32) + 8 * (l & 3);
    const unsigned p0 = hh * 20480 + l * 16;
    auto rda = [&](auto S, auto U) {
      lds_t a = lds + (a0 ^ (U * 32)) + (GQA ? (S & 3) * 2048 : (S & 1) * 4096);
      fa[S & 1][U] = ldtr(a, a + (GQA ? 32768 : 2048));
    };
    auto rdb = [&](auto S, auto T) { fb[T] = ld128(lds + p0 + ((S >> 2) & 1) * 40960 + ((S & 3) * 5 + T) * 1024); };
    unsigned ov = 0, ow = 0;
    auto piece = [&](auto G, auto I) {
      if constexpr (G < 16) dmap<true, I>(rV, vv, (G * 32 + I * 8) * 512);
      else dmap<false, I>(rO, ov, ((G - 16) * (GQA ? 16 : 32) + I * 8) * 896);
    };
    constexpr auto TW = [](int s) -> int { return s == 12; };
    unsigned seen = 0;
    bool bad = false;
    float rl[5];
    f32x4 lv[2];
    auto oput = [&](auto T, auto H) {
      const f32x4 u = acc[T][2 * H], v = acc[T][2 * H + 1];
      const float r = rl[T];
      *(LAS u32x4*)(lds + ow + H * 5120 + T * 1024) = u32x4{pk2(mul1(u[0], r), mul1(u[1], r)), pk2(mul1(u[2], r), mul1(u[3], r)),
                                                            pk2(mul1(v[0], r), mul1(v[1], r)), pk2(mul1(v[2], r), mul1(v[3], r))};
    };
    wait_vm_bar<GQA ? 6 : 4>();
    STAMP(ST + 5);
    sfor<0, 4>([&](auto U) { rda(IC<0>{}, U); });
    sfor<0, 5>([&](auto T) { rdb(IC<0>{}, T); });
    if constexpr (!GQA) { set_m0(ring); need(fa[0]); sfor<0, 4>([&](auto I) { piece(IC<2>{}, I); }); }
    sfor<0, 16>([&](auto S) {
      constexpr int s = S;
      SBAR();
      STAMP2(3, s);
      if constexpr (s == 0) prio_reset();
      if constexpr (GQA) {
        constexpr int nv = 4 + TW(s - 1) + TW(s - 2) + TW(s - 3);
        if constexpr ((s & 3) == 3) wait_vm_bar<nv>();
        else { need(fa[s & 1]); if constexpr (ABL & 2) wait_vm<nv>(); else wait_vm_bar_raw<nv>(); }
      } else {
        if constexpr (s == 15) bar();
        else { if constexpr ((s & 3) == 3 && !(ABL & 2)) bar(); wait_vm<4 + TW(s - 1) + TW(s - 2)>(); }
      }
      if constexpr (SAFE && s == 8) {
        if (grown) {
          LAS float* em = (LAS float*)(lds + LDS_EXM + (hh * 16 + (lane_id() & 15)) * 48);
          sfor<0, 5>([&](auto T) {
            const float d = __builtin_fmaf(em[384 + T], SCL, mul1(em[T + 0], -SCL));
            const float a = d > GROW ? __builtin_amdgcn_exp2f(-d) : 1.f;
            sfor<0, 4>([&](auto U) { acc[T][U] *= a; });
          });
        }
      }
      if constexpr (!SAFE && s == 14) { if (__builtin_amdgcn_ballot_w64(bad) != 0) *(LAS unsigned*)(lds + LDS_BAD) = 1u; }
      if constexpr (!SAFE && s == 15) redo = *(LAS const unsigned*)(lds + LDS_BAD);
      if constexpr (s == 11) ov = voff_spl(lane_id(), 896);
      if constexpr (s == 15) ow = hh * 40960 + cq * 10240 + lane_id() * 16;
      sfor<0, 20>([&](auto I) {
        constexpr int i = I, t = i >> 2, u = i & 3;
        acc[t][u] = MFMA(fa[s & 1][u], fb[t], s == 0 ? ZERO4 : acc[t][u]);
        if constexpr (GQA) {
          if constexpr (i == 0) set_m0(ring + (s & 3) * 2048);
          if constexpr (i == 1 || i == 2) piece(IC<s + 4>{}, IC<i - 1>{});
          if constexpr (s < 15 && i >= 4 && i < 7) rda(IC<s + 1>{}, IC<i - 4>{});
          if constexpr (s < 15 && i == 8) rda(IC<s + 1>{}, IC<3>{});
        } else if constexpr (s < 15) {
          if constexpr (i < 4) rda(IC<s + 1>{}, IC<i & 3>{});
          if constexpr (i == 5) set_m0(ring + ((s + 1) & 1) * 4096);
          if constexpr (i == DM0) need(fa[(s + 1) & 1]);
          if constexpr (i >= DM0 && i < DM0 + 4) piece(IC<s + 3>{}, IC<(i - DM0) & 3>{});
        }
        if constexpr (s < 15 && u == 3) rdb(IC<s + 1>{}, IC<t>{});
        if constexpr (i >= 1 && i < 19) {
          constexpr int x = 18 * s + i - 1, f0 = GQA ? 140 : 210;
          if constexpr (SAFE && 216 + x < 360) mop(IC<(216 + x) % 360>{});
          if constexpr (!SAFE && f0 + x < 280 && !(ABL & 4)) fop(IC<(f0 + x) % 280>{});
        }
        if constexpr (!(ABL & 1) && s == 0 && i + PRE1 < 10) pput(IC<1>{}, IC<(i + PRE1) % 10>{}, pw);
        if constexpr (!(ABL & 1) && s >= 3 && s <= 10 && (i == 5 || i == 10 || i == 14)) {
          constexpr int q4 = (s - 3) & 3, k = q4 * 3 - (q4 > 2) + (i == 5 ? 0 : i == 10 ? 1 : 2);
          if constexpr (k < 10 && !(q4 == 2 && i == 14)) pput(IC<s < 7 ? 2 : 3>{}, IC<k % 10>{}, pw);
        }
        if constexpr (s == 10 && (i == 3 || i == 9 || i == 15)) {
          const int l2 = lane_id();
          LAS float* el = (LAS float*)(lds + LDS_EXL + ((GQA ? 0 : hh) * 80 + (l2 & 15)) * 48 + (GQA ? w : cq) * 4);
          if constexpr (i == 3) el[(l2 >> 4) * 192] = red4<OpAdd>(ls[0], ls[1], ls[2], ls[3]);
          if constexpr (i == 9 && GQA) el[768 + ((l2 >> 4) & 1) * 960] = red2<OpAdd>(ls[4], ls[5 * MT - 1]);
          if constexpr (i == 9 && !GQA) el[768] = red2<OpAdd>(ls[4], ls[4]);
          if constexpr (i == 15 && GQA) el[960 + (l2 >> 4) * 192] = red4<OpAdd>(ls[5], ls[6], ls[7], ls[8]);
        }
        if constexpr (TW(s) && i == 14) { seen = __hip_atomic_load(sync_word<LAST ? 1 : 0>(p, b, j), RLX); asm volatile("" ::: "memory"); }
        if constexpr ((s == 12 || s == 13) && (i % 8) == 1 && (s - 12) * 3 + i / 8 < 5) {
          lds_t e = lds + LDS_EXL + (hh * 80 + ((s - 12) * 3 + i / 8) * 16 + (lane_id() & 15)) * 48;
          lv[0] = *(LAS const f32x4*)e;
          if constexpr (GQA) lv[1] = *(LAS const f32x4*)(e + 16);
        }
        if constexpr ((s == 12 || s == 13) && i == (i / 8) * 8 + (i < 16 ? 6 : 3) && (s - 12) * 3 + i / 8 < 5) {
          float sum = ((lv[0][0] + lv[0][1]) + lv[0][2]) + lv[0][3];
          if constexpr (GQA) sum = (((sum + lv[1][0]) + lv[1][1]) + lv[1][2]) + lv[1][3];
          rl[((s - 12) * 3 + i / 8) % 5] = __builtin_amdgcn_rcpf(sum);
          if constexpr (!SAFE) bad |= !(sum >= SUM_LO && sum <= SUM_HI);
        }
        if constexpr (!(ABL & 8) && s == 15 && i >= 6 && (i & 1) == 0) oput(IC<(i - 6) / 4>{}, IC<((i - 6) / 2) % 2>{});
        SBAR();
      });
    });
    STAMP2(3, 16);
    STAMP(ST + 6);
    sfor<7, 10>([&](auto I) { oput(IC<I / 2>{}, IC<I % 2>{}); });
    settle<LAST ? 1 : 0>(p, lds, b, j, seen);
    bar();
  }
  STAMP(ST + 7);
  return __builtin_amdgcn_readfirstlane(redo) != 0;
}

__device__ __forceinline__ void issue_wq01(const Params& p, int P, int j, int w, unsigned lds0) {
  const rsrc_t rW = mk_rsrc(p.wq[P], (512 * j + 64 * w) * 2, W_BYTES - (512 * j + 64 * w) * 2);
  const unsigned we = voff_nat(lane_id(), 8192);
  sfor<0, 2>([&](auto G) {
    set_m0(lds0 + LDS_RING + w * 8192 + G * 4096);
    sfor<0, 4>([&](auto I) { dmap<false, I>(rW, we ^ ((I & 1) * 64), (G * 32 + I * 8) * 8192); });
  });
}

template <int P, bool LAST, int ST>
__device__ __forceinline__ void run_problem(const Params& p, lds_t lds, unsigned lds0, int b, int j, int w, u32x4 (&held)[10]) {
  if (__builtin_expect((front<P, LAST, ST, false>(p, lds, lds0, b, j, w, held) && !ABL) || FORCE_SAFE, 0)) {
    wait_vm_bar<0>();
    issue_x(p, P, b, w, lds0);
    issue_wq01(p, P, j, w, lds0);
    wait_vm_bar<0>();
    front<P, LAST, 30, true>(p, lds, lds0, fresh(b), fresh(j), fresh(w), held);
  }
  b = fresh(b); j = fresh(j); w = fresh(w);
  const int hh = w >> 2, cq = w & 3;
  const unsigned ring = lds0 + LDS_RING + w * 8192;
  f32x4 acc[5][4];
  bf16x8 fa[2][4], fb[5];
  const unsigned wwin = hh ? 512 + (cq * 96 & ~63) : w * 128;
  const rsrc_t rO = mk_rsrc(p.wo[P], 512 * j * 896 + wwin, W_BYTES - (512 * j * 896 + wwin));

  RedRows red;
  auto oproj = [&](auto NTILE) {
    constexpr int NT = NTILE, NS = 5 * NT, D0 = NT == 4 ? DM0 : 5;
    const rsrc_t rN = mk_rsrc(p.wq[1 - P], (512 * j + 64 * w) * 2, W_BYTES - (512 * j + 64 * w) * 2);
    const int l = lane_id(), g = l >> 4, q = (l >> 2) & 3;
    const unsigned ov = voff_spl(l, 896), we = voff_nat(l, 8192);
    auto piece = [&](auto G, auto I) {
      if constexpr (G < 16) dmap<false, I>(rO, ov, (G * 32 + I * 8) * 896);
      else dmap<false, I>(rN, we ^ ((I & 1) * 64), ((G - 16) * 32 + I * 8) * 8192);
    };
    unsigned a0[NT], o1 = l * 16 + 40960;
    sfor<0, NT>([&](auto U) {
      a0[U] = LDS_RING + w * 8192 + (4 * g + q) * 128 + ((((q >> 1) | ((g & 1) << 1)) ^ (U + (NT == 3 ? cq & 1 : 0))) * 32) + 8 * (l & 3);
    });
    asm volatile("" : "+v"(o1));
    auto rda = [&](auto S, auto U) { lds_t a = lds + a0[U] + (S & 1) * 4096; fa[S & 1][U] = ldtr(a, a + 2048); };
    auto rdb = [&](auto S, auto T) { fb[T] = ld128(lds + (S < 8 ? l * 16 : o1) + ((S & 7) * 5 + T) * 1024); };
    auto needa = [&](auto X) {
      if constexpr (NT == 4) need(fa[X]);
      else need3(fa[X]);
    };
    const rsrc_t rS = mk_rsrc(p.slab, ((P * 32 + b) * 8 + j) * SLAB + (NT == 4 ? w * 10240 : 40960 + cq * 8192), 10240);
    auto sput = [&](auto R) {
      constexpr int r = R;
      constexpr int tu = NT == 4 ? r / 2 : r < 5 ? r : 2 * (r - 5), cu = NT == 4 ? 2 * (r & 1) : r < 5 ? 0 : 2;
      constexpr int tv = NT == 4 ? r / 2 : r < 5 ? r : r == 7 ? 4 : 2 * (r - 5) + 1, cv = NT == 4 ? 2 * (r & 1) + 1 : r < 5 ? 1 : 2;
      const f32x4 u = acc[tu][cu], v = acc[tv][cv];
      auto r0 = __builtin_amdgcn_permlane16_swap(pk2(u[0], u[1]), pk2(v[0], v[1]), false, false);
      auto r1 = __builtin_amdgcn_permlane16_swap(pk2(u[2], u[3]), pk2(v[2], v[3]), false, false);
      if constexpr (DEFER && !LAST) held[r] = u32x4{r0[0], r1[0], r0[1], r1[1]};
      else __builtin_amdgcn_raw_buffer_store_b128(u32x4{r0[0], r1[0], r0[1], r1[1]}, rS, l * 16 + r * 1024, 0, SAUX  );
    };
    wait_vm<4>();
    sfor<0, NT>([&](auto U) { rda(IC<0>{}, U); });
    sfor<0, 5>([&](auto T) { rdb(IC<0>{}, T); });
    set_m0(ring);
    needa(IC<0>{});
    sfor<0, 4>([&](auto I) { piece(IC<2>{}, I); });
    sfor<0, 16>([&](auto S) {
      constexpr int s = S;
      SBAR();
      STAMP2(5, s);
      if constexpr (s == 0) prio_reset();
      if constexpr (s == PO) prio_yield(hh);
      if constexpr (s < 15) wait_vm<4>();
      else if constexpr (!LAST) { bar(); issue_x<X2NT != 0>(p, 1 - P, b, w, lds0); }
      sfor<0, NS>([&](auto I) {
        constexpr int i = I, t = i / NT, u = i % NT;
        acc[t][u] = MFMA(fa[s & 1][u], fb[t], s == 0 ? ZERO4 : acc[t][u]);
        if constexpr (s < 15) {
          if constexpr (i < NT) rda(IC<s + 1>{}, IC<i % NT>{});
          if constexpr (u == NT - 1) rdb(IC<s + 1>{}, IC<t>{});
          if constexpr (!(LAST && s >= 13)) {
            if constexpr (i == D0 - 2) set_m0(ring + ((s + 1) & 1) * 4096);
            if constexpr (i == D0) needa(IC<(s + 1) & 1>{});
            if constexpr (i >= D0 && i < D0 + 4) piece(IC<s + 3>{}, IC<(i - D0) & 3>{});
          }
          if constexpr (LAST && s >= 13 && i >= 3 && i < 15 && i % 3 == 0) red_load<0, (4 * (s - 13) + i / 3 - 1) & 7>(red, p, 1 - P, b, j, w);
        } else if constexpr (NT == 4) {
          if constexpr (i >= 6 && (i & 1) == 0) sput(IC<(i - 6) / 2>{});
        } else {
          if constexpr (i == 5 || i == 8 || i == 11 || i == 14) sput(IC<(i - 5) / 3>{});
          if constexpr (i == 10) sput(IC<5>{});
        }
        SBAR();
      });
    });
    STAMP2(5, 16);
    STAMP(ST + 8);
    if constexpr (NT == 4) sfor<7, 10>(sput);
    else { sput(IC<6>{}); sput(IC<4>{}); sput(IC<7>{}); }
    if constexpr (DEFER && !LAST && NT == 3) held[8] = held[9] = held[7];
    if constexpr (!LAST) wait_vm_bar<DEFER ? 0 : NT == 4 ? 10 : 8>();
  };
  if (hh == 0) oproj(IC<4>{});
  else oproj(IC<3>{});
  STAMP(ST + 9);
  if constexpr (LAST) {
    wait_vm<0>();
    STAMP(21);
    bar();
    STAMP(22);
    if (w == 0) arrive<2>(p, lds, b, j);
    if (w == 7) sfor<0, 8>([&](auto J) { red_load<1, J>(red, p, 1 - P, b, j, w); });
    red_store(red, p, 1 - P, b, j, w);
    if (w == 0) settle<2>(p, lds, b, j, __hip_atomic_load(sync_word<2>(p, b, j), RLX));
    bar();
    STAMP(23);
    sfor<0, 8>([&](auto J) { red_load<0, J>(red, p, P, b, j, w); });
    if (w == 7) sfor<0, 8>([&](auto J) { red_load<1, J>(red, p, P, b, j, w); });
    red_store(red, p, P, b, j, w);
  }
}

__global__ __launch_bounds__(512, 2) void fused_attn_kernel(Params p) {
  extern __shared__ __attribute__((aligned(16))) char smem[];
  lds_t lds = (lds_t)smem;
  const unsigned lds0 = (unsigned)(uintptr_t)lds;
  const int w = __builtin_amdgcn_readfirstlane(threadIdx.x >> 6);
  const int j = blockIdx.x & 7, b = blockIdx.x >> 3;
  const int first = b & 1;
  STAMP(0);
  {
    if constexpr (CODEW) {
      unsigned long long pc;
      asm volatile("s_getpc_b64 %0" : "=s"(pc));
      warm(lds0, mk_rsrc((const void*)(pc & ~127ull), (b * 8 + w) * 5 * 128, 4 * 128 + 4), lane_id() * 128);
    }
    unsigned found = 0;
    if (w == 7) found = __hip_atomic_load(flag_word(p, b, lane_id() & 7), RLX);
    asm volatile("" ::: "memory");
    issue_x(p, first, b, w, lds0);
    asm volatile("s_barrier" ::: "memory");
    const int l = lane_id();
    issue_wq01(p, first, j, w, lds0);
    const unsigned id = (w & 3) * 64 + l;
    if (w < 4) warm(lds0, mk_rsrc(p.wq[first], 0, W_BYTES), ((b >> 1) * 28 + (id >> 3)) * 8192 + 1024 * j + (id & 7) * 128);
    else warm(lds0, mk_rsrc(p.wo[first], 0, W_BYTES), 512 * j * 896 + (b >> 1) * 28672 + id * 128);
    if constexpr (XW) warm(lds0, mk_rsrc(p.x[1 - first], b * X_BYTES + (j * 70 + w * 9) * 128, w == 7 ? 6 * 128 + 4 : 8 * 128 + 4), l * 128);
    wait_vm_bar<9 + XW>();
    if (w == 7) {
      *(LAS unsigned*)(lds + LDS_OLD + (l & 7) * 4) = found;
      if (l < 8) __hip_atomic_store(ack_word(p, b, l, j), found + 1u, RLX);
    }
  }
  STAMP(10);
  u32x4 held[10];
  if (first == 0) {
    run_problem<0, false, 0>(p, lds, lds0, fresh(b), fresh(j), fresh(w), held);
    run_problem<1, true, 10>(p, lds, lds0, fresh(b), fresh(j), fresh(w), held);
  } else {
    run_problem<1, false, 0>(p, lds, lds0, fresh(b), fresh(j), fresh(w), held);
    run_problem<0, true, 10>(p, lds, lds0, fresh(b), fresh(j), fresh(w), held);
  }
  STAMP(20);
}

extern "C" void kernel_launch(void* const* d_in, const int* in_sizes, int n_in, void* d_out, int out_size, void* d_ws,
                              size_t ws_size, hipStream_t stream) {
  Params p;
  for (int i = 0; i < 2; ++i) {
    p.x[i] = d_in[5 * i + 0]; p.k[i] = d_in[5 * i + 1]; p.v[i] = d_in[5 * i + 2]; p.wq[i] = d_in[5 * i + 3]; p.wo[i] = d_in[5 * i + 4];
  }
  p.sync = (unsigned*)d_ws;
  p.slab = WSTAIL ? (char*)d_ws + ((ws_size - 512 * (size_t)SLAB) & ~(size_t)4095) : (char*)d_ws + 262144;
  p.out = d_out;
  p.diag = (unsigned long long*)((char*)d_ws + (64u << 20));
  (void)hipFuncSetAttribute((const void*)fused_attn_kernel, hipFuncAttributeMaxDynamicSharedMemorySize, LDS_TOTAL);
  fused_attn_kernel<<<256, 512, LDS_TOTAL, stream>>>(p);
}
```

```cpp
#include <hip/hip_runtime.h>
#include <hip/hip_bf16.h>
#include <stdint.h>
#include <type_traits>

typedef short bf16x8 __attribute__((ext_vector_type(8)));
typedef short s16x4 __attribute__((ext_vector_type(4)));
typedef float f32x4 __attribute__((ext_vector_type(4)));
typedef float f32x2 __attribute__((ext_vector_type(2)));
typedef __bf16 bf16x2 __attribute__((ext_vector_type(2)));
typedef unsigned u32x4 __attribute__((ext_vector_type(4)));
typedef unsigned u32x2 __attribute__((ext_vector_type(2)));
#define LAS __attribute__((address_space(3)))
typedef __amdgpu_buffer_rsrc_t rsrc_t;
typedef LAS char* lds_t;

#define DIAG 0
#define DLAST 1
#define PO 12
#define FORCE_SAFE 0
#define PRE1F 0
#define PSF 22
#define ABL 0
#define SAUX 16
#define LAUX 16
#define XW 0
#define ONT 0
#define KVPOL "nt"
#define WPOL ""
#define X2NT 0
#define WSTAIL 0
#define CODEW 1
#define K1E 4
#define FQ 1
#define PBB 0
#define PBO 0
#define FAKEKV 0

constexpr int LDS_RING = 81920;
constexpr int LDS_EXM = 147456;
constexpr int LDS_EXL = 151552;
constexpr int LDS_DUMMY = 159232;
constexpr int LDS_OLD = 159488;
constexpr int LDS_BAD = LDS_OLD + 32;
constexpr int LDS_TOTAL = DIAG ? 163840 : 159552;
constexpr unsigned X_BYTES = 80 * 448 * 2;
constexpr unsigned W_BYTES = 448 * 4096 * 2;
constexpr unsigned KV_HEAD = 512 * 256 * 2;
constexpr unsigned SLAB = 72 * 1024;
constexpr float SCL = 0.0625f * 1.4426950408889634f;
constexpr float SUM_LO = 0x1p-60f, SUM_HI = 0x1p60f;
constexpr float GROW = 20.f;

template <int I, int N, class F>
__device__ __forceinline__ void sfor(F&& f) {
  if constexpr (I < N) { f(std::integral_constant<int, I>{}); sfor<I + 1, N>(f); }
}
#define SBAR() __builtin_amdgcn_sched_barrier(0)
__device__ __forceinline__ f32x4 MFMA(bf16x8 a, bf16x8 b, f32x4 c) {
  f32x4 d = __builtin_amdgcn_mfma_f32_16x16x32_bf16(a, b, c, 0, 0, 0);
  asm volatile("" : "+v"(d));
  return d;
}
template <int N> __device__ __forceinline__ void wait_vm() { asm volatile("s_waitcnt vmcnt(%0)" ::"i"(N) : "memory"); }
__device__ __forceinline__ void bar() { asm volatile("s_waitcnt lgkmcnt(0)\n\ts_barrier" ::: "memory"); }
template <int N> __device__ __forceinline__ void wait_vm_bar_raw() { asm volatile("s_waitcnt vmcnt(%0)\n\ts_barrier" ::"i"(N) : "memory"); }
template <int N> __device__ __forceinline__ void wait_vm_bar() { asm volatile("s_waitcnt vmcnt(%0) lgkmcnt(0)\n\ts_barrier" ::"i"(N) : "memory"); }
__device__ __forceinline__ void prio_reset() { asm volatile("s_setprio 1"); }
__device__ __forceinline__ void prio_yield(int hh) { asm volatile("s_cmp_lg_u32 %0, 0\n\ts_cbranch_scc1 1f\n\ts_setprio 0\n1:" ::"s"(hh) : "scc"); }
__device__ __forceinline__ int lane_id() {
  int l; asm volatile("v_mbcnt_lo_u32_b32 %0, -1, 0\n\tv_mbcnt_hi_u32_b32 %0, -1, %0" : "=v"(l)); return l;
}
__device__ __forceinline__ int fresh(int x) { asm volatile("" : "+s"(x)); return x; }
__device__ __forceinline__ unsigned pk2(float a, float b) {
  return __builtin_bit_cast(unsigned, __builtin_convertvector(f32x2{a, b}, bf16x2));
}
__device__ __forceinline__ rsrc_t mk_rsrc(const void* p, unsigned off, unsigned bytes) {
  return __builtin_amdgcn_make_buffer_rsrc((void*)((const char*)p + off), (short)0, (int)bytes, 0x00020000);
}
__device__ __forceinline__ void need(const bf16x8 (&f)[4]) { asm volatile("" ::"v"(f[0]), "v"(f[1]), "v"(f[2]), "v"(f[3]) : "memory"); }
__device__ __forceinline__ void need3(const bf16x8 (&f)[4]) { asm volatile("" ::"v"(f[0]), "v"(f[1]), "v"(f[2]) : "memory"); }
__device__ __forceinline__ void set_m0(unsigned m0v) { asm volatile("s_mov_b32 m0, %0\n\ts_nop 0" ::"s"(m0v) : "memory"); }
template <bool NT, int I>
__device__ __forceinline__ void dmap(rsrc_t r, unsigned v, unsigned src) {
  if constexpr (NT && !FAKEKV) asm volatile("buffer_load_dwordx4 %0, %1, %2 offen offset:%3 " KVPOL " lds" ::"v"(v), "s"(r), "s"(src - 1024u * I), "i"(1024 * I) : "memory");
  else asm volatile("buffer_load_dwordx4 %0, %1, %2 offen offset:%3 lds" ::"v"(v), "s"(r), "s"(src - 1024u * I), "i"(1024 * I) : "memory");
}
__device__ __forceinline__ void warm(unsigned lds0, rsrc_t r, unsigned v) {
  asm volatile("s_mov_b32 m0, %0\n\ts_nop 0\n\tbuffer_load_dword %1, %2, 0 offen " WPOL " lds" ::"s"(lds0 + LDS_DUMMY), "v"(v), "s"(r) : "memory");
}
extern "C" __device__ float llvm_fmul_legacy(float, float) __asm("llvm.amdgcn.fmul.legacy");
__device__ __forceinline__ float mul1(float a, float b) { return llvm_fmul_legacy(a, b); }
__device__ __forceinline__ bf16x8 ld128(lds_t p) { return *(LAS const bf16x8*)p; }
__device__ __forceinline__ bf16x8 ldtr(lds_t p0, lds_t p1) {
  s16x4 lo = __builtin_amdgcn_ds_read_tr16_b64_v4i16((LAS s16x4*)p0);
  s16x4 hi = __builtin_amdgcn_ds_read_tr16_b64_v4i16((LAS s16x4*)p1);
  return __builtin_shufflevector(lo, hi, 0, 1, 2, 3, 4, 5, 6, 7);
}
__device__ __forceinline__ float max3(float a, float b, float c) { return __builtin_fmaxf(__builtin_fmaxf(a, b), c); }
struct OpMax { static __device__ __forceinline__ float f(float a, float b) { return max3(a, b, b); } };
struct OpAdd { static __device__ __forceinline__ float f(float a, float b) { return a + b; } };
template <class Op> __device__ __forceinline__ float sw16(float a, float b) {
  auto r = __builtin_amdgcn_permlane16_swap(__float_as_uint(a), __float_as_uint(b), false, false);
  return Op::f(__uint_as_float(r[0]), __uint_as_float(r[1]));
}
template <class Op> __device__ __forceinline__ float sw32(float a, float b) {
  auto r = __builtin_amdgcn_permlane32_swap(__float_as_uint(a), __float_as_uint(b), false, false);
  return Op::f(__uint_as_float(r[0]), __uint_as_float(r[1]));
}
template <class Op> __device__ __forceinline__ float red4(float a, float b, float c, float d) { return sw32<Op>(sw16<Op>(a, b), sw16<Op>(c, d)); }
template <class Op> __device__ __forceinline__ float red2(float a, float b) { const float x = sw16<Op>(a, b); return sw32<Op>(x, x); }

__device__ __forceinline__ unsigned voff_nat(int l, unsigned pitch) { return (l >> 3) * pitch + 16 * ((l & 7) ^ (((l >> 4) & 1) << 1)); }
__device__ __forceinline__ unsigned voff_spl(int l, unsigned pitch) { return (l >> 3) * pitch + 16 * ((l & 7) ^ (((l >> 4) & 3) << 1)); }
__device__ __forceinline__ unsigned voff_k(int l) { return (l >> 3) * 512 + 16 * ((l & 7) ^ ((l >> 4) & 3)); }

struct Params {
  const void *x[2], *k[2], *v[2], *wq[2], *wo[2];
  void* slab;
  unsigned* sync;
  void* out;
  unsigned long long* diag;
};

#define STAMP(i)
#define STAMP2(ph, i)

constexpr int DM0 = 7;
constexpr int fin_tile(int grp) { return grp == 0 ? 0 : grp == 9 ? 4 : (grp & 1) ? (grp + 1) / 2 : grp / 2 - 1; }
constexpr int fin_half(int grp) { return grp == 9 || (grp > 0 && !(grp & 1)); }
template <int V> using IC = std::integral_constant<int, V>;
constexpr f32x4 ZERO4 = {0.f, 0.f, 0.f, 0.f};

template <bool NT = false>
__device__ __forceinline__ void issue_x(const Params& p, int P, int b, int w, unsigned lds0) {
  w = fresh(w);
  const int l = lane_id();
  const unsigned v = (l >> 3) * 896 + 16 * ((l & 7) ^ ((l >> 4) & 3));
  const rsrc_t rx = mk_rsrc(p.x[P], b * X_BYTES, X_BYTES);
  sfor<0, 9>([&](auto I) {
    const unsigned pc = w * 9 + I, blk = (pc * 205) >> 11, rg = pc - blk * 10;
    set_m0(lds0 + pc * 1024);
    dmap<NT, 0>(rx, v ^ ((rg & 1) * 64), rg * 7168 + blk * 128);
  });
}

typedef __attribute__((address_space(1))) unsigned gu32;
#define RLX __ATOMIC_RELAXED, __HIP_MEMORY_SCOPE_AGENT
__device__ __forceinline__ gu32* flag_word(const Params& p, int b, int j) { return (gu32*)p.sync + (b * 8 + j) * 32; }
__device__ __forceinline__ gu32* ack_word(const Params& p, int b, int prod, int cons) { return (gu32*)p.sync + 8192 + ((b * 8 + prod) * 8 + cons) * 16; }
template <int STAGE>
__device__ __forceinline__ void arrive(const Params& p, lds_t lds, int b, int j) {
  const unsigned old = *(LAS const unsigned*)(lds + LDS_OLD + j * 4);
  if (lane_id() == 0) __hip_atomic_store(flag_word(p, b, j), old + STAGE, RLX);
}
template <int STAGE> __device__ __forceinline__ gu32* sync_word(const Params& p, int b, int j) {
  const int c = lane_id() & 7;
  return STAGE ? flag_word(p, b, c) : ack_word(p, b, j, c);
}
template <int STAGE>
__device__ __forceinline__ void settle(const Params& p, lds_t lds, int b, int j, unsigned seen) {
  const unsigned old = *(LAS const unsigned*)(lds + LDS_OLD + (STAGE ? lane_id() & 7 : j) * 4);
  for (unsigned spins = 0; spins < (1u << 22); ++spins) {
    if (__builtin_amdgcn_ballot_w64(STAGE ? seen - old - STAGE > 2u - STAGE : seen != old + 1u) == 0) break;
    __builtin_amdgcn_s_sleep(2);
    seen = __hip_atomic_load(sync_word<STAGE>(p, b, j), RLX);
  }
  asm volatile("" ::: "memory");
}
struct RedRows { u32x4 v[2][8]; };
template <int K, int J>
__device__ __forceinline__ void red_load(RedRows& r, const Params& p, int P, int b, int j, int w) {
  const rsrc_t rS = mk_rsrc(p.slab, (P * 32 + b) * 8 * SLAB, 8 * SLAB);
  r.v[K][J] = __builtin_amdgcn_raw_buffer_load_b128(rS, lane_id() * 16, J * SLAB + (9 * j + w + K) * 1024, LAUX  );
}
__device__ __forceinline__ void red_store(const RedRows& r, const Params& p, int P, int b, int j, int w) {
  const int l = lane_id(), n = l & 15, g = l >> 4;
  sfor<0, 2>([&](auto K) {
    if (K == 0 || w == 7) {
      float sm[8];
      sfor<0, 4>([&](auto I) { sm[2 * I] = __uint_as_float(r.v[K][0][I + 0] << 16); sm[2 * I + 1] = __uint_as_float(r.v[K][0][I + 0] & 0xffff0000u); });
      sfor<1, 8>([&](auto J) {
        sfor<0, 4>([&](auto I) { sm[2 * I] += __uint_as_float(r.v[K][J][I + 0] << 16); sm[2 * I + 1] += __uint_as_float(r.v[K][J][I + 0] & 0xffff0000u); });
      });
      const int f = 9 * j + w + K;
      int t, col;
      if (f < 40) { const int ww = f / 10; t = (f - ww * 10) >> 1; col = 64 * ww + 32 * (f & 1) + 16 * (g & 1); }
      else { const int rr = f & 7, c0 = 256 + 48 * ((f - 40) >> 3); t = rr < 5 ? rr : 2 * (rr - 5) + (g & 1); col = c0 + (rr < 5 ? 16 * (g & 1) : 32); }
      if (t < 5)
      {
        u32x4* dst = (u32x4*)((char*)p.out + ((size_t)(b * 80 + t * 16 + n) * 896 + P * 448 + col + 8 * (g >> 1)) * 2);
        const u32x4 val = u32x4{pk2(sm[0], sm[1]), pk2(sm[2], sm[3]), pk2(sm[4], sm[5]), pk2(sm[6], sm[7])};
        if constexpr (ONT) __builtin_nontemporal_store(val, dst); else *dst = val;
      }
    }
  });
}

template <int P  , bool LAST, int ST, bool SAFE>
__device__ __forceinline__ bool front(const Params& p, lds_t lds, unsigned lds0, int b, int j, int w) {
  constexpr bool GQA = (P == 0);
  const int bk = FAKEKV ? 0 : b;
  const int hh = w >> 2, cq = w & 3;
  const unsigned ring = lds0 + LDS_RING + w * 8192;
  f32x4 acc[5][4];
  bf16x8 fa[2][4], fb[5];

  const unsigned koff = GQA ? (bk * 8 + j) * KV_HEAD + w * 16 * 512 : (bk * 16 + 2 * j + hh) * KV_HEAD + cq * 32 * 512;
  const rsrc_t rK = mk_rsrc(p.k[P], koff, KV_HEAD - (GQA ? w * 16 * 512 : cq * 32 * 512));
  auto k_src = [](int g, int i) -> unsigned {
    return GQA ? ((g >> 2) * 256 + (i >> 1) * 128 + (i & 1) * 8) * 512 + (g & 3) * 128 : ((g >> 2) * 128 + i * 8) * 512 + (g & 3) * 128;
  };

  {
    const rsrc_t rW = mk_rsrc(p.wq[P], (512 * j + 64 * w) * 2, W_BYTES - (512 * j + 64 * w) * 2);
    const int l = lane_id(), n = l & 15, g = l >> 4, q = (l >> 2) & 3;
    const unsigned we = voff_nat(l, 8192), ke = voff_k(l);
    const unsigned a0 = LDS_RING + w * 8192 + (8 * g + q) * 128 + (((q >> 1) | ((g & 1) << 1)) * 32) + 8 * (l & 3);
    unsigned x0[2][2];
    x0[0][0] = n * 128 + 16 * (g ^ ((n >> 1) & 7)); x0[1][0] = x0[0][0] ^ 64; x0[0][1] = x0[0][0] + 40960; x0[1][1] = x0[1][0] + 40960;
    asm volatile("" : "+v"(x0[0][1]), "+v"(x0[1][1]));
    const unsigned qw = hh * 40960 + cq * 10240 + l * 16;
    float scl = SCL;
    asm volatile("" : "+s"(scl));
    auto rda = [&](auto S, auto U) { lds_t a = lds + (a0 ^ (U * 32)) + (S & 1) * 4096; fa[S & 1][U] = ldtr(a, a + 512); };
    auto ldb = [&](auto S, auto T) { return ld128(lds + x0[S & 1][S >= 8] + ((S >> 1) & 3) * 10240 + T * 2048); };
    auto piece = [&](auto G, auto I) {
      if constexpr (G < 14) dmap<false, I>(rW, we ^ ((I & 1) * 64), (G * 32 + I * 8) * 8192);
      else dmap<true, I>(rK, ke ^ ((I & 1) * 64), k_src(G - 14, I));
    };
    u32x4 cv;
    auto qput = [&](auto X) {
      constexpr int T = X / 8, H = (X / 4) & 1, c = X & 3;
      const f32x4 u = acc[T][2 * H + c / 2];
      cv[c] = SAFE ? pk2(u[2 * (c & 1)], u[2 * (c & 1) + 1]) : pk2(mul1(u[2 * (c & 1)], scl), mul1(u[2 * (c & 1) + 1], scl));
      if constexpr (c == 3) *(LAS u32x4*)(lds + qw + H * 5120 + T * 1024) = cv;
    };
    bf16x8 fb2[5];
    if constexpr (!LAST && !SAFE) wait_vm<5 + XW>();
    sfor<0, 4>([&](auto U) { rda(IC<0>{}, U); });
    sfor<0, 5>([&](auto T) { fb[T] = ldb(IC<0>{}, T); });
    set_m0(ring);
    need(fa[0]);
    sfor<0, 4>([&](auto I) { piece(IC<2>{}, I); });
    sfor<0, FQ ? 12 : 13>([&](auto S) {
      constexpr int s = S;
      SBAR();
      STAMP2(4, s);
      if constexpr (s == 0) prio_reset();
      if constexpr (s == (FQ ? 8 : 9)) prio_yield(hh);
      if constexpr (s == 0) { if constexpr (!LAST && !SAFE) wait_vm<5 + XW>(); }
      else wait_vm<(LAST && s == 6) ? 0 : 4>();
      sfor<0, 20>([&](auto I) {
        constexpr int i = I, t = i >> 2, u = i & 3;
        acc[t][u] = MFMA(fa[s & 1][u], fb[t], s == 0 ? ZERO4 : acc[t][u]);
        if constexpr (i < 4) rda(IC<s + 1>{}, IC<i & 3>{});
        if constexpr (u == 3) fb[t] = ldb(IC<s + 1>{}, IC<t>{});
        if constexpr (FQ && s == 11 && u == 1) fb2[t] = ldb(IC<13>{}, IC<t>{});
        if constexpr (i == 5) set_m0(ring + ((s + 1) & 1) * 4096);
        if constexpr (i == DM0) need(fa[(s + 1) & 1]);
        if constexpr (i >= DM0 && i < DM0 + 4) piece(IC<s + 3>{}, IC<(i - DM0) & 3>{});
        SBAR();
      });
    });
    STAMP2(4, 12);
    bar();
    if constexpr (SAFE) *(LAS f32x2*)(lds + LDS_EXM + w * 512 + l * 8) = f32x2{-INFINITY, -INFINITY};
    else *(LAS unsigned*)(lds + LDS_BAD) = 0u;
    if constexpr (!FQ) {
      sfor<0, 20>([&](auto I) {
        constexpr int i = I, t = i >> 2, u = i & 3;
        acc[t][u] = MFMA(fa[1][u], fb[t], acc[t][u]);
        if constexpr (i >= 6) { qput(IC<i >= 6 ? 2 * (i - 6) : 0>{}); qput(IC<i >= 6 ? 2 * (i - 6) + 1 : 0>{}); }
        SBAR();
      });
    } else wait_vm<4>();
    if constexpr (FQ) sfor<0, 40>([&](auto I) {
      constexpr int i = I, t = fin_tile(i >> 2), hf = fin_half(i >> 2), u = i & 3;
      acc[t][u] = MFMA(fa[hf][u], hf ? fb2[t] : fb[t], acc[t][u]);
      if constexpr (i < 4) rda(IC<13>{}, IC<i & 3>{});
      if constexpr (i == 5) set_m0(ring + 4096);
      if constexpr (i == DM0) need(fa[1]);
      if constexpr (i >= DM0 && i < DM0 + K1E) piece(IC<15>{}, IC<(i - DM0) & 3>{});
      if constexpr (i >= 13) qput(IC<i >= 13 ? i - 13 : 0>{});
      SBAR();
    });
    STAMP2(4, 13);
    STAMP2(4, 14);
    STAMP(ST + 1);
    sfor<FQ ? 27 : 28, 40>(qput);
    if constexpr (FQ) sfor<K1E, 4>([&](auto I) { piece(IC<15>{}, I); });
    bar();
    if constexpr (LAST) { if (w == 0) arrive<1>(p, lds, b, j); }
  }
  STAMP(ST + 2);

  const rsrc_t rV = GQA ? mk_rsrc(p.v[P], (bk * 8 + j) * KV_HEAD + hh * 16 * 512 + cq * 128, KV_HEAD - (hh * 16 * 512 + cq * 128))
                        : mk_rsrc(p.v[P], (bk * 16 + 2 * j + hh) * KV_HEAD + cq * 128, KV_HEAD - cq * 128);
  constexpr int MT = GQA ? 2 : 1;
  constexpr int NG = GQA ? 8 : 16;
  f32x4 sc[4][10];
  float mx[5 * MT], nm[5 * MT], ls[5 * MT], ev[4];
  u32x2 pq[4][10];
  auto mop = [&](auto ID) {
    constexpr int id = ID, kq = id / 90, k = (id / 9) % 10, part = id % 9, r = GQA ? k : (k >> 1);
    if constexpr (part == 0 || part == 2) asm volatile("" : "+v"(nm[r]));
    if constexpr (part == 0) { ev[0] = __builtin_fmaf(sc[kq][k][0], SCL, nm[r]); ev[1] = __builtin_fmaf(sc[kq][k][1], SCL, nm[r]); }
    if constexpr (part == 1) ev[0] = __builtin_amdgcn_exp2f(ev[0]);
    if constexpr (part == 2) { ev[2] = __builtin_fmaf(sc[kq][k][2], SCL, nm[r]); ev[3] = __builtin_fmaf(sc[kq][k][3], SCL, nm[r]); }
    if constexpr (part == 3) ev[1] = __builtin_amdgcn_exp2f(ev[1]);
    if constexpr (part == 4) ev[2] = __builtin_amdgcn_exp2f(ev[2]);
    if constexpr (part == 5) { if constexpr (kq == 0 && (GQA || (k & 1) == 0)) ls[r] = ev[0] + ev[1]; else ls[r] = (ls[r] + ev[0]) + ev[1]; }
    if constexpr (part == 6) ev[3] = __builtin_amdgcn_exp2f(ev[3]);
    if constexpr (part == 7) { pq[kq][k][0] = pk2(ev[0], ev[1]); ls[r] += ev[2]; }
    if constexpr (part == 8) { pq[kq][k][1] = pk2(ev[2], ev[3]); ls[r] += ev[3]; }
    if constexpr (part == 0) asm volatile("" : "+v"(ev[0]), "+v"(ev[1]));
    if constexpr (part == 1) asm volatile("" : "+v"(ev[0]));
    if constexpr (part == 2) asm volatile("" : "+v"(ev[2]), "+v"(ev[3]));
    if constexpr (part == 3) asm volatile("" : "+v"(ev[1]));
    if constexpr (part == 4) asm volatile("" : "+v"(ev[2]));
    if constexpr (part == 5) asm volatile("" : "+v"(ls[r]));
    if constexpr (part == 6) asm volatile("" : "+v"(ev[3]));
    if constexpr (part == 7) asm volatile("" : "+v"(ls[r]), "+v"(pq[kq][k][0]));
    if constexpr (part == 8) asm volatile("" : "+v"(ls[r]), "+v"(pq[kq][k][1]));
  };
  auto fop = [&](auto ID) {
    constexpr int id = ID, kq = id / 70, k = (id / 7) % 10, part = id % 7, r = GQA ? k : (k >> 1);
    if constexpr (part == 0) asm volatile("" : "+v"(sc[kq][k]));
    if constexpr (part == 0) { ev[0] = __builtin_amdgcn_exp2f(sc[kq][k][0]); asm volatile("" : "+v"(ev[0])); }
    if constexpr (part == 1) { ev[1] = __builtin_amdgcn_exp2f(sc[kq][k][1]); asm volatile("" : "+v"(ev[1])); }
    if constexpr (part == 2) { ev[2] = __builtin_amdgcn_exp2f(sc[kq][k][2]); asm volatile("" : "+v"(ev[2])); }
    if constexpr (part == 3) {
      if constexpr (kq == 0 && (GQA || (k & 1) == 0)) ls[r] = ev[0] + ev[1]; else ls[r] = (ls[r] + ev[0]) + ev[1];
      asm volatile("" : "+v"(ls[r]));
    }
    if constexpr (part == 4) { ev[3] = __builtin_amdgcn_exp2f(sc[kq][k][3]); asm volatile("" : "+v"(ev[3])); }
    if constexpr (part == 5) { pq[kq][k][0] = pk2(ev[0], ev[1]); ls[r] += ev[2]; asm volatile("" : "+v"(ls[r]), "+v"(pq[kq][k][0])); }
    if constexpr (part == 6) { pq[kq][k][1] = pk2(ev[2], ev[3]); ls[r] += ev[3]; asm volatile("" : "+v"(ls[r]), "+v"(pq[kq][k][1])); }
  };
  auto pput = [&](auto KQ, auto K, unsigned pw) {
    constexpr int kq = KQ, k = K;
    if constexpr (GQA) *(LAS u32x2*)(lds + pw + (kq & 1) * 40960 + (k / 5) * 20480 + (k % 5) * 1024) = pq[kq][k];
    else if constexpr (k & 1) *(LAS u32x4*)(lds + pw + (kq & 1) * 40960 + (k / 2) * 1024) = u32x4{pq[kq][k - 1][0], pq[kq][k - 1][1], pq[kq][k][0], pq[kq][k][1]};
  };
  bool grown = false;
  unsigned pw;
  constexpr int PRE1 = SAFE ? 10 : GQA ? PRE1F : 0;
  {
    const int l = lane_id(), n = l & 15, g = l >> 4;
    const unsigned ke = voff_k(l);
    unsigned vv = 0;
    const unsigned k0 = LDS_RING + w * 8192 + n * 128 + 16 * ((g >> 1) ^ ((n >> 1) & 7)) + 8 * (g & 1);
    const unsigned q0 = (GQA ? 0 : hh * 40960) + l * 16;
    unsigned q1 = q0 + 40960;
    asm volatile("" : "+v"(q1));
    constexpr int NSUB = 2 * MT;
    auto rdk = [&](auto G, auto I) {
      lds_t a = lds + (I & 1) * 2048 + (G & 1) * 4096;
      const s16x4 lo = *(LAS const s16x4*)(a + (k0 ^ ((I >> 1) * 64))), hi = *(LAS const s16x4*)(a + (k0 ^ ((I >> 1) * 64 + 32)));
      fa[G & 1][I] = __builtin_shufflevector(lo, hi, 0, 1, 2, 3, 4, 5, 6, 7);
    };
    auto rdq = [&](auto Z, auto T) {
      constexpr int z = Z, gi = z / NSUB, e = (z % NSUB) / MT, h = z % MT;
      fb[T] = ld128(lds + (h ? q1 : q0) + ((2 * (gi & 3) + e) * 5 + T) * 1024);
    };
    auto piece = [&](auto G, auto I) {
      if constexpr (G < NG) dmap<true, I>(rK, ke ^ ((I & 1) * 64), k_src(G, I));
      else if constexpr (!GQA) dmap<true, I>(rV, vv, ((G - NG) * 32 + I * 8) * 512);
      else dmap<true, I>(rV, vv, ((2 * (G - NG) + (I >> 1)) * 32 + (I & 1) * 8) * 512);
    };
    auto qmax = [&](auto KQ, auto K) {
      constexpr int kq = KQ, k = K, r = GQA ? k : (k >> 1);
      if constexpr ((kq & 1) == 0 && (GQA || (k & 1) == 0)) mx[r] = max3(max3(sc[kq][k][0], sc[kq][k][1], sc[kq][k][2]), sc[kq][k][3], sc[kq][k][3]);
      else mx[r] = max3(max3(mx[r], sc[kq][k][0], sc[kq][k][1]), sc[kq][k][2], sc[kq][k][3]);
      asm volatile("" : "+v"(mx[r]));
    };
    auto xmax = [&](auto HALF, auto C) {
      const int l2 = lane_id(), g2 = l2 >> 4;
      LAS float* em = (LAS float*)(lds + LDS_EXM + HALF * 1536 + ((GQA ? 0 : hh) * 16 + (l2 & 15)) * 48);
      if constexpr (C == 0) __builtin_amdgcn_ds_fmaxf(em + g2, red4<OpMax>(mx[0], mx[1], mx[2], mx[3]), 0, 0, false);
      if constexpr (C == 1 && GQA) __builtin_amdgcn_ds_fmaxf(em + (g2 & 1) * 192 + 4, red2<OpMax>(mx[4], mx[5 * MT - 1]), 0, 0, false);
      if constexpr (C == 1 && !GQA) __builtin_amdgcn_ds_fmaxf(em + 4, red2<OpMax>(mx[4], mx[4]), 0, 0, false);
      if constexpr (C == 2 && GQA) __builtin_amdgcn_ds_fmaxf(em + 192 + g2, red4<OpMax>(mx[5], mx[6], mx[7], mx[8]), 0, 0, false);
    };
    auto rmax = [&](auto HALF) {
      LAS float* em = (LAS float*)(lds + LDS_EXM + HALF * 1536 + ((GQA ? 0 : hh) * 16 + (lane_id() & 15)) * 48);
      sfor<0, MT>([&](auto H) {
        const f32x4 v = *(LAS const f32x4*)(em + H * 192);
        sfor<0, 4>([&](auto T) { mx[H * 5 + T] = v[T + 0]; });
        mx[H * 5 + 4] = em[H * 192 + 4];
      });
    };
    wait_vm<4>();
    sfor<0, 4>([&](auto I) { rdk(IC<0>{}, I); });
    sfor<0, 5>([&](auto T) { rdq(IC<0>{}, T); });
    if constexpr (!SAFE) {
      set_m0(ring);
      need(fa[0]);
      sfor<0, 4>([&](auto I) { piece(IC<2>{}, I); });
    }
    sfor<0, 32>([&](auto Z) {
      constexpr int z = Z, gi = z / NSUB, sub = z % NSUB, e = sub / MT, h = sub % MT;
      SBAR();
      STAMP2(2, z);
      if constexpr (z == 0 || (SAFE && z == 20)) prio_reset();
      if constexpr (SAFE ? (z == 13 || z == 28) : z == PSF) prio_yield(hh);
      if constexpr (SAFE && z == 19) { bar(); rmax(IC<0>{}); }
      if constexpr (z == (NG - (SAFE ? 2 : 3)) * NSUB) vv = voff_spl(lane_id(), 512);
      if constexpr (sub == (SAFE ? NSUB - 1 : 0) && gi + 1 < NG) wait_vm<4>();
      sfor<0, 10>([&](auto I) {
        constexpr int i = I, t = i >> 1, u = i & 1;
        constexpr int kq = GQA ? 2 * (gi >> 2) + u : (gi >> 2), k = GQA ? 5 * h + t : i;
        sc[kq][k] = MFMA(fa[gi & 1][2 * e + u], fb[t], ((gi & 3) == 0 && e == 0) ? ZERO4 : sc[kq][k]);
        if constexpr (u == 1 && z + 1 < 32) rdq(IC<z + 1>{}, IC<t>{});
        if constexpr (SAFE) {
          if constexpr (sub == 0 && i == 1) { set_m0(ring + (gi & 1) * 4096); need(fa[gi & 1]); }
          if constexpr (sub == 0 && i >= 2 && i < 6) piece(IC<gi + 2>{}, IC<(i - 2) & 3>{});
          if constexpr (sub == NSUB - 1 && i >= 6 && gi + 1 < NG) rdk(IC<gi + 1>{}, IC<(i - 6) & 3>{});
        } else if constexpr (gi + 1 < NG) {
          if constexpr (sub == 0 && i < 4) rdk(IC<gi + 1>{}, IC<i & 3>{});
          if constexpr (sub == 0 && i == 5) set_m0(ring + ((gi + 1) & 1) * 4096);
          if constexpr (sub == 0 && i == 6) need(fa[(gi + 1) & 1]);
          if constexpr (sub == 0 && i >= 6) piece(IC<gi + 3>{}, IC<(i - 6) & 3>{});
        }
        if constexpr (SAFE) {
          if constexpr (z == 16 || z == 17) qmax(IC<(z - 16) & 1>{}, I);
          if constexpr (z == 18 && (i == 1 || i == 4 || i == 7)) xmax(IC<0>{}, IC<i / 3>{});
          if constexpr (z == 19 && i >= 5) sfor<0, MT>([&](auto H) { nm[H * 5 + i - 5] = mul1(mx[H * 5 + i - 5], -SCL); });
          if constexpr (z >= 20) { constexpr int x = (z - 20) * 10 + i; sfor<(3 * x + 1) / 2, (3 * x + 4) / 2>(mop); }
          if constexpr (!GQA && z == 26) qmax(IC<2>{}, I);
        } else if constexpr (GQA) {
          if constexpr (z >= 17 && z < 31) fop(IC<(z >= 17 ? z - 17 : 0) * 10 + i>{});
        } else {
          if constexpr (z >= 9 && (z & 7) >= 1) fop(IC<(z >= 9 ? (z / 8 - 1) * 70 + ((z & 7) - 1) * 10 + i : 0)>{});
        }
        SBAR();
      });
    });
    STAMP2(2, 32);
    STAMP(ST + 3);
    if constexpr (SAFE) {
      sfor<4 - MT, 4>([&](auto KQ) { sfor<0, 10>([&](auto K) { qmax(KQ, K); }); });
      sfor<0, 3>([&](auto C) { xmax(IC<1>{}, C); });
    }
    pw = (GQA ? (w >> 1) * 5120 + (w & 1) * 8 : hh * 20480 + cq * 5120) + lane_id() * 16;
    bar();
    if constexpr (SAFE) rmax(IC<1>{});
    sfor<0, 10>([&](auto K) { pput(IC<0>{}, K, pw); });
    sfor<0, PRE1>([&](auto K) { pput(IC<1>{}, K, pw); });
    if constexpr (SAFE) {
      bool any = false;
      sfor<0, 5 * MT>([&](auto R) { any |= __builtin_fmaf(mx[R], SCL, nm[R]) > GROW; });
      grown = __builtin_amdgcn_ballot_w64(any) != 0;
      if (grown) {
        sfor<0, 5 * MT>([&](auto R) {
          const float d = __builtin_fmaf(mx[R], SCL, nm[R]);
          if (d > GROW) { ls[R] *= __builtin_amdgcn_exp2f(-d); nm[R] = -SCL * mx[R]; }
        });
      }
      sfor<180, 216>(mop);
    }
  }
  STAMP(ST + 4);

  unsigned redo = 0;
  const unsigned wwin = hh ? 512 + (cq * 96 & ~63) : w * 128;
  const rsrc_t rO = mk_rsrc(p.wo[P], 512 * j * 896 + wwin, W_BYTES - (512 * j * 896 + wwin));
  {
    const int l = lane_id(), g = l >> 4, q = (l >> 2) & 3;
    const unsigned vv = voff_spl(l, 512);
    const unsigned a0 = LDS_RING + (GQA ? cq : w) * 8192 + (4 * g + q) * 128 + (((q >> 1) | ((g & 1) << 1)) * 32) + 8 * (l & 3);
    const unsigned p0 = hh * 20480 + l * 16;
    auto rda = [&](auto S, auto U) {
      lds_t a = lds + (a0 ^ (U * 32)) + (GQA ? (S & 3) * 2048 : (S & 1) * 4096);
      fa[S & 1][U] = ldtr(a, a + (GQA ? 32768 : 2048));
    };
    auto rdb = [&](auto S, auto T) { fb[T] = ld128(lds + p0 + ((S >> 2) & 1) * 40960 + ((S & 3) * 5 + T) * 1024); };
    unsigned ov = 0, ow = 0;
    auto piece = [&](auto G, auto I) {
      if constexpr (G < 16) dmap<true, I>(rV, vv, (G * 32 + I * 8) * 512);
      else dmap<false, I>(rO, ov, ((G - 16) * (GQA ? 16 : 32) + I * 8) * 896);
    };
    constexpr auto TW = [](int s) -> int { return s == 12; };
    unsigned seen = 0;
    bool bad = false;
    float rl[5], rs[2] = {0.f, 0.f};
    f32x4 lv[2];
    unsigned el = 0, eg = 0;
    auto oput = [&](auto T, auto H) {
      const f32x4 u = acc[T][2 * H], v = acc[T][2 * H + 1];
      const float r = rl[T];
      *(LAS u32x4*)(lds + ow + H * 5120 + T * 1024) = u32x4{pk2(mul1(u[0], r), mul1(u[1], r)), pk2(mul1(u[2], r), mul1(u[3], r)),
                                                            pk2(mul1(v[0], r), mul1(v[1], r)), pk2(mul1(v[2], r), mul1(v[3], r))};
    };
    if constexpr (GQA) {
      wait_vm_bar<6>();
      sfor<0, 4>([&](auto U) { rda(IC<0>{}, U); });
    } else if constexpr (PBB) {
      wait_vm<4>();
      sfor<0, 4>([&](auto U) { rda(IC<0>{}, U); });
      set_m0(ring);
      bar();
      sfor<0, 4>([&](auto I) { piece(IC<2>{}, I); });
    } else {
      bar();
      wait_vm<4>();
      sfor<0, 4>([&](auto U) { rda(IC<0>{}, U); });
    }
    STAMP(ST + 5);
    sfor<0, 5>([&](auto T) { rdb(IC<0>{}, T); });
    if constexpr (!GQA && !PBB) { set_m0(ring); need(fa[0]); sfor<0, 4>([&](auto I) { piece(IC<2>{}, I); }); }
    sfor<0, 16>([&](auto S) {
      constexpr int s = S;
      SBAR();
      STAMP2(3, s);
      if constexpr (s == 0) prio_reset();
      if constexpr (GQA) {
        constexpr int nv = 4 + TW(s - 1) + TW(s - 2) + TW(s - 3);
        if constexpr ((s & 3) == 3) wait_vm_bar<nv>();
        else { need(fa[s & 1]); if constexpr (ABL & 2) wait_vm<nv>(); else wait_vm_bar_raw<nv>(); }
      } else {
        if constexpr (s == 15) bar();
        else { if constexpr ((s & 3) == 3 && !(ABL & 2)) bar(); wait_vm<4 + TW(s - 1) + TW(s - 2)>(); }
      }
      if constexpr (SAFE && s == 8) {
        if (grown) {
          LAS float* em = (LAS float*)(lds + LDS_EXM + (hh * 16 + (lane_id() & 15)) * 48);
          sfor<0, 5>([&](auto T) {
            const float d = __builtin_fmaf(em[384 + T], SCL, mul1(em[T + 0], -SCL));
            const float a = d > GROW ? __builtin_amdgcn_exp2f(-d) : 1.f;
            sfor<0, 4>([&](auto U) { acc[T][U] *= a; });
          });
        }
      }
      if constexpr (!SAFE && s == 14) { if (__builtin_amdgcn_ballot_w64(bad) != 0) *(LAS unsigned*)(lds + LDS_BAD) = 1u; }
      if constexpr (!SAFE && s == 15) redo = *(LAS const unsigned*)(lds + LDS_BAD);
      if constexpr (s == 11) ov = voff_spl(lane_id(), 896);
      if constexpr (s == 15) ow = hh * 40960 + cq * 10240 + lane_id() * 16;
      sfor<0, 20>([&](auto I) {
        constexpr int i = I, t = i >> 2, u = i & 3;
        acc[t][u] = MFMA(fa[s & 1][u], fb[t], s == 0 ? ZERO4 : acc[t][u]);
        if constexpr (GQA) {
          if constexpr (i == 0) set_m0(ring + (s & 3) * 2048);
          if constexpr (i == 1 || i == 2) piece(IC<s + 4>{}, IC<i - 1>{});
          if constexpr (s < 15 && i >= 4 && i < 7) rda(IC<s + 1>{}, IC<i - 4>{});
          if constexpr (s < 15 && i == 8) rda(IC<s + 1>{}, IC<3>{});
        } else if constexpr (s < 15) {
          if constexpr (i < 4) rda(IC<s + 1>{}, IC<i & 3>{});
          if constexpr (i == 5) set_m0(ring + ((s + 1) & 1) * 4096);
          if constexpr (i == DM0) need(fa[(s + 1) & 1]);
          if constexpr (i >= DM0 && i < DM0 + 4) piece(IC<s + 3>{}, IC<(i - DM0) & 3>{});
        }
        if constexpr (s < 15 && u == 3) rdb(IC<s + 1>{}, IC<t>{});
        if constexpr (i >= 1 && i < 19) {
          constexpr int x = 18 * s + i - 1, f0 = GQA ? 140 : 210;
          if constexpr (SAFE && 216 + x < 360) mop(IC<(216 + x) % 360>{});
          if constexpr (!SAFE && f0 + x < 280 && !(ABL & 4)) fop(IC<(f0 + x) % 280>{});
        }
        if constexpr (!(ABL & 1) && s < 3 && (i == 5 || i == 10 || i == 14 || i == 18)) {
          constexpr int k = PRE1 + 4 * s + (i == 5 ? 0 : i == 10 ? 1 : i == 14 ? 2 : 3);
          if constexpr (k < 10) pput(IC<1>{}, IC<k % 10>{}, pw);
        }
        if constexpr (!(ABL & 1) && s >= 3 && s <= 10 && (i == 5 || i == 10 || i == 14)) {
          constexpr int q4 = (s - 3) & 3, k = q4 * 3 - (q4 > 2) + (i == 5 ? 0 : i == 10 ? 1 : 2);
          if constexpr (k < 10 && !(q4 == 2 && i == 14)) pput(IC<s < 7 ? 2 : 3>{}, IC<k % 10>{}, pw);
        }
        if constexpr (s == 10) {
          if constexpr (i == 0) { const int l2 = lane_id(); el = LDS_EXL + ((GQA ? 0 : hh) * 80 + (l2 & 15)) * 48 + (GQA ? w : cq) * 4; eg = l2 >> 4; }
          if constexpr (i == 2) rs[0] = sw16<OpAdd>(ls[0], ls[1]);
          if constexpr (i == 3) rs[1] = sw16<OpAdd>(ls[2], ls[3]);
          if constexpr (i == 4) rs[0] = sw32<OpAdd>(rs[0], rs[1]);
          if constexpr (i == 6) *(LAS float*)(lds + el + eg * 768) = rs[0];
          if constexpr (i == 7) rs[1] = sw16<OpAdd>(ls[4], ls[5 * MT - 1]);
          if constexpr (i == 8) rs[1] = sw32<OpAdd>(rs[1], rs[1]);
          if constexpr (i == 9) *(LAS float*)(lds + el + 3072 + (GQA ? (eg & 1) * 3840 : 0)) = rs[1];
          if constexpr (GQA) {
            if constexpr (i == 11) rs[0] = sw16<OpAdd>(ls[5], ls[6]);
            if constexpr (i == 12) rs[1] = sw16<OpAdd>(ls[7], ls[8]);
            if constexpr (i == 13) rs[0] = sw32<OpAdd>(rs[0], rs[1]);
            if constexpr (i == 16) *(LAS float*)(lds + el + 3840 + eg * 768) = rs[0];
          }
          if constexpr (i >= 2 && i < 14) asm volatile("" : "+v"(rs[0]), "+v"(rs[1]));
        }
        if constexpr (TW(s) && i == 14) { seen = __hip_atomic_load(sync_word<LAST ? 1 : 0>(p, b, j), RLX); asm volatile("" ::: "memory"); }
        if constexpr (s >= 11 && s < 14) {
          constexpr int x = (s - 11) * 20 + i - 2, T = x / 10, c = x % 10;
          if constexpr (x == -2) el = LDS_EXL + (hh * 80 + (lane_id() & 15)) * 48;
          if constexpr (x >= 0 && x < 50) {
            if constexpr (c == 0) { lv[0] = *(LAS const f32x4*)(lds + el + T * 768); if constexpr (GQA) lv[1] = *(LAS const f32x4*)(lds + el + T * 768 + 16); }
            if constexpr (c == 4) rs[0] = (lv[0][0] + lv[0][1]) + lv[0][2];
            if constexpr (c == 5) rs[0] = GQA ? (rs[0] + lv[0][3]) + lv[1][0] : rs[0] + lv[0][3];
            if constexpr (c == 6 && GQA) rs[0] = (rs[0] + lv[1][1]) + lv[1][2];
            if constexpr (c == 7 && GQA) rs[0] += lv[1][3];
            if constexpr (c == 8) rl[T % 5] = __builtin_amdgcn_rcpf(rs[0]);
            if constexpr (c == 9 && !SAFE) bad |= !(rs[0] >= SUM_LO && rs[0] <= SUM_HI);
            if constexpr (c >= 4 && c < 8) asm volatile("" : "+v"(rs[0]));
            if constexpr (c == 8) asm volatile("" : "+v"(rl[T % 5]));
          }
        }
        if constexpr (!(ABL & 8) && s == 15 && i >= 6 && (i & 1) == 0) oput(IC<(i - 6) / 4>{}, IC<((i - 6) / 2) % 2>{});
        SBAR();
      });
    });
    STAMP2(3, 16);
    STAMP(ST + 6);
    sfor<7, 10>([&](auto I) { oput(IC<I / 2>{}, IC<I % 2>{}); });
    settle<LAST ? 1 : 0>(p, lds, b, j, seen);
  }
  return __builtin_amdgcn_readfirstlane(redo) != 0;
}

__device__ __forceinline__ void issue_wq01(const Params& p, int P, int j, int w, unsigned lds0) {
  const rsrc_t rW = mk_rsrc(p.wq[P], (512 * j + 64 * w) * 2, W_BYTES - (512 * j + 64 * w) * 2);
  const unsigned we = voff_nat(lane_id(), 8192);
  sfor<0, 2>([&](auto G) {
    set_m0(lds0 + LDS_RING + w * 8192 + G * 4096);
    sfor<0, 4>([&](auto I) { dmap<false, I>(rW, we ^ ((I & 1) * 64), (G * 32 + I * 8) * 8192); });
  });
}

template <int P, bool LAST, int ST>
__device__ __forceinline__ void run_problem(const Params& p, lds_t lds, unsigned lds0, int b, int j, int w) {
  if (__builtin_expect((front<P, LAST, ST, false>(p, lds, lds0, b, j, w) && !ABL) || FORCE_SAFE, 0)) {
    wait_vm_bar<0>();
    issue_x(p, P, b, w, lds0);
    issue_wq01(p, P, j, w, lds0);
    wait_vm_bar<0>();
    front<P, LAST, 30, true>(p, lds, lds0, fresh(b), fresh(j), fresh(w));
  }
  b = fresh(b); j = fresh(j); w = fresh(w);
  const int hh = w >> 2, cq = w & 3;
  const unsigned ring = lds0 + LDS_RING + w * 8192;
  f32x4 acc[5][4];
  bf16x8 fa[2][4], fb[5];
  const unsigned wwin = hh ? 512 + (cq * 96 & ~63) : w * 128;
  const rsrc_t rO = mk_rsrc(p.wo[P], 512 * j * 896 + wwin, W_BYTES - (512 * j * 896 + wwin));

  RedRows red;
  auto oproj = [&](auto NTILE) {
    constexpr int NT = NTILE, NS = 5 * NT, D0 = NT == 4 ? DM0 : 5;
    const rsrc_t rN = mk_rsrc(p.wq[1 - P], (512 * j + 64 * w) * 2, W_BYTES - (512 * j + 64 * w) * 2);
    const int l = lane_id(), g = l >> 4, q = (l >> 2) & 3;
    const unsigned ov = voff_spl(l, 896), we = voff_nat(l, 8192);
    auto piece = [&](auto G, auto I) {
      if constexpr (G < 16) dmap<false, I>(rO, ov, (G * 32 + I * 8) * 896);
      else dmap<false, I>(rN, we ^ ((I & 1) * 64), ((G - 16) * 32 + I * 8) * 8192);
    };
    unsigned a0[NT], o1 = l * 16 + 40960;
    sfor<0, NT>([&](auto U) {
      a0[U] = LDS_RING + w * 8192 + (4 * g + q) * 128 + ((((q >> 1) | ((g & 1) << 1)) ^ (U + (NT == 3 ? cq & 1 : 0))) * 32) + 8 * (l & 3);
    });
    asm volatile("" : "+v"(o1));
    auto rda = [&](auto S, auto U) { lds_t a = lds + a0[U] + (S & 1) * 4096; fa[S & 1][U] = ldtr(a, a + 2048); };
    auto rdb = [&](auto S, auto T) { fb[T] = ld128(lds + (S < 8 ? l * 16 : o1) + ((S & 7) * 5 + T) * 1024); };
    auto needa = [&](auto X) {
      if constexpr (NT == 4) need(fa[X]);
      else need3(fa[X]);
    };
    const rsrc_t rS = mk_rsrc(p.slab, ((P * 32 + b) * 8 + j) * SLAB + (NT == 4 ? w * 10240 : 40960 + cq * 8192), 10240);
    auto sput = [&](auto R) {
      constexpr int r = R;
      constexpr int tu = NT == 4 ? r / 2 : r < 5 ? r : 2 * (r - 5), cu = NT == 4 ? 2 * (r & 1) : r < 5 ? 0 : 2;
      constexpr int tv = NT == 4 ? r / 2 : r < 5 ? r : r == 7 ? 4 : 2 * (r - 5) + 1, cv = NT == 4 ? 2 * (r & 1) + 1 : r < 5 ? 1 : 2;
      const f32x4 u = acc[tu][cu], v = acc[tv][cv];
      auto r0 = __builtin_amdgcn_permlane16_swap(pk2(u[0], u[1]), pk2(v[0], v[1]), false, false);
      auto r1 = __builtin_amdgcn_permlane16_swap(pk2(u[2], u[3]), pk2(v[2], v[3]), false, false);
      __builtin_amdgcn_raw_buffer_store_b128(u32x4{r0[0], r1[0], r0[1], r1[1]}, rS, l * 16 + r * 1024, 0, SAUX  );
    };
    if constexpr (!PBO) bar();
    wait_vm<4>();
    sfor<0, NT>([&](auto U) { rda(IC<0>{}, U); });
    set_m0(ring);
    if constexpr (PBO) bar();
    STAMP(ST + 7);
    sfor<0, 5>([&](auto T) { rdb(IC<0>{}, T); });
    if constexpr (!PBO) needa(IC<0>{});
    sfor<0, 4>([&](auto I) { piece(IC<2>{}, I); });
    sfor<0, 16>([&](auto S) {
      constexpr int s = S;
      SBAR();
      STAMP2(5, s);
      if constexpr (s == 0) prio_reset();
      if constexpr (s == PO) prio_yield(hh);
      if constexpr (s < 15) wait_vm<4>();
      else if constexpr (!LAST) { bar(); issue_x<X2NT != 0>(p, 1 - P, b, w, lds0); }
      sfor<0, NS>([&](auto I) {
        constexpr int i = I, t = i / NT, u = i % NT;
        acc[t][u] = MFMA(fa[s & 1][u], fb[t], s == 0 ? ZERO4 : acc[t][u]);
        if constexpr (s < 15) {
          if constexpr (i < NT) rda(IC<s + 1>{}, IC<i % NT>{});
          if constexpr (u == NT - 1) rdb(IC<s + 1>{}, IC<t>{});
          if constexpr (!(LAST && s >= 13)) {
            if constexpr (i == D0 - 2) set_m0(ring + ((s + 1) & 1) * 4096);
            if constexpr (i == D0) needa(IC<(s + 1) & 1>{});
            if constexpr (i >= D0 && i < D0 + 4) piece(IC<s + 3>{}, IC<(i - D0) & 3>{});
          }
          if constexpr (LAST && s >= 13 && i >= 3 && i < 15 && i % 3 == 0) red_load<0, (4 * (s - 13) + i / 3 - 1) & 7>(red, p, 1 - P, b, j, w);
        } else if constexpr (NT == 4) {
          if constexpr (i >= 6 && (i & 1) == 0) sput(IC<(i - 6) / 2>{});
        } else {
          if constexpr (i == 5 || i == 8 || i == 11 || i == 14) sput(IC<(i - 5) / 3>{});
          if constexpr (i == 10) sput(IC<5>{});
        }
        SBAR();
      });
    });
    STAMP2(5, 16);
    STAMP(ST + 8);
    if constexpr (NT == 4) sfor<7, 10>(sput);
    else { sput(IC<6>{}); sput(IC<4>{}); sput(IC<7>{}); }
    if constexpr (!LAST) wait_vm_bar<NT == 4 ? 10 : 8>();
  };
  if (hh == 0) oproj(IC<4>{});
  else oproj(IC<3>{});
  STAMP(ST + 9);
  if constexpr (LAST) {
    wait_vm<0>();
    STAMP(21);
    bar();
    STAMP(22);
    if (w == 0) arrive<2>(p, lds, b, j);
    if (w == 7) sfor<0, 8>([&](auto J) { red_load<1, J>(red, p, 1 - P, b, j, w); });
    red_store(red, p, 1 - P, b, j, w);
    if (w == 0) settle<2>(p, lds, b, j, __hip_atomic_load(sync_word<2>(p, b, j), RLX));
    bar();
    STAMP(23);
    sfor<0, 8>([&](auto J) { red_load<0, J>(red, p, P, b, j, w); });
    if (w == 7) sfor<0, 8>([&](auto J) { red_load<1, J>(red, p, P, b, j, w); });
    red_store(red, p, P, b, j, w);
  }
}

__global__ __launch_bounds__(512, 2) void fused_attn_kernel(Params p) {
  extern __shared__ __attribute__((aligned(16))) char smem[];
  lds_t lds = (lds_t)smem;
  const unsigned lds0 = (unsigned)(uintptr_t)lds;
  const int w = __builtin_amdgcn_readfirstlane(threadIdx.x >> 6);
  const int j = blockIdx.x & 7, b = blockIdx.x >> 3;
  const int first = b & 1;
  STAMP(0);
  {
    if constexpr (CODEW) {
      unsigned long long pc;
      asm volatile("s_getpc_b64 %0" : "=s"(pc));
      warm(lds0, mk_rsrc((const void*)(pc & ~127ull), (b * 8 + w) * 5 * 128, 4 * 128 + 4), lane_id() * 128);
    }
    unsigned found = 0;
    if (w == 7) found = __hip_atomic_load(flag_word(p, b, lane_id() & 7), RLX);
    asm volatile("" ::: "memory");
    issue_x(p, first, b, w, lds0);
    asm volatile("s_barrier" ::: "memory");
    const int l = lane_id();
    issue_wq01(p, first, j, w, lds0);
    const unsigned id = (w & 3) * 64 + l;
    if (w < 4) warm(lds0, mk_rsrc(p.wq[first], 0, W_BYTES), ((b >> 1) * 28 + (id >> 3)) * 8192 + 1024 * j + (id & 7) * 128);
    else warm(lds0, mk_rsrc(p.wo[first], 0, W_BYTES), 512 * j * 896 + (b >> 1) * 28672 + id * 128);
    if constexpr (XW) warm(lds0, mk_rsrc(p.x[1 - first], b * X_BYTES + (j * 70 + w * 9) * 128, w == 7 ? 6 * 128 + 4 : 8 * 128 + 4), l * 128);
    wait_vm_bar<9 + XW>();
    if (w == 7) {
      *(LAS unsigned*)(lds + LDS_OLD + (l & 7) * 4) = found;
      if (l < 8) __hip_atomic_store(ack_word(p, b, l, j), found + 1u, RLX);
    }
  }
  STAMP(10);
  if (first == 0) {
    run_problem<0, false, 0>(p, lds, lds0, fresh(b), fresh(j), fresh(w));
    run_problem<1, true, 10>(p, lds, lds0, fresh(b), fresh(j), fresh(w));
  } else {
    run_problem<1, false, 0>(p, lds, lds0, fresh(b), fresh(j), fresh(w));
    run_problem<0, true, 10>(p, lds, lds0, fresh(b), fresh(j), fresh(w));
  }
  STAMP(20);
}

extern "C" void kernel_launch(void* const* d_in, const int* in_sizes, int n_in, void* d_out, int out_size, void* d_ws,
                              size_t ws_size, hipStream_t stream) {
  Params p;
  for (int i = 0; i < 2; ++i) {
    p.x[i] = d_in[5 * i + 0]; p.k[i] = d_in[5 * i + 1]; p.v[i] = d_in[5 * i + 2]; p.wq[i] = d_in[5 * i + 3]; p.wo[i] = d_in[5 * i + 4];
  }
  p.sync = (unsigned*)d_ws;
  p.slab = WSTAIL ? (char*)d_ws + ((ws_size - 512 * (size_t)SLAB) & ~(size_t)4095) : (char*)d_ws + 262144;
  p.out = d_out;
  p.diag = (unsigned long long*)((char*)d_ws + (64u << 20));
  (void)hipFuncSetAttribute((const void*)fused_attn_kernel, hipFuncAttributeMaxDynamicSharedMemorySize, LDS_TOTAL);
  fused_attn_kernel<<<256, 512, LDS_TOTAL, stream>>>(p);
}
```

```cpp
#include <hip/hip_runtime.h>
#include <hip/hip_bf16.h>
#include <stdint.h>
#include <type_traits>

typedef short bf16x8 __attribute__((ext_vector_type(8)));
typedef short s16x4 __attribute__((ext_vector_type(4)));
typedef float f32x4 __attribute__((ext_vector_type(4)));
typedef float f32x2 __attribute__((ext_vector_type(2)));
typedef __bf16 bf16x2 __attribute__((ext_vector_type(2)));
typedef unsigned u32x4 __attribute__((ext_vector_type(4)));
typedef unsigned u32x2 __attribute__((ext_vector_type(2)));
#define LAS __attribute__((address_space(3)))
typedef __amdgpu_buffer_rsrc_t rsrc_t;
typedef LAS char* lds_t;

#define DIAG 0
#define DLAST 1
#define PO 12
#define FAKEKV 0

constexpr int LDS_RING = 81920;
constexpr int LDS_EXM = 147456;
constexpr int LDS_EXL = 151552;
constexpr int LDS_DUMMY = 159232;
constexpr int LDS_OLD = 159488;
constexpr int LDS_TOTAL = DIAG ? 163840 : 159552;
constexpr unsigned X_BYTES = 80 * 448 * 2;
constexpr unsigned W_BYTES = 448 * 4096 * 2;
constexpr unsigned KV_HEAD = 512 * 256 * 2;
constexpr unsigned SLAB = 72 * 1024;
constexpr int CODE_LINES = 5;
constexpr float SCL = 0.0625f * 1.4426950408889634f;
constexpr float GROW = 20.f;

template <int I, int N, class F>
__device__ __forceinline__ void sfor(F&& f) {
  if constexpr (I < N) { f(std::integral_constant<int, I>{}); sfor<I + 1, N>(f); }
}
#define SBAR() __builtin_amdgcn_sched_barrier(0)
#define MFMA(a, b, c) __builtin_amdgcn_mfma_f32_16x16x32_bf16(a, b, c, 0, 0, 0)
template <int N> __device__ __forceinline__ void wait_vm() { asm volatile("s_waitcnt vmcnt(%0)" ::"i"(N) : "memory"); }
__device__ __forceinline__ void bar() { asm volatile("s_waitcnt lgkmcnt(0)\n\ts_barrier" ::: "memory"); }
template <int N> __device__ __forceinline__ void wait_vm_bar_raw() { asm volatile("s_waitcnt vmcnt(%0)\n\ts_barrier" ::"i"(N) : "memory"); }
template <int N> __device__ __forceinline__ void wait_vm_bar() { asm volatile("s_waitcnt vmcnt(%0) lgkmcnt(0)\n\ts_barrier" ::"i"(N) : "memory"); }
__device__ __forceinline__ void prio_reset() { asm volatile("s_setprio 1"); }
__device__ __forceinline__ void prio_yield(int hh) { asm volatile("s_cmp_lg_u32 %0, 0\n\ts_cbranch_scc1 1f\n\ts_setprio 0\n1:" ::"s"(hh) : "scc"); }
__device__ __forceinline__ int lane_id() {
  int l; asm volatile("v_mbcnt_lo_u32_b32 %0, -1, 0\n\tv_mbcnt_hi_u32_b32 %0, -1, %0" : "=v"(l)); return l;
}
__device__ __forceinline__ int fresh(int x) { asm volatile("" : "+s"(x)); return x; }
__device__ __forceinline__ unsigned pk2(float a, float b) {
  return __builtin_bit_cast(unsigned, __builtin_convertvector(f32x2{a, b}, bf16x2));
}
__device__ __forceinline__ rsrc_t mk_rsrc(const void* p, unsigned off, unsigned bytes) {
  return __builtin_amdgcn_make_buffer_rsrc((void*)((const char*)p + off), (short)0, (int)bytes, 0x00020000);
}
__device__ __forceinline__ void need(const bf16x8 (&f)[4]) { asm volatile("" ::"v"(f[0]), "v"(f[1]), "v"(f[2]), "v"(f[3]) : "memory"); }
__device__ __forceinline__ void need3(const bf16x8 (&f)[4]) { asm volatile("" ::"v"(f[0]), "v"(f[1]), "v"(f[2]) : "memory"); }
__device__ __forceinline__ void set_m0(unsigned m0v) { asm volatile("s_mov_b32 m0, %0\n\ts_nop 0" ::"s"(m0v) : "memory"); }
template <bool NT, int I>
__device__ __forceinline__ void dmap(rsrc_t r, unsigned v, unsigned src) {
  if constexpr (NT && !FAKEKV) asm volatile("buffer_load_dwordx4 %0, %1, %2 offen offset:%3 nt lds" ::"v"(v), "s"(r), "s"(src - 1024u * I), "i"(1024 * I) : "memory");
  else asm volatile("buffer_load_dwordx4 %0, %1, %2 offen offset:%3 lds" ::"v"(v), "s"(r), "s"(src - 1024u * I), "i"(1024 * I) : "memory");
}
__device__ __forceinline__ void warm(unsigned lds0, rsrc_t r, unsigned v) {
  asm volatile("s_mov_b32 m0, %0\n\ts_nop 0\n\tbuffer_load_dword %1, %2, 0 offen lds" ::"s"(lds0 + LDS_DUMMY), "v"(v), "s"(r) : "memory");
}
extern "C" __device__ float llvm_fmul_legacy(float, float) __asm("llvm.amdgcn.fmul.legacy");
__device__ __forceinline__ float mul1(float a, float b) { return llvm_fmul_legacy(a, b); }
__device__ __forceinline__ bf16x8 ld128(lds_t p) { return *(LAS const bf16x8*)p; }
__device__ __forceinline__ bf16x8 ldtr(lds_t p0, lds_t p1) {
  s16x4 lo = __builtin_amdgcn_ds_read_tr16_b64_v4i16((LAS s16x4*)p0);
  s16x4 hi = __builtin_amdgcn_ds_read_tr16_b64_v4i16((LAS s16x4*)p1);
  return __builtin_shufflevector(lo, hi, 0, 1, 2, 3, 4, 5, 6, 7);
}
__device__ __forceinline__ float max3(float a, float b, float c) { return __builtin_fmaxf(__builtin_fmaxf(a, b), c); }
struct OpMax { static __device__ __forceinline__ float f(float a, float b) { return max3(a, b, b); } };
struct OpAdd { static __device__ __forceinline__ float f(float a, float b) { return a + b; } };
template <class Op> __device__ __forceinline__ float sw16(float a, float b) {
  auto r = __builtin_amdgcn_permlane16_swap(__float_as_uint(a), __float_as_uint(b), false, false);
  return Op::f(__uint_as_float(r[0]), __uint_as_float(r[1]));
}
template <class Op> __device__ __forceinline__ float sw32(float a, float b) {
  auto r = __builtin_amdgcn_permlane32_swap(__float_as_uint(a), __float_as_uint(b), false, false);
  return Op::f(__uint_as_float(r[0]), __uint_as_float(r[1]));
}
template <class Op> __device__ __forceinline__ float red4(float a, float b, float c, float d) { return sw32<Op>(sw16<Op>(a, b), sw16<Op>(c, d)); }
template <class Op> __device__ __forceinline__ float red2(float a, float b) { const float x = sw16<Op>(a, b); return sw32<Op>(x, x); }

__device__ __forceinline__ unsigned voff_nat(int l, unsigned pitch) { return (l >> 3) * pitch + 16 * ((l & 7) ^ (((l >> 4) & 1) << 1)); }
__device__ __forceinline__ unsigned voff_spl(int l, unsigned pitch) { return (l >> 3) * pitch + 16 * ((l & 7) ^ (((l >> 4) & 3) << 1)); }
__device__ __forceinline__ unsigned voff_k(int l) { return (l >> 3) * 512 + 16 * ((l & 7) ^ ((l >> 4) & 3)); }

struct Params {
  const void *x[2], *k[2], *v[2], *wq[2], *wo[2];
  void* slab;
  unsigned* sync;
  void* out;
  unsigned long long* diag;
};

#define STAMP(i)
#define STAMP2(ph, i)

constexpr int DM0 = 8;
template <int V> using IC = std::integral_constant<int, V>;
constexpr f32x4 ZERO4 = {0.f, 0.f, 0.f, 0.f};

__device__ __forceinline__ void issue_x(const Params& p, int P, int b, int w, unsigned lds0) {
  w = fresh(w);
  const int l = lane_id();
  const unsigned v = (l >> 3) * 896 + 16 * ((l & 7) ^ ((l >> 4) & 3));
  const rsrc_t rx = mk_rsrc(p.x[P], b * X_BYTES, X_BYTES);
  sfor<0, 9>([&](auto I) {
    const unsigned pc = w * 9 + I, blk = (pc * 205) >> 11, rg = pc - blk * 10;
    set_m0(lds0 + pc * 1024);
    dmap<false, 0>(rx, v ^ ((rg & 1) * 64), rg * 7168 + blk * 128);
  });
}

typedef __attribute__((address_space(1))) unsigned gu32;
#define RLX __ATOMIC_RELAXED, __HIP_MEMORY_SCOPE_AGENT
__device__ __forceinline__ gu32* flag_word(const Params& p, int b, int j) { return (gu32*)p.sync + (b * 8 + j) * 32; }
__device__ __forceinline__ gu32* ack_word(const Params& p, int b, int prod, int cons) { return (gu32*)p.sync + 8192 + ((b * 8 + prod) * 8 + cons) * 16; }
template <int STAGE>
__device__ __forceinline__ void arrive(const Params& p, lds_t lds, int b, int j) {
  const unsigned old = *(LAS const unsigned*)(lds + LDS_OLD + j * 4);
  if (lane_id() == 0) __hip_atomic_store(flag_word(p, b, j), old + STAGE, RLX);
}
template <int STAGE> __device__ __forceinline__ gu32* sync_word(const Params& p, int b, int j) {
  const int c = lane_id() & 7;
  return STAGE ? flag_word(p, b, c) : ack_word(p, b, j, c);
}
template <int STAGE>
__device__ __forceinline__ void settle(const Params& p, lds_t lds, int b, int j, unsigned seen) {
  const unsigned old = *(LAS const unsigned*)(lds + LDS_OLD + (STAGE ? lane_id() & 7 : j) * 4);
  for (unsigned spins = 0; spins < (1u << 22); ++spins) {
    if (__builtin_amdgcn_ballot_w64(STAGE ? seen - old - STAGE > 2u - STAGE : seen != old + 1u) == 0) break;
    __builtin_amdgcn_s_sleep(2);
    seen = __hip_atomic_load(sync_word<STAGE>(p, b, j), RLX);
  }
  asm volatile("" ::: "memory");
}
struct RedRows { u32x4 v[2][8]; };
template <int K, int J>
__device__ __forceinline__ void red_load(RedRows& r, const Params& p, int P, int b, int j, int w) {
  const rsrc_t rS = mk_rsrc(p.slab, (P * 32 + b) * 8 * SLAB, 8 * SLAB);
  r.v[K][J] = __builtin_amdgcn_raw_buffer_load_b128(rS, lane_id() * 16, J * SLAB + (9 * j + w + K) * 1024, 16  );
}
__device__ __forceinline__ void red_store(const RedRows& r, const Params& p, int P, int b, int j, int w) {
  const int l = lane_id(), n = l & 15, g = l >> 4;
  sfor<0, 2>([&](auto K) {
    if (K == 0 || w == 7) {
      float sm[8];
      sfor<0, 4>([&](auto I) { sm[2 * I] = __uint_as_float(r.v[K][0][I + 0] << 16); sm[2 * I + 1] = __uint_as_float(r.v[K][0][I + 0] & 0xffff0000u); });
      sfor<1, 8>([&](auto J) {
        sfor<0, 4>([&](auto I) { sm[2 * I] += __uint_as_float(r.v[K][J][I + 0] << 16); sm[2 * I + 1] += __uint_as_float(r.v[K][J][I + 0] & 0xffff0000u); });
      });
      const int f = 9 * j + w + K;
      int t, col;
      if (f < 40) { const int ww = f / 10; t = (f - ww * 10) >> 1; col = 64 * ww + 32 * (f & 1) + 16 * (g & 1); }
      else { const int rr = f & 7, c0 = 256 + 48 * ((f - 40) >> 3); t = rr < 5 ? rr : 2 * (rr - 5) + (g & 1); col = c0 + (rr < 5 ? 16 * (g & 1) : 32); }
      if (t < 5)
        *(u32x4*)((char*)p.out + ((size_t)(b * 80 + t * 16 + n) * 896 + P * 448 + col + 8 * (g >> 1)) * 2) =
            u32x4{pk2(sm[0], sm[1]), pk2(sm[2], sm[3]), pk2(sm[4], sm[5]), pk2(sm[6], sm[7])};
    }
  });
}

template <int P  , bool LAST, int ST>
__device__ __forceinline__ void run_problem(const Params& p, lds_t lds, unsigned lds0, int b, int j, int w) {
  constexpr bool GQA = (P == 0);
  const int bk = FAKEKV ? 0 : b;
  const int hh = w >> 2, cq = w & 3;
  const unsigned ring = lds0 + LDS_RING + w * 8192;
  f32x4 acc[5][4];
  bf16x8 fa[2][4], fb[5];

  const unsigned koff = GQA ? (bk * 8 + j) * KV_HEAD + w * 16 * 512 : (bk * 16 + 2 * j + hh) * KV_HEAD + cq * 32 * 512;
  const rsrc_t rK = mk_rsrc(p.k[P], koff, KV_HEAD - (GQA ? w * 16 * 512 : cq * 32 * 512));
  auto k_src = [](int g, int i) -> unsigned {
    return GQA ? ((g >> 2) * 256 + (i >> 1) * 128 + (i & 1) * 8) * 512 + (g & 3) * 128 : ((g >> 2) * 128 + i * 8) * 512 + (g & 3) * 128;
  };

  {
    const rsrc_t rW = mk_rsrc(p.wq[P], (512 * j + 64 * w) * 2, W_BYTES - (512 * j + 64 * w) * 2);
    const int l = lane_id(), n = l & 15, g = l >> 4, q = (l >> 2) & 3;
    const unsigned we = voff_nat(l, 8192), ke = voff_k(l);
    const unsigned a0 = LDS_RING + w * 8192 + (8 * g + q) * 128 + (((q >> 1) | ((g & 1) << 1)) * 32) + 8 * (l & 3);
    unsigned x0[2][2];
    x0[0][0] = n * 128 + 16 * (g ^ ((n >> 1) & 7)); x0[1][0] = x0[0][0] ^ 64; x0[0][1] = x0[0][0] + 40960; x0[1][1] = x0[1][0] + 40960;
    asm volatile("" : "+v"(x0[0][1]), "+v"(x0[1][1]));
    const unsigned qw = hh * 40960 + cq * 10240 + l * 16;
    auto rda = [&](auto S, auto U) { lds_t a = lds + (a0 ^ (U * 32)) + (S & 1) * 4096; fa[S & 1][U] = ldtr(a, a + 512); };
    auto rdb = [&](auto S, auto T) { fb[T] = ld128(lds + x0[S & 1][S >= 8] + ((S >> 1) & 3) * 10240 + T * 2048); };
    auto piece = [&](auto G, auto I) {
      if constexpr (G < 14) dmap<false, I>(rW, we ^ ((I & 1) * 64), (G * 32 + I * 8) * 8192);
      else dmap<true, I>(rK, ke ^ ((I & 1) * 64), k_src(G - 14, I));
    };
    auto qput = [&](auto T, auto H) {
      const f32x4 u = acc[T][2 * H], v = acc[T][2 * H + 1];
      *(LAS u32x4*)(lds + qw + H * 5120 + T * 1024) = u32x4{pk2(u[0], u[1]), pk2(u[2], u[3]), pk2(v[0], v[1]), pk2(v[2], v[3])};
    };
    if constexpr (!LAST) wait_vm<5>();
    sfor<0, 4>([&](auto U) { rda(IC<0>{}, U); });
    sfor<0, 5>([&](auto T) { rdb(IC<0>{}, T); });
    set_m0(ring);
    need(fa[0]);
    sfor<0, 4>([&](auto I) { piece(IC<2>{}, I); });
    sfor<0, 14>([&](auto S) {
      constexpr int s = S;
      SBAR();
      STAMP2(4, s);
      if constexpr (s == 0) prio_reset();
      if constexpr (s == 9) prio_yield(hh);
      if constexpr (s == 0) { if constexpr (!LAST) wait_vm<5>(); }
      else if constexpr (s < 13) wait_vm<(LAST && s == 6) ? 0 : 4>();
      else {
        bar();
        *(LAS f32x2*)(lds + LDS_EXM + w * 512 + l * 8) = f32x2{-INFINITY, -INFINITY};
        if constexpr (LAST) { if (w == 0) arrive<1>(p, lds, b, j); }
      }
      sfor<0, 20>([&](auto I) {
        constexpr int i = I, t = i >> 2, u = i & 3;
        acc[t][u] = MFMA(fa[s & 1][u], fb[t], s == 0 ? ZERO4 : acc[t][u]);
        if constexpr (s < 13) {
          if constexpr (i < 4) rda(IC<s + 1>{}, IC<i & 3>{});
          if constexpr (u == 3) rdb(IC<s + 1>{}, IC<t>{});
          if constexpr (i == 5) set_m0(ring + ((s + 1) & 1) * 4096);
          if constexpr (i == DM0) need(fa[(s + 1) & 1]);
          if constexpr (i >= DM0 && i < DM0 + 4) piece(IC<s + 3>{}, IC<(i - DM0) & 3>{});
        } else if constexpr (i >= 6 && (i & 1) == 0) qput(IC<(i - 6) / 4>{}, IC<((i - 6) / 2) % 2>{});
        SBAR();
      });
    });
    STAMP2(4, 14);
    STAMP(ST + 1);
    sfor<7, 10>([&](auto I) { qput(IC<I / 2>{}, IC<I % 2>{}); });
    bar();
  }
  STAMP(ST + 2);

  const rsrc_t rV = GQA ? mk_rsrc(p.v[P], (bk * 8 + j) * KV_HEAD + hh * 16 * 512 + cq * 128, KV_HEAD - (hh * 16 * 512 + cq * 128))
                        : mk_rsrc(p.v[P], (bk * 16 + 2 * j + hh) * KV_HEAD + cq * 128, KV_HEAD - cq * 128);
  constexpr int MT = GQA ? 2 : 1;
  constexpr int NG = GQA ? 8 : 16;
  f32x4 sc[4][10];
  float mx[5 * MT], nm[5 * MT], ls[5 * MT], ev[4];
  u32x2 pq[4][10];
  auto mop = [&](auto ID) {
    constexpr int id = ID, kq = id / 90, k = (id / 9) % 10, part = id % 9, r = GQA ? k : (k >> 1);
    if constexpr (part == 0) { ev[0] = __builtin_fmaf(sc[kq][k][0], SCL, nm[r]); ev[1] = __builtin_fmaf(sc[kq][k][1], SCL, nm[r]); }
    if constexpr (part == 1) ev[0] = __builtin_amdgcn_exp2f(ev[0]);
    if constexpr (part == 2) { ev[2] = __builtin_fmaf(sc[kq][k][2], SCL, nm[r]); ev[3] = __builtin_fmaf(sc[kq][k][3], SCL, nm[r]); }
    if constexpr (part == 3) ev[1] = __builtin_amdgcn_exp2f(ev[1]);
    if constexpr (part == 4) ev[2] = __builtin_amdgcn_exp2f(ev[2]);
    if constexpr (part == 5) { if constexpr (kq == 0 && (GQA || (k & 1) == 0)) ls[r] = ev[0] + ev[1]; else ls[r] = (ls[r] + ev[0]) + ev[1]; }
    if constexpr (part == 6) ev[3] = __builtin_amdgcn_exp2f(ev[3]);
    if constexpr (part == 7) { pq[kq][k][0] = pk2(ev[0], ev[1]); ls[r] += ev[2]; }
    if constexpr (part == 8) { pq[kq][k][1] = pk2(ev[2], ev[3]); ls[r] += ev[3]; }
    if constexpr (part == 0) asm volatile("" : "+v"(ev[0]), "+v"(ev[1]));
    if constexpr (part == 1) asm volatile("" : "+v"(ev[0]));
    if constexpr (part == 2) asm volatile("" : "+v"(ev[2]), "+v"(ev[3]));
    if constexpr (part == 3) asm volatile("" : "+v"(ev[1]));
    if constexpr (part == 4) asm volatile("" : "+v"(ev[2]));
    if constexpr (part == 5) asm volatile("" : "+v"(ls[r]));
    if constexpr (part == 6) asm volatile("" : "+v"(ev[3]));
    if constexpr (part == 7) asm volatile("" : "+v"(ls[r]), "+v"(pq[kq][k][0]));
    if constexpr (part == 8) asm volatile("" : "+v"(ls[r]), "+v"(pq[kq][k][1]));
  };
  auto pput = [&](auto KQ, auto K, unsigned pw) {
    constexpr int kq = KQ, k = K;
    if constexpr (GQA) *(LAS u32x2*)(lds + pw + (kq & 1) * 40960 + (k / 5) * 20480 + (k % 5) * 1024) = pq[kq][k];
    else if constexpr (k & 1) *(LAS u32x4*)(lds + pw + (kq & 1) * 40960 + (k / 2) * 1024) = u32x4{pq[kq][k - 1][0], pq[kq][k - 1][1], pq[kq][k][0], pq[kq][k][1]};
  };
  bool grown;
  unsigned pw;
  {
    const int l = lane_id(), n = l & 15, g = l >> 4;
    const unsigned ke = voff_k(l);
    unsigned vv = 0;
    const unsigned k0 = LDS_RING + w * 8192 + n * 128 + 16 * ((g >> 1) ^ ((n >> 1) & 7)) + 8 * (g & 1);
    const unsigned q0 = (GQA ? 0 : hh * 40960) + l * 16;
    unsigned q1 = q0 + 40960;
    asm volatile("" : "+v"(q1));
    constexpr int NSUB = 2 * MT;
    auto rdk = [&](auto G, auto I) {
      lds_t a = lds + (I & 1) * 2048 + (G & 1) * 4096;
      const s16x4 lo = *(LAS const s16x4*)(a + (k0 ^ ((I >> 1) * 64))), hi = *(LAS const s16x4*)(a + (k0 ^ ((I >> 1) * 64 + 32)));
      fa[G & 1][I] = __builtin_shufflevector(lo, hi, 0, 1, 2, 3, 4, 5, 6, 7);
    };
    auto rdq = [&](auto Z, auto T) {
      constexpr int z = Z, gi = z / NSUB, e = (z % NSUB) / MT, h = z % MT;
      fb[T] = ld128(lds + (h ? q1 : q0) + ((2 * (gi & 3) + e) * 5 + T) * 1024);
    };
    auto piece = [&](auto G, auto I) {
      if constexpr (G < NG) dmap<true, I>(rK, ke ^ ((I & 1) * 64), k_src(G, I));
      else if constexpr (!GQA) dmap<false, I>(rV, vv, ((G - NG) * 32 + I * 8) * 512);
      else dmap<true, I>(rV, vv, ((2 * (G - NG) + (I >> 1)) * 32 + (I & 1) * 8) * 512);
    };
    auto qmax = [&](auto KQ, auto K) {
      constexpr int kq = KQ, k = K, r = GQA ? k : (k >> 1);
      if constexpr ((kq & 1) == 0 && (GQA || (k & 1) == 0)) mx[r] = max3(max3(sc[kq][k][0], sc[kq][k][1], sc[kq][k][2]), sc[kq][k][3], sc[kq][k][3]);
      else mx[r] = max3(max3(mx[r], sc[kq][k][0], sc[kq][k][1]), sc[kq][k][2], sc[kq][k][3]);
      asm volatile("" : "+v"(mx[r]));
    };
    auto xmax = [&](auto HALF, auto C) {
      const int l2 = lane_id(), g2 = l2 >> 4;
      LAS float* em = (LAS float*)(lds + LDS_EXM + HALF * 1536 + ((GQA ? 0 : hh) * 16 + (l2 & 15)) * 48);
      if constexpr (C == 0) __builtin_amdgcn_ds_fmaxf(em + g2, red4<OpMax>(mx[0], mx[1], mx[2], mx[3]), 0, 0, false);
      if constexpr (C == 1 && GQA) __builtin_amdgcn_ds_fmaxf(em + (g2 & 1) * 192 + 4, red2<OpMax>(mx[4], mx[5 * MT - 1]), 0, 0, false);
      if constexpr (C == 1 && !GQA) __builtin_amdgcn_ds_fmaxf(em + 4, red2<OpMax>(mx[4], mx[4]), 0, 0, false);
      if constexpr (C == 2 && GQA) __builtin_amdgcn_ds_fmaxf(em + 192 + g2, red4<OpMax>(mx[5], mx[6], mx[7], mx[8]), 0, 0, false);
    };
    auto rmax = [&](auto HALF) {
      LAS float* em = (LAS float*)(lds + LDS_EXM + HALF * 1536 + ((GQA ? 0 : hh) * 16 + (lane_id() & 15)) * 48);
      sfor<0, MT>([&](auto H) {
        const f32x4 v = *(LAS const f32x4*)(em + H * 192);
        sfor<0, 4>([&](auto T) { mx[H * 5 + T] = v[T + 0]; });
        mx[H * 5 + 4] = em[H * 192 + 4];
      });
    };
    wait_vm<4>();
    sfor<0, 4>([&](auto I) { rdk(IC<0>{}, I); });
    sfor<0, 5>([&](auto T) { rdq(IC<0>{}, T); });
    set_m0(ring);
    need(fa[0]);
    sfor<0, 4>([&](auto I) { piece(IC<2>{}, I); });
    sfor<0, 32>([&](auto Z) {
      constexpr int z = Z, gi = z / NSUB, sub = z % NSUB, e = sub / MT, h = sub % MT;
      SBAR();
      STAMP2(2, z);
      if constexpr (z == 0 || z == 20) prio_reset();
      if constexpr (z == 13 || z == 28) prio_yield(hh);
      if constexpr (z == 19) { bar(); rmax(IC<0>{}); }
      if constexpr (z == (NG - 3) * NSUB) vv = voff_spl(lane_id(), 512);
      if constexpr (sub == 0 && gi + 1 < NG) wait_vm<4>();
      sfor<0, 10>([&](auto I) {
        constexpr int i = I, t = i >> 1, u = i & 1;
        constexpr int kq = GQA ? 2 * (gi >> 2) + u : (gi >> 2), k = GQA ? 5 * h + t : i;
        sc[kq][k] = MFMA(fa[gi & 1][2 * e + u], fb[t], ((gi & 3) == 0 && e == 0) ? ZERO4 : sc[kq][k]);
        if constexpr (u == 1 && z + 1 < 32) rdq(IC<z + 1>{}, IC<t>{});
        if constexpr (gi + 1 < NG) {
          if constexpr (sub == 0 && i < 4) rdk(IC<gi + 1>{}, IC<i & 3>{});
          if constexpr (sub == 0 && i == 5) set_m0(ring + ((gi + 1) & 1) * 4096);
          if constexpr (sub == 0 && i == 6) need(fa[(gi + 1) & 1]);
          if constexpr (sub == 0 && i >= 6) piece(IC<gi + 3>{}, IC<(i - 6) & 3>{});
        }
        if constexpr (z == 16 || z == 17) qmax(IC<z - 16>{}, I);
        if constexpr (z == 18 && (i == 1 || i == 4 || i == 7)) xmax(IC<0>{}, IC<i / 3>{});
        if constexpr (z == 19 && i >= 5) sfor<0, MT>([&](auto H) { nm[H * 5 + i - 5] = mul1(mx[H * 5 + i - 5], -SCL); });
        if constexpr (z >= 20) { constexpr int x = (z - 20) * 10 + i; sfor<(3 * x + 1) / 2, (3 * x + 4) / 2>(mop); }
        if constexpr (!GQA && z == 26) qmax(IC<2>{}, I);
        SBAR();
      });
    });
    STAMP2(2, 32);
    STAMP(ST + 3);
    sfor<4 - MT, 4>([&](auto KQ) { sfor<0, 10>([&](auto K) { qmax(KQ, K); }); });
    sfor<0, 3>([&](auto C) { xmax(IC<1>{}, C); });
    pw = (GQA ? (w >> 1) * 5120 + (w & 1) * 8 : hh * 20480 + cq * 5120) + lane_id() * 16;
    bar();
    rmax(IC<1>{});
    sfor<0, 10>([&](auto K) { pput(IC<0>{}, K, pw); });
    sfor<0, GQA ? 6 : 0>([&](auto K) { pput(IC<1>{}, K, pw); });
    bool any = false;
    sfor<0, 5 * MT>([&](auto R) { any |= __builtin_fmaf(mx[R], SCL, nm[R]) > GROW; });
    grown = __builtin_amdgcn_ballot_w64(any) != 0;
    if (__builtin_expect(grown, 0)) {
      sfor<0, 5 * MT>([&](auto R) {
        const float d = __builtin_fmaf(mx[R], SCL, nm[R]);
        if (d > GROW) { ls[R] *= __builtin_amdgcn_exp2f(-d); nm[R] = -SCL * mx[R]; }
      });
    }
  }
  STAMP(ST + 4);

  const unsigned wwin = hh ? 512 + (cq * 96 & ~63) : w * 128;
  const rsrc_t rO = mk_rsrc(p.wo[P], 512 * j * 896 + wwin, W_BYTES - (512 * j * 896 + wwin));
  {
    const int l = lane_id(), g = l >> 4, q = (l >> 2) & 3;
    const unsigned vv = voff_spl(l, 512);
    const unsigned a0 = LDS_RING + (GQA ? cq : w) * 8192 + (4 * g + q) * 128 + (((q >> 1) | ((g & 1) << 1)) * 32) + 8 * (l & 3);
    const unsigned p0 = hh * 20480 + l * 16;
    auto rda = [&](auto S, auto U) {
      lds_t a = lds + (a0 ^ (U * 32)) + (GQA ? (S & 3) * 2048 : (S & 1) * 4096);
      fa[S & 1][U] = ldtr(a, a + (GQA ? 32768 : 2048));
    };
    auto rdb = [&](auto S, auto T) { fb[T] = ld128(lds + p0 + ((S >> 2) & 1) * 40960 + ((S & 3) * 5 + T) * 1024); };
    unsigned ov = 0, ow = 0;
    auto piece = [&](auto G, auto I) {
      if constexpr (G < 16) dmap<GQA, I>(rV, vv, (G * 32 + I * 8) * 512);
      else dmap<false, I>(rO, ov, ((G - 16) * (GQA ? 16 : 32) + I * 8) * 896);
    };
    constexpr auto TW = [](int s) -> int { return s == 12; };
    unsigned seen = 0;
    float rl[5], rs[2] = {0.f, 0.f};
    f32x4 lv[2];
    unsigned el = 0, eg = 0;
    auto oput = [&](auto T, auto H) {
      const f32x4 u = acc[T][2 * H], v = acc[T][2 * H + 1];
      const float r = rl[T];
      *(LAS u32x4*)(lds + ow + H * 5120 + T * 1024) = u32x4{pk2(mul1(u[0], r), mul1(u[1], r)), pk2(mul1(u[2], r), mul1(u[3], r)),
                                                            pk2(mul1(v[0], r), mul1(v[1], r)), pk2(mul1(v[2], r), mul1(v[3], r))};
    };
    if constexpr (GQA) {
      wait_vm_bar<6>();
      sfor<0, 4>([&](auto U) { rda(IC<0>{}, U); });
    } else {
      wait_vm<4>();
      sfor<0, 4>([&](auto U) { rda(IC<0>{}, U); });
      set_m0(ring);
      bar();
      sfor<0, 4>([&](auto I) { piece(IC<2>{}, I); });
    }
    STAMP(ST + 5);
    sfor<0, 5>([&](auto T) { rdb(IC<0>{}, T); });
    sfor<0, 16>([&](auto S) {
      constexpr int s = S;
      SBAR();
      STAMP2(3, s);
      if constexpr (s == 0) prio_reset();
      if constexpr (GQA) {
        constexpr int nv = 4 + TW(s - 1) + TW(s - 2) + TW(s - 3);
        if constexpr ((s & 3) == 3) wait_vm_bar<nv>();
        else { need(fa[s & 1]); wait_vm_bar_raw<nv>(); }
      } else {
        if constexpr (s == 15) bar();
        else { if constexpr ((s & 3) == 3) bar(); wait_vm<4 + TW(s - 1) + TW(s - 2)>(); }
      }
      if constexpr (s == 8) {
        if (__builtin_expect(grown, 0)) {
          LAS float* em = (LAS float*)(lds + LDS_EXM + (hh * 16 + (lane_id() & 15)) * 48);
          sfor<0, 5>([&](auto T) {
            const float d = __builtin_fmaf(em[384 + T], SCL, mul1(em[T + 0], -SCL));
            const float a = d > GROW ? __builtin_amdgcn_exp2f(-d) : 1.f;
            sfor<0, 4>([&](auto U) { acc[T][U] *= a; });
          });
        }
      }
      if constexpr (s == 11) ov = voff_spl(lane_id(), 896);
      if constexpr (s == 15) ow = hh * 40960 + cq * 10240 + lane_id() * 16;
      sfor<0, 20>([&](auto I) {
        constexpr int i = I, t = i >> 2, u = i & 3;
        acc[t][u] = MFMA(fa[s & 1][u], fb[t], s == 0 ? ZERO4 : acc[t][u]);
        if constexpr (GQA) {
          if constexpr (i == 0) set_m0(ring + (s & 3) * 2048);
          if constexpr (i == 1 || i == 2) piece(IC<s + 4>{}, IC<i - 1>{});
          if constexpr (s < 15 && i >= 4 && i < 7) rda(IC<s + 1>{}, IC<i - 4>{});
          if constexpr (s < 15 && i == 8) rda(IC<s + 1>{}, IC<3>{});
        } else if constexpr (s < 15) {
          if constexpr (i < 4) rda(IC<s + 1>{}, IC<i & 3>{});
          if constexpr (i == 5) set_m0(ring + ((s + 1) & 1) * 4096);
          if constexpr (i == DM0) need(fa[(s + 1) & 1]);
          if constexpr (i >= DM0 && i < DM0 + 4) piece(IC<s + 3>{}, IC<(i - DM0) & 3>{});
        }
        if constexpr (s < 15 && u == 3) rdb(IC<s + 1>{}, IC<t>{});
        if constexpr (s < 10 && i >= 1 && i < 19) mop(IC<(180 + 18 * s + i - 1) % 360>{});
        if constexpr (s < 3 && (i == 5 || i == 10 || i == 14 || i == 18)) {
          constexpr int k = (GQA ? 6 : 0) + 4 * s + (i == 5 ? 0 : i == 10 ? 1 : i == 14 ? 2 : 3);
          if constexpr (k < 10) pput(IC<1>{}, IC<k % 10>{}, pw);
        }
        if constexpr (s >= 3 && s <= 10 && (i == 5 || i == 10 || i == 14)) {
          constexpr int q4 = (s - 3) & 3, k = q4 * 3 - (q4 > 2) + (i == 5 ? 0 : i == 10 ? 1 : 2);
          if constexpr (k < 10 && !(q4 == 2 && i == 14)) pput(IC<s < 7 ? 2 : 3>{}, IC<k % 10>{}, pw);
        }
        if constexpr (s == 10) {
          if constexpr (i == 0) { const int l2 = lane_id(); el = LDS_EXL + ((GQA ? 0 : hh) * 80 + (l2 & 15)) * 48 + (GQA ? w : cq) * 4; eg = l2 >> 4; }
          if constexpr (i == 2) rs[0] = sw16<OpAdd>(ls[0], ls[1]);
          if constexpr (i == 3) rs[1] = sw16<OpAdd>(ls[2], ls[3]);
          if constexpr (i == 4) rs[0] = sw32<OpAdd>(rs[0], rs[1]);
          if constexpr (i == 6) *(LAS float*)(lds + el + eg * 768) = rs[0];
          if constexpr (i == 7) rs[1] = sw16<OpAdd>(ls[4], ls[5 * MT - 1]);
          if constexpr (i == 8) rs[1] = sw32<OpAdd>(rs[1], rs[1]);
          if constexpr (i == 9) *(LAS float*)(lds + el + 3072 + (GQA ? (eg & 1) * 3840 : 0)) = rs[1];
          if constexpr (GQA) {
            if constexpr (i == 11) rs[0] = sw16<OpAdd>(ls[5], ls[6]);
            if constexpr (i == 12) rs[1] = sw16<OpAdd>(ls[7], ls[8]);
            if constexpr (i == 13) rs[0] = sw32<OpAdd>(rs[0], rs[1]);
            if constexpr (i == 16) *(LAS float*)(lds + el + 3840 + eg * 768) = rs[0];
          }
          if constexpr (i >= 2 && i < 14) asm volatile("" : "+v"(rs[0]), "+v"(rs[1]));
        }
        if constexpr (TW(s) && i == 14) { seen = __hip_atomic_load(sync_word<LAST ? 1 : 0>(p, b, j), RLX); asm volatile("" ::: "memory"); }
        if constexpr (s >= 11 && s < 14) {
          constexpr int x = (s - 11) * 20 + i - 2, T = x / 10, c = x % 10;
          if constexpr (x == -2) el = LDS_EXL + (hh * 80 + (lane_id() & 15)) * 48;
          if constexpr (x >= 0 && x < 50) {
            if constexpr (c == 0) { lv[0] = *(LAS const f32x4*)(lds + el + T * 768); if constexpr (GQA) lv[1] = *(LAS const f32x4*)(lds + el + T * 768 + 16); }
            if constexpr (c == 4) rs[0] = (lv[0][0] + lv[0][1]) + lv[0][2];
            if constexpr (c == 5) rs[0] = GQA ? (rs[0] + lv[0][3]) + lv[1][0] : rs[0] + lv[0][3];
            if constexpr (c == 6 && GQA) rs[0] = (rs[0] + lv[1][1]) + lv[1][2];
            if constexpr (c == 7 && GQA) rs[0] += lv[1][3];
            if constexpr (c == 8) rl[T % 5] = __builtin_amdgcn_rcpf(rs[0]);
            if constexpr (c >= 4 && c < 8) asm volatile("" : "+v"(rs[0]));
            if constexpr (c == 8) asm volatile("" : "+v"(rl[T % 5]));
          }
        }
        if constexpr (s == 15 && i >= 6 && (i & 1) == 0) oput(IC<(i - 6) / 4>{}, IC<((i - 6) / 2) % 2>{});
        SBAR();
      });
    });
    STAMP2(3, 16);
    STAMP(ST + 6);
    sfor<7, 10>([&](auto I) { oput(IC<I / 2>{}, IC<I % 2>{}); });
    settle<LAST ? 1 : 0>(p, lds, b, j, seen);
  }

  RedRows red;
  auto oproj = [&](auto NTILE) {
    constexpr int NT = NTILE, NS = 5 * NT, D0 = NT == 4 ? DM0 : 5;
    const rsrc_t rN = mk_rsrc(p.wq[1 - P], (512 * j + 64 * w) * 2, W_BYTES - (512 * j + 64 * w) * 2);
    const int l = lane_id(), g = l >> 4, q = (l >> 2) & 3;
    const unsigned ov = voff_spl(l, 896), we = voff_nat(l, 8192);
    auto piece = [&](auto G, auto I) {
      if constexpr (G < 16) dmap<false, I>(rO, ov, (G * 32 + I * 8) * 896);
      else dmap<false, I>(rN, we ^ ((I & 1) * 64), ((G - 16) * 32 + I * 8) * 8192);
    };
    unsigned a0[NT], o1 = l * 16 + 40960;
    sfor<0, NT>([&](auto U) {
      a0[U] = LDS_RING + w * 8192 + (4 * g + q) * 128 + ((((q >> 1) | ((g & 1) << 1)) ^ (U + (NT == 3 ? cq & 1 : 0))) * 32) + 8 * (l & 3);
    });
    asm volatile("" : "+v"(o1));
    auto rda = [&](auto S, auto U) { lds_t a = lds + a0[U] + (S & 1) * 4096; fa[S & 1][U] = ldtr(a, a + 2048); };
    auto rdb = [&](auto S, auto T) { fb[T] = ld128(lds + (S < 8 ? l * 16 : o1) + ((S & 7) * 5 + T) * 1024); };
    auto needa = [&](auto X) {
      if constexpr (NT == 4) need(fa[X]);
      else need3(fa[X]);
    };
    const rsrc_t rS = mk_rsrc(p.slab, ((P * 32 + b) * 8 + j) * SLAB + (NT == 4 ? w * 10240 : 40960 + cq * 8192), 10240);
    auto sput = [&](auto R) {
      constexpr int r = R;
      constexpr int tu = NT == 4 ? r / 2 : r < 5 ? r : 2 * (r - 5), cu = NT == 4 ? 2 * (r & 1) : r < 5 ? 0 : 2;
      constexpr int tv = NT == 4 ? r / 2 : r < 5 ? r : r == 7 ? 4 : 2 * (r - 5) + 1, cv = NT == 4 ? 2 * (r & 1) + 1 : r < 5 ? 1 : 2;
      const f32x4 u = acc[tu][cu], v = acc[tv][cv];
      auto r0 = __builtin_amdgcn_permlane16_swap(pk2(u[0], u[1]), pk2(v[0], v[1]), false, false);
      auto r1 = __builtin_amdgcn_permlane16_swap(pk2(u[2], u[3]), pk2(v[2], v[3]), false, false);
      __builtin_amdgcn_raw_buffer_store_b128(u32x4{r0[0], r1[0], r0[1], r1[1]}, rS, l * 16 + r * 1024, 0, 16  );
    };
    wait_vm<4>();
    sfor<0, NT>([&](auto U) { rda(IC<0>{}, U); });
    set_m0(ring);
    bar();
    STAMP(ST + 7);
    sfor<0, 5>([&](auto T) { rdb(IC<0>{}, T); });
    sfor<0, 4>([&](auto I) { piece(IC<2>{}, I); });
    sfor<0, 16>([&](auto S) {
      constexpr int s = S;
      SBAR();
      STAMP2(5, s);
      if constexpr (s == 0) prio_reset();
      if constexpr (s == PO) prio_yield(hh);
      if constexpr (s < 15) wait_vm<4>();
      else if constexpr (!LAST) { bar(); issue_x(p, 1 - P, b, w, lds0); }
      sfor<0, NS>([&](auto I) {
        constexpr int i = I, t = i / NT, u = i % NT;
        acc[t][u] = MFMA(fa[s & 1][u], fb[t], s == 0 ? ZERO4 : acc[t][u]);
        if constexpr (s < 15) {
          if constexpr (i < NT) rda(IC<s + 1>{}, IC<i % NT>{});
          if constexpr (u == NT - 1) rdb(IC<s + 1>{}, IC<t>{});
          if constexpr (!(LAST && s >= 13)) {
            if constexpr (i == D0 - 2) set_m0(ring + ((s + 1) & 1) * 4096);
            if constexpr (i == D0) needa(IC<(s + 1) & 1>{});
            if constexpr (i >= D0 && i < D0 + 4) piece(IC<s + 3>{}, IC<(i - D0) & 3>{});
          }
          if constexpr (LAST && s >= 13 && i >= 3 && i < 15 && i % 3 == 0) red_load<0, (4 * (s - 13) + i / 3 - 1) & 7>(red, p, 1 - P, b, j, w);
        } else if constexpr (NT == 4) {
          if constexpr (i >= 6 && (i & 1) == 0) sput(IC<(i - 6) / 2>{});
        } else {
          if constexpr (i == 5 || i == 8 || i == 11 || i == 14) sput(IC<(i - 5) / 3>{});
          if constexpr (i == 10) sput(IC<5>{});
        }
        SBAR();
      });
    });
    STAMP2(5, 16);
    STAMP(ST + 8);
    if constexpr (NT == 4) sfor<7, 10>(sput);
    else { sput(IC<6>{}); sput(IC<4>{}); sput(IC<7>{}); }
    if constexpr (!LAST) wait_vm_bar<NT == 4 ? 10 : 8>();
  };
  if (hh == 0) oproj(IC<4>{});
  else oproj(IC<3>{});
  STAMP(ST + 9);
  if constexpr (LAST) {
    wait_vm<0>();
    STAMP(21);
    bar();
    STAMP(22);
    if (w == 0) arrive<2>(p, lds, b, j);
    if (w == 7) sfor<0, 8>([&](auto J) { red_load<1, J>(red, p, 1 - P, b, j, w); });
    red_store(red, p, 1 - P, b, j, w);
    if (w == 0) settle<2>(p, lds, b, j, __hip_atomic_load(sync_word<2>(p, b, j), RLX));
    bar();
    STAMP(23);
    sfor<0, 8>([&](auto J) { red_load<0, J>(red, p, P, b, j, w); });
    if (w == 7) sfor<0, 8>([&](auto J) { red_load<1, J>(red, p, P, b, j, w); });
    red_store(red, p, P, b, j, w);
  }
}

__global__ __launch_bounds__(512, 2) void fused_attn_kernel(Params p) {
  extern __shared__ __attribute__((aligned(16))) char smem[];
  lds_t lds = (lds_t)smem;
  const unsigned lds0 = (unsigned)(uintptr_t)lds;
  const int w = __builtin_amdgcn_readfirstlane(threadIdx.x >> 6);
  const int j = blockIdx.x & 7, b = blockIdx.x >> 3;
  const int first = b & 1;
  STAMP(0);
  {
    {
      unsigned long long pc;
      asm volatile("s_getpc_b64 %0" : "=s"(pc));
      warm(lds0, mk_rsrc((const void*)(pc & ~127ull), (b * 8 + w) * CODE_LINES * 128, (CODE_LINES - 1) * 128 + 4), lane_id() * 128);
    }
    unsigned found = 0;
    if (w == 7) found = __hip_atomic_load(flag_word(p, b, lane_id() & 7), RLX);
    asm volatile("" ::: "memory");
    issue_x(p, first, b, w, lds0);
    asm volatile("s_barrier" ::: "memory");
    const int l = lane_id();
    const rsrc_t rW = mk_rsrc(p.wq[first], (512 * j + 64 * w) * 2, W_BYTES - (512 * j + 64 * w) * 2);
    const unsigned we = voff_nat(l, 8192), ring = lds0 + LDS_RING + w * 8192;
    sfor<0, 2>([&](auto G) {
      set_m0(ring + G * 4096);
      sfor<0, 4>([&](auto I) { dmap<false, I>(rW, we ^ ((I & 1) * 64), (G * 32 + I * 8) * 8192); });
    });
    const unsigned id = (w & 3) * 64 + l;
    if (w < 4) warm(lds0, mk_rsrc(p.wq[first], 0, W_BYTES), ((b >> 1) * 28 + (id >> 3)) * 8192 + 1024 * j + (id & 7) * 128);
    else warm(lds0, mk_rsrc(p.wo[first], 0, W_BYTES), 512 * j * 896 + (b >> 1) * 28672 + id * 128);
    wait_vm_bar<9>();
    if (w == 7) {
      *(LAS unsigned*)(lds + LDS_OLD + (l & 7) * 4) = found;
      if (l < 8) __hip_atomic_store(ack_word(p, b, l, j), found + 1u, RLX);
    }
  }
  STAMP(10);
  if (first == 0) {
    run_problem<0, false, 0>(p, lds, lds0, fresh(b), fresh(j), fresh(w));
    run_problem<1, true, 10>(p, lds, lds0, fresh(b), fresh(j), fresh(w));
  } else {
    run_problem<1, false, 0>(p, lds, lds0, fresh(b), fresh(j), fresh(w));
    run_problem<0, true, 10>(p, lds, lds0, fresh(b), fresh(j), fresh(w));
  }
  STAMP(20);
}

extern "C" void kernel_launch(void* const* d_in, const int* in_sizes, int n_in, void* d_out, int out_size, void* d_ws,
                              size_t ws_size, hipStream_t stream) {
  Params p;
  for (int i = 0; i < 2; ++i) {
    p.x[i] = d_in[5 * i + 0]; p.k[i] = d_in[5 * i + 1]; p.v[i] = d_in[5 * i + 2]; p.wq[i] = d_in[5 * i + 3]; p.wo[i] = d_in[5 * i + 4];
  }
  char* top = (char*)d_ws + ((ws_size - 512 * (size_t)SLAB - 262144) & ~(size_t)4095);
  p.sync = (unsigned*)top;
  p.slab = top + 262144;
  p.out = d_out;
  p.diag = (unsigned long long*)((char*)d_ws + (64u << 20));
  (void)hipFuncSetAttribute((const void*)fused_attn_kernel, hipFuncAttributeMaxDynamicSharedMemorySize, LDS_TOTAL);
  fused_attn_kernel<<<256, 512, LDS_TOTAL, stream>>>(p);
}
```

```cpp
#include <hip/hip_runtime.h>
#include <hip/hip_bf16.h>
#include <stdint.h>
#include <type_traits>

typedef short bf16x8 __attribute__((ext_vector_type(8)));
typedef short s16x4 __attribute__((ext_vector_type(4)));
typedef float f32x4 __attribute__((ext_vector_type(4)));
typedef float f32x2 __attribute__((ext_vector_type(2)));
typedef __bf16 bf16x2 __attribute__((ext_vector_type(2)));
typedef unsigned u32x4 __attribute__((ext_vector_type(4)));
typedef unsigned u32x2 __attribute__((ext_vector_type(2)));
#define LAS __attribute__((address_space(3)))
typedef __amdgpu_buffer_rsrc_t rsrc_t;
typedef LAS char* lds_t;

#define DIAG 0
#define DLAST 1
#define PO 12
#define NTKA 1
#define NTVA 1
#define NTKB 1
#define NTVB 0
#define FAKEKV 0

constexpr int LDS_RING = 81920;
constexpr int LDS_EXM = 147456;
constexpr int LDS_EXL = 151552;
constexpr int LDS_DUMMY = 159232;
constexpr int LDS_OLD = 159488;
constexpr int LDS_TOTAL = DIAG ? 163840 : 159552;
constexpr unsigned X_BYTES = 80 * 448 * 2;
constexpr unsigned W_BYTES = 448 * 4096 * 2;
constexpr unsigned KV_HEAD = 512 * 256 * 2;
constexpr unsigned SLAB = 72 * 1024;
constexpr int CODE_LINES = 5;
constexpr float SCL = 0.0625f * 1.4426950408889634f;
constexpr float GROW = 20.f;

template <int I, int N, class F>
__device__ __forceinline__ void sfor(F&& f) {
  if constexpr (I < N) { f(std::integral_constant<int, I>{}); sfor<I + 1, N>(f); }
}
#define SBAR() __builtin_amdgcn_sched_barrier(0)
#define MFMA(a, b, c) __builtin_amdgcn_mfma_f32_16x16x32_bf16(a, b, c, 0, 0, 0)
template <int N> __device__ __forceinline__ void wait_vm() { asm volatile("s_waitcnt vmcnt(%0)" ::"i"(N) : "memory"); }
__device__ __forceinline__ void bar() { asm volatile("s_waitcnt lgkmcnt(0)\n\ts_barrier" ::: "memory"); }
template <int N> __device__ __forceinline__ void wait_vm_bar_raw() { asm volatile("s_waitcnt vmcnt(%0)\n\ts_barrier" ::"i"(N) : "memory"); }
template <int N> __device__ __forceinline__ void wait_vm_bar() { asm volatile("s_waitcnt vmcnt(%0) lgkmcnt(0)\n\ts_barrier" ::"i"(N) : "memory"); }
__device__ __forceinline__ void prio_reset() { asm volatile("s_setprio 1"); }
__device__ __forceinline__ void prio_yield(int hh) { asm volatile("s_cmp_lg_u32 %0, 0\n\ts_cbranch_scc1 1f\n\ts_setprio 0\n1:" ::"s"(hh) : "scc"); }
__device__ __forceinline__ int lane_id() {
  int l; asm volatile("v_mbcnt_lo_u32_b32 %0, -1, 0\n\tv_mbcnt_hi_u32_b32 %0, -1, %0" : "=v"(l)); return l;
}
__device__ __forceinline__ int fresh(int x) { asm volatile("" : "+s"(x)); return x; }
__device__ __forceinline__ unsigned pk2(float a, float b) {
  return __builtin_bit_cast(unsigned, __builtin_convertvector(f32x2{a, b}, bf16x2));
}
__device__ __forceinline__ rsrc_t mk_rsrc(const void* p, unsigned off, unsigned bytes) {
  return __builtin_amdgcn_make_buffer_rsrc((void*)((const char*)p + off), (short)0, (int)bytes, 0x00020000);
}
__device__ __forceinline__ void need(const bf16x8 (&f)[4]) { asm volatile("" ::"v"(f[0]), "v"(f[1]), "v"(f[2]), "v"(f[3]) : "memory"); }
__device__ __forceinline__ void need3(const bf16x8 (&f)[4]) { asm volatile("" ::"v"(f[0]), "v"(f[1]), "v"(f[2]) : "memory"); }
__device__ __forceinline__ void set_m0(unsigned m0v) { asm volatile("s_mov_b32 m0, %0\n\ts_nop 0" ::"s"(m0v) : "memory"); }
template <bool NT, int I>
__device__ __forceinline__ void dmap(rsrc_t r, unsigned v, unsigned src) {
  if constexpr (NT && !FAKEKV) asm volatile("buffer_load_dwordx4 %0, %1, %2 offen offset:%3 nt lds" ::"v"(v), "s"(r), "s"(src - 1024u * I), "i"(1024 * I) : "memory");
  else asm volatile("buffer_load_dwordx4 %0, %1, %2 offen offset:%3 lds" ::"v"(v), "s"(r), "s"(src - 1024u * I), "i"(1024 * I) : "memory");
}
__device__ __forceinline__ void warm(unsigned lds0, rsrc_t r, unsigned v) {
  asm volatile("s_mov_b32 m0, %0\n\ts_nop 0\n\tbuffer_load_dword %1, %2, 0 offen lds" ::"s"(lds0 + LDS_DUMMY), "v"(v), "s"(r) : "memory");
}
extern "C" __device__ float llvm_fmul_legacy(float, float) __asm("llvm.amdgcn.fmul.legacy");
__device__ __forceinline__ float mul1(float a, float b) { return llvm_fmul_legacy(a, b); }
__device__ __forceinline__ bf16x8 ld128(lds_t p) { return *(LAS const bf16x8*)p; }
__device__ __forceinline__ bf16x8 ldtr(lds_t p0, lds_t p1) {
  s16x4 lo = __builtin_amdgcn_ds_read_tr16_b64_v4i16((LAS s16x4*)p0);
  s16x4 hi = __builtin_amdgcn_ds_read_tr16_b64_v4i16((LAS s16x4*)p1);
  return __builtin_shufflevector(lo, hi, 0, 1, 2, 3, 4, 5, 6, 7);
}
__device__ __forceinline__ float max3(float a, float b, float c) { return __builtin_fmaxf(__builtin_fmaxf(a, b), c); }
struct OpMax { static __device__ __forceinline__ float f(float a, float b) { return max3(a, b, b); } };
struct OpAdd { static __device__ __forceinline__ float f(float a, float b) { return a + b; } };
template <class Op> __device__ __forceinline__ float sw16(float a, float b) {
  auto r = __builtin_amdgcn_permlane16_swap(__float_as_uint(a), __float_as_uint(b), false, false);
  return Op::f(__uint_as_float(r[0]), __uint_as_float(r[1]));
}
template <class Op> __device__ __forceinline__ float sw32(float a, float b) {
  auto r = __builtin_amdgcn_permlane32_swap(__float_as_uint(a), __float_as_uint(b), false, false);
  return Op::f(__uint_as_float(r[0]), __uint_as_float(r[1]));
}
template <class Op> __device__ __forceinline__ float red4(float a, float b, float c, float d) { return sw32<Op>(sw16<Op>(a, b), sw16<Op>(c, d)); }
template <class Op> __device__ __forceinline__ float red2(float a, float b) { const float x = sw16<Op>(a, b); return sw32<Op>(x, x); }

__device__ __forceinline__ unsigned voff_nat(int l, unsigned pitch) { return (l >> 3) * pitch + 16 * ((l & 7) ^ (((l >> 4) & 1) << 1)); }
__device__ __forceinline__ unsigned voff_spl(int l, unsigned pitch) { return (l >> 3) * pitch + 16 * ((l & 7) ^ (((l >> 4) & 3) << 1)); }
__device__ __forceinline__ unsigned voff_k(int l) { return (l >> 3) * 512 + 16 * ((l & 7) ^ ((l >> 4) & 3)); }

struct Params {
  const void *x[2], *k[2], *v[2], *wq[2], *wo[2];
  void* slab;
  unsigned* sync;
  void* out;
  unsigned long long* diag;
};

#define STAMP(i)
#define STAMP2(ph, i)

constexpr int DM0 = 8;
template <int V> using IC = std::integral_constant<int, V>;
constexpr f32x4 ZERO4 = {0.f, 0.f, 0.f, 0.f};

__device__ __forceinline__ void issue_x(const Params& p, int P, int b, int w, unsigned lds0) {
  w = fresh(w);
  const int l = lane_id();
  const unsigned v = (l >> 3) * 896 + 16 * ((l & 7) ^ ((l >> 4) & 3));
  const rsrc_t rx = mk_rsrc(p.x[P], b * X_BYTES, X_BYTES);
  sfor<0, 9>([&](auto I) {
    const unsigned pc = w * 9 + I, blk = (pc * 205) >> 11, rg = pc - blk * 10;
    set_m0(lds0 + pc * 1024);
    dmap<false, 0>(rx, v ^ ((rg & 1) * 64), rg * 7168 + blk * 128);
  });
}

typedef __attribute__((address_space(1))) unsigned gu32;
#define RLX __ATOMIC_RELAXED, __HIP_MEMORY_SCOPE_AGENT
__device__ __forceinline__ gu32* flag_word(const Params& p, int b, int j) { return (gu32*)p.sync + (b * 8 + j) * 32; }
__device__ __forceinline__ gu32* ack_word(const Params& p, int b, int prod, int cons) { return (gu32*)p.sync + 8192 + ((b * 8 + prod) * 8 + cons) * 16; }
template <int STAGE>
__device__ __forceinline__ void arrive(const Params& p, lds_t lds, int b, int j) {
  const unsigned old = *(LAS const unsigned*)(lds + LDS_OLD + j * 4);
  if (lane_id() == 0) __hip_atomic_store(flag_word(p, b, j), old + STAGE, RLX);
}
template <int STAGE> __device__ __forceinline__ gu32* sync_word(const Params& p, int b, int j) {
  const int c = lane_id() & 7;
  return STAGE ? flag_word(p, b, c) : ack_word(p, b, j, c);
}
template <int STAGE>
__device__ __forceinline__ void settle(const Params& p, lds_t lds, int b, int j, unsigned seen) {
  const unsigned old = *(LAS const unsigned*)(lds + LDS_OLD + (STAGE ? lane_id() & 7 : j) * 4);
  for (unsigned spins = 0; spins < (1u << 22); ++spins) {
    if (__builtin_amdgcn_ballot_w64(STAGE ? seen - old - STAGE > 2u - STAGE : seen != old + 1u) == 0) break;
    __builtin_amdgcn_s_sleep(2);
    seen = __hip_atomic_load(sync_word<STAGE>(p, b, j), RLX);
  }
  asm volatile("" ::: "memory");
}
struct RedRows { u32x4 v[2][8]; };
template <int K, int J>
__device__ __forceinline__ void red_load(RedRows& r, const Params& p, int P, int b, int j, int w) {
  const rsrc_t rS = mk_rsrc(p.slab, (P * 32 + b) * 8 * SLAB, 8 * SLAB);
  r.v[K][J] = __builtin_amdgcn_raw_buffer_load_b128(rS, lane_id() * 16, J * SLAB + (9 * j + w + K) * 1024, 16  );
}
__device__ __forceinline__ void red_store(const RedRows& r, const Params& p, int P, int b, int j, int w) {
  const int l = lane_id(), n = l & 15, g = l >> 4;
  sfor<0, 2>([&](auto K) {
    if (K == 0 || w == 7) {
      float sm[8];
      sfor<0, 4>([&](auto I) { sm[2 * I] = __uint_as_float(r.v[K][0][I + 0] << 16); sm[2 * I + 1] = __uint_as_float(r.v[K][0][I + 0] & 0xffff0000u); });
      sfor<1, 8>([&](auto J) {
        sfor<0, 4>([&](auto I) { sm[2 * I] += __uint_as_float(r.v[K][J][I + 0] << 16); sm[2 * I + 1] += __uint_as_float(r.v[K][J][I + 0] & 0xffff0000u); });
      });
      const int f = 9 * j + w + K;
      int t, col;
      if (f < 40) { const int ww = f / 10; t = (f - ww * 10) >> 1; col = 64 * ww + 32 * (f & 1) + 16 * (g & 1); }
      else { const int rr = f & 7, c0 = 256 + 48 * ((f - 40) >> 3); t = rr < 5 ? rr : 2 * (rr - 5) + (g & 1); col = c0 + (rr < 5 ? 16 * (g & 1) : 32); }
      if (t < 5)
        *(u32x4*)((char*)p.out + ((size_t)(b * 80 + t * 16 + n) * 896 + P * 448 + col + 8 * (g >> 1)) * 2) =
            u32x4{pk2(sm[0], sm[1]), pk2(sm[2], sm[3]), pk2(sm[4], sm[5]), pk2(sm[6], sm[7])};
    }
  });
}

template <int P  , bool LAST, int ST>
__device__ __forceinline__ void run_problem(const Params& p, lds_t lds, unsigned lds0, int b, int j, int w) {
  constexpr bool GQA = (P == 0);
  const int bk = FAKEKV ? 0 : b;
  const int hh = w >> 2, cq = w & 3;
  const unsigned ring = lds0 + LDS_RING + w * 8192;
  f32x4 acc[5][4];
  bf16x8 fa[2][4], fb[5];

  const unsigned koff = GQA ? (bk * 8 + j) * KV_HEAD + w * 16 * 512 : (bk * 16 + 2 * j + hh) * KV_HEAD + cq * 32 * 512;
  const rsrc_t rK = mk_rsrc(p.k[P], koff, KV_HEAD - (GQA ? w * 16 * 512 : cq * 32 * 512));
  auto k_src = [](int g, int i) -> unsigned {
    return GQA ? ((g >> 2) * 256 + (i >> 1) * 128 + (i & 1) * 8) * 512 + (g & 3) * 128 : ((g >> 2) * 128 + i * 8) * 512 + (g & 3) * 128;
  };

  {
    const rsrc_t rW = mk_rsrc(p.wq[P], (512 * j + 64 * w) * 2, W_BYTES - (512 * j + 64 * w) * 2);
    const int l = lane_id(), n = l & 15, g = l >> 4, q = (l >> 2) & 3;
    const unsigned we = voff_nat(l, 8192), ke = voff_k(l);
    const unsigned a0 = LDS_RING + w * 8192 + (8 * g + q) * 128 + (((q >> 1) | ((g & 1) << 1)) * 32) + 8 * (l & 3);
    unsigned x0[2][2];
    x0[0][0] = n * 128 + 16 * (g ^ ((n >> 1) & 7)); x0[1][0] = x0[0][0] ^ 64; x0[0][1] = x0[0][0] + 40960; x0[1][1] = x0[1][0] + 40960;
    asm volatile("" : "+v"(x0[0][1]), "+v"(x0[1][1]));
    const unsigned qw = hh * 40960 + cq * 10240 + l * 16;
    auto rda = [&](auto S, auto U) { lds_t a = lds + (a0 ^ (U * 32)) + (S & 1) * 4096; fa[S & 1][U] = ldtr(a, a + 512); };
    auto rdb = [&](auto S, auto T) { fb[T] = ld128(lds + x0[S & 1][S >= 8] + ((S >> 1) & 3) * 10240 + T * 2048); };
    auto piece = [&](auto G, auto I) {
      if constexpr (G < 14) dmap<false, I>(rW, we ^ ((I & 1) * 64), (G * 32 + I * 8) * 8192);
      else dmap<(GQA ? NTKA : NTKB) != 0, I>(rK, ke ^ ((I & 1) * 64), k_src(G - 14, I));
    };
    auto qput = [&](auto T, auto H) {
      const f32x4 u = acc[T][2 * H], v = acc[T][2 * H + 1];
      *(LAS u32x4*)(lds + qw + H * 5120 + T * 1024) = u32x4{pk2(u[0], u[1]), pk2(u[2], u[3]), pk2(v[0], v[1]), pk2(v[2], v[3])};
    };
    if constexpr (!LAST) wait_vm<5>();
    sfor<0, 4>([&](auto U) { rda(IC<0>{}, U); });
    sfor<0, 5>([&](auto T) { rdb(IC<0>{}, T); });
    set_m0(ring);
    need(fa[0]);
    sfor<0, 4>([&](auto I) { piece(IC<2>{}, I); });
    sfor<0, 14>([&](auto S) {
      constexpr int s = S;
      SBAR();
      STAMP2(4, s);
      if constexpr (s == 0) prio_reset();
      if constexpr (s == 9) prio_yield(hh);
      if constexpr (s == 0) { if constexpr (!LAST) wait_vm<5>(); }
      else if constexpr (s < 13) wait_vm<(LAST && s == 6) ? 0 : 4>();
      else {
        bar();
        *(LAS f32x2*)(lds + LDS_EXM + w * 512 + l * 8) = f32x2{-INFINITY, -INFINITY};
        if constexpr (LAST) { if (w == 0) arrive<1>(p, lds, b, j); }
      }
      sfor<0, 20>([&](auto I) {
        constexpr int i = I, t = i >> 2, u = i & 3;
        acc[t][u] = MFMA(fa[s & 1][u], fb[t], s == 0 ? ZERO4 : acc[t][u]);
        if constexpr (s < 13) {
          if constexpr (i < 4) rda(IC<s + 1>{}, IC<i & 3>{});
          if constexpr (u == 3) rdb(IC<s + 1>{}, IC<t>{});
          if constexpr (i == 5) set_m0(ring + ((s + 1) & 1) * 4096);
          if constexpr (i == DM0) need(fa[(s + 1) & 1]);
          if constexpr (i >= DM0 && i < DM0 + 4) piece(IC<s + 3>{}, IC<(i - DM0) & 3>{});
        } else if constexpr (i >= 6 && (i & 1) == 0) qput(IC<(i - 6) / 4>{}, IC<((i - 6) / 2) % 2>{});
        SBAR();
      });
    });
    STAMP2(4, 14);
    STAMP(ST + 1);
    sfor<7, 10>([&](auto I) { qput(IC<I / 2>{}, IC<I % 2>{}); });
    bar();
  }
  STAMP(ST + 2);

  const rsrc_t rV = GQA ? mk_rsrc(p.v[P], (bk * 8 + j) * KV_HEAD + hh * 16 * 512 + cq * 128, KV_HEAD - (hh * 16 * 512 + cq * 128))
                        : mk_rsrc(p.v[P], (bk * 16 + 2 * j + hh) * KV_HEAD + cq * 128, KV_HEAD - cq * 128);
  constexpr int MT = GQA ? 2 : 1;
  constexpr int NG = GQA ? 8 : 16;
  f32x4 sc[4][10];
  float mx[5 * MT], nm[5 * MT], ls[5 * MT], ev[4];
  u32x2 pq[4][10];
  auto mop = [&](auto ID) {
    constexpr int id = ID, kq = id / 90, k = (id / 9) % 10, part = id % 9, r = GQA ? k : (k >> 1);
    if constexpr (part == 0) { ev[0] = __builtin_fmaf(sc[kq][k][0], SCL, nm[r]); ev[1] = __builtin_fmaf(sc[kq][k][1], SCL, nm[r]); }
    if constexpr (part == 1) ev[0] = __builtin_amdgcn_exp2f(ev[0]);
    if constexpr (part == 2) { ev[2] = __builtin_fmaf(sc[kq][k][2], SCL, nm[r]); ev[3] = __builtin_fmaf(sc[kq][k][3], SCL, nm[r]); }
    if constexpr (part == 3) ev[1] = __builtin_amdgcn_exp2f(ev[1]);
    if constexpr (part == 4) ev[2] = __builtin_amdgcn_exp2f(ev[2]);
    if constexpr (part == 5) { if constexpr (kq == 0 && (GQA || (k & 1) == 0)) ls[r] = ev[0] + ev[1]; else ls[r] = (ls[r] + ev[0]) + ev[1]; }
    if constexpr (part == 6) ev[3] = __builtin_amdgcn_exp2f(ev[3]);
    if constexpr (part == 7) { pq[kq][k][0] = pk2(ev[0], ev[1]); ls[r] += ev[2]; }
    if constexpr (part == 8) { pq[kq][k][1] = pk2(ev[2], ev[3]); ls[r] += ev[3]; }
    if constexpr (part == 0) asm volatile("" : "+v"(ev[0]), "+v"(ev[1]));
    if constexpr (part == 1) asm volatile("" : "+v"(ev[0]));
    if constexpr (part == 2) asm volatile("" : "+v"(ev[2]), "+v"(ev[3]));
    if constexpr (part == 3) asm volatile("" : "+v"(ev[1]));
    if constexpr (part == 4) asm volatile("" : "+v"(ev[2]));
    if constexpr (part == 5) asm volatile("" : "+v"(ls[r]));
    if constexpr (part == 6) asm volatile("" : "+v"(ev[3]));
    if constexpr (part == 7) asm volatile("" : "+v"(ls[r]), "+v"(pq[kq][k][0]));
    if constexpr (part == 8) asm volatile("" : "+v"(ls[r]), "+v"(pq[kq][k][1]));
  };
  auto pput = [&](auto KQ, auto K, unsigned pw) {
    constexpr int kq = KQ, k = K;
    if constexpr (GQA) *(LAS u32x2*)(lds + pw + (kq & 1) * 40960 + (k / 5) * 20480 + (k % 5) * 1024) = pq[kq][k];
    else if constexpr (k & 1) *(LAS u32x4*)(lds + pw + (kq & 1) * 40960 + (k / 2) * 1024) = u32x4{pq[kq][k - 1][0], pq[kq][k - 1][1], pq[kq][k][0], pq[kq][k][1]};
  };
  bool grown;
  unsigned pw;
  {
    const int l = lane_id(), n = l & 15, g = l >> 4;
    const unsigned ke = voff_k(l);
    unsigned vv = 0;
    const unsigned k0 = LDS_RING + w * 8192 + n * 128 + 16 * ((g >> 1) ^ ((n >> 1) & 7)) + 8 * (g & 1);
    const unsigned q0 = (GQA ? 0 : hh * 40960) + l * 16;
    unsigned q1 = q0 + 40960;
    asm volatile("" : "+v"(q1));
    constexpr int NSUB = 2 * MT;
    auto rdk = [&](auto G, auto I) {
      lds_t a = lds + (I & 1) * 2048 + (G & 1) * 4096;
      const s16x4 lo = *(LAS const s16x4*)(a + (k0 ^ ((I >> 1) * 64))), hi = *(LAS const s16x4*)(a + (k0 ^ ((I >> 1) * 64 + 32)));
      fa[G & 1][I] = __builtin_shufflevector(lo, hi, 0, 1, 2, 3, 4, 5, 6, 7);
    };
    auto rdq = [&](auto Z, auto T) {
      constexpr int z = Z, gi = z / NSUB, e = (z % NSUB) / MT, h = z % MT;
      fb[T] = ld128(lds + (h ? q1 : q0) + ((2 * (gi & 3) + e) * 5 + T) * 1024);
    };
    auto piece = [&](auto G, auto I) {
      if constexpr (G < NG) dmap<(GQA ? NTKA : NTKB) != 0, I>(rK, ke ^ ((I & 1) * 64), k_src(G, I));
      else if constexpr (!GQA) dmap<(GQA ? NTVA : NTVB) != 0, I>(rV, vv, ((G - NG) * 32 + I * 8) * 512);
      else dmap<(GQA ? NTVA : NTVB) != 0, I>(rV, vv, ((2 * (G - NG) + (I >> 1)) * 32 + (I & 1) * 8) * 512);
    };
    auto qmax = [&](auto KQ, auto K) {
      constexpr int kq = KQ, k = K, r = GQA ? k : (k >> 1);
      if constexpr ((kq & 1) == 0 && (GQA || (k & 1) == 0)) mx[r] = max3(max3(sc[kq][k][0], sc[kq][k][1], sc[kq][k][2]), sc[kq][k][3], sc[kq][k][3]);
      else mx[r] = max3(max3(mx[r], sc[kq][k][0], sc[kq][k][1]), sc[kq][k][2], sc[kq][k][3]);
      asm volatile("" : "+v"(mx[r]));
    };
    auto xmax = [&](auto HALF, auto C) {
      const int l2 = lane_id(), g2 = l2 >> 4;
      LAS float* em = (LAS float*)(lds + LDS_EXM + HALF * 1536 + ((GQA ? 0 : hh) * 16 + (l2 & 15)) * 48);
      if constexpr (C == 0) __builtin_amdgcn_ds_fmaxf(em + g2, red4<OpMax>(mx[0], mx[1], mx[2], mx[3]), 0, 0, false);
      if constexpr (C == 1 && GQA) __builtin_amdgcn_ds_fmaxf(em + (g2 & 1) * 192 + 4, red2<OpMax>(mx[4], mx[5 * MT - 1]), 0, 0, false);
      if constexpr (C == 1 && !GQA) __builtin_amdgcn_ds_fmaxf(em + 4, red2<OpMax>(mx[4], mx[4]), 0, 0, false);
      if constexpr (C == 2 && GQA) __builtin_amdgcn_ds_fmaxf(em + 192 + g2, red4<OpMax>(mx[5], mx[6], mx[7], mx[8]), 0, 0, false);
    };
    auto rmax = [&](auto HALF) {
      LAS float* em = (LAS float*)(lds + LDS_EXM + HALF * 1536 + ((GQA ? 0 : hh) * 16 + (lane_id() & 15)) * 48);
      sfor<0, MT>([&](auto H) {
        const f32x4 v = *(LAS const f32x4*)(em + H * 192);
        sfor<0, 4>([&](auto T) { mx[H * 5 + T] = v[T + 0]; });
        mx[H * 5 + 4] = em[H * 192 + 4];
      });
    };
    wait_vm<4>();
    sfor<0, 4>([&](auto I) { rdk(IC<0>{}, I); });
    sfor<0, 5>([&](auto T) { rdq(IC<0>{}, T); });
    set_m0(ring);
    need(fa[0]);
    sfor<0, 4>([&](auto I) { piece(IC<2>{}, I); });
    sfor<0, 32>([&](auto Z) {
      constexpr int z = Z, gi = z / NSUB, sub = z % NSUB, e = sub / MT, h = sub % MT;
      SBAR();
      STAMP2(2, z);
      if constexpr (z == 0 || z == 20) prio_reset();
      if constexpr (z == 13 || z == 28) prio_yield(hh);
      if constexpr (z == 19) { bar(); rmax(IC<0>{}); }
      if constexpr (z == (NG - 3) * NSUB) vv = voff_spl(lane_id(), 512);
      if constexpr (sub == 0 && gi + 1 < NG) wait_vm<4>();
      sfor<0, 10>([&](auto I) {
        constexpr int i = I, t = i >> 1, u = i & 1;
        constexpr int kq = GQA ? 2 * (gi >> 2) + u : (gi >> 2), k = GQA ? 5 * h + t : i;
        sc[kq][k] = MFMA(fa[gi & 1][2 * e + u], fb[t], ((gi & 3) == 0 && e == 0) ? ZERO4 : sc[kq][k]);
        if constexpr (u == 1 && z + 1 < 32) rdq(IC<z + 1>{}, IC<t>{});
        if constexpr (gi + 1 < NG) {
          if constexpr (sub == 0 && i < 4) rdk(IC<gi + 1>{}, IC<i & 3>{});
          if constexpr (sub == 0 && i == 5) set_m0(ring + ((gi + 1) & 1) * 4096);
          if constexpr (sub == 0 && i == 6) need(fa[(gi + 1) & 1]);
          if constexpr (sub == 0 && i >= 6) piece(IC<gi + 3>{}, IC<(i - 6) & 3>{});
        }
        if constexpr (z == 16 || z == 17) qmax(IC<z - 16>{}, I);
        if constexpr (z == 18 && (i == 1 || i == 4 || i == 7)) xmax(IC<0>{}, IC<i / 3>{});
        if constexpr (z == 19 && i >= 5) sfor<0, MT>([&](auto H) { nm[H * 5 + i - 5] = mul1(mx[H * 5 + i - 5], -SCL); });
        if constexpr (z >= 20) { constexpr int x = (z - 20) * 10 + i; sfor<(3 * x + 1) / 2, (3 * x + 4) / 2>(mop); }
        if constexpr (!GQA && z == 26) qmax(IC<2>{}, I);
        SBAR();
      });
    });
    STAMP2(2, 32);
    STAMP(ST + 3);
    sfor<4 - MT, 4>([&](auto KQ) { sfor<0, 10>([&](auto K) { qmax(KQ, K); }); });
    sfor<0, 3>([&](auto C) { xmax(IC<1>{}, C); });
    pw = (GQA ? (w >> 1) * 5120 + (w & 1) * 8 : hh * 20480 + cq * 5120) + lane_id() * 16;
    bar();
    rmax(IC<1>{});
    sfor<0, 10>([&](auto K) { pput(IC<0>{}, K, pw); });
    sfor<0, GQA ? 6 : 0>([&](auto K) { pput(IC<1>{}, K, pw); });
    bool any = false;
    sfor<0, 5 * MT>([&](auto R) { any |= __builtin_fmaf(mx[R], SCL, nm[R]) > GROW; });
    grown = __builtin_amdgcn_ballot_w64(any) != 0;
    if (__builtin_expect(grown, 0)) {
      sfor<0, 5 * MT>([&](auto R) {
        const float d = __builtin_fmaf(mx[R], SCL, nm[R]);
        if (d > GROW) { ls[R] *= __builtin_amdgcn_exp2f(-d); nm[R] = -SCL * mx[R]; }
      });
    }
  }
  STAMP(ST + 4);

  const unsigned wwin = hh ? 512 + (cq * 96 & ~63) : w * 128;
  const rsrc_t rO = mk_rsrc(p.wo[P], 512 * j * 896 + wwin, W_BYTES - (512 * j * 896 + wwin));
  {
    const int l = lane_id(), g = l >> 4, q = (l >> 2) & 3;
    const unsigned vv = voff_spl(l, 512);
    const unsigned a0 = LDS_RING + (GQA ? cq : w) * 8192 + (4 * g + q) * 128 + (((q >> 1) | ((g & 1) << 1)) * 32) + 8 * (l & 3);
    const unsigned p0 = hh * 20480 + l * 16;
    auto rda = [&](auto S, auto U) {
      lds_t a = lds + (a0 ^ (U * 32)) + (GQA ? (S & 3) * 2048 : (S & 1) * 4096);
      fa[S & 1][U] = ldtr(a, a + (GQA ? 32768 : 2048));
    };
    auto rdb = [&](auto S, auto T) { fb[T] = ld128(lds + p0 + ((S >> 2) & 1) * 40960 + ((S & 3) * 5 + T) * 1024); };
    unsigned ov = 0, ow = 0;
    auto piece = [&](auto G, auto I) {
      if constexpr (G < 16) dmap<(GQA ? NTVA : NTVB) != 0, I>(rV, vv, (G * 32 + I * 8) * 512);
      else dmap<false, I>(rO, ov, ((G - 16) * (GQA ? 16 : 32) + I * 8) * 896);
    };
    constexpr auto TW = [](int s) -> int { return s == 12; };
    unsigned seen = 0;
    float rl[5], rs[2] = {0.f, 0.f};
    f32x4 lv[2];
    unsigned el = 0, eg = 0;
    auto oput = [&](auto T, auto H) {
      const f32x4 u = acc[T][2 * H], v = acc[T][2 * H + 1];
      const float r = rl[T];
      *(LAS u32x4*)(lds + ow + H * 5120 + T * 1024) = u32x4{pk2(mul1(u[0], r), mul1(u[1], r)), pk2(mul1(u[2], r), mul1(u[3], r)),
                                                            pk2(mul1(v[0], r), mul1(v[1], r)), pk2(mul1(v[2], r), mul1(v[3], r))};
    };
    if constexpr (GQA) {
      wait_vm_bar<6>();
      sfor<0, 4>([&](auto U) { rda(IC<0>{}, U); });
    } else {
      wait_vm<4>();
      sfor<0, 4>([&](auto U) { rda(IC<0>{}, U); });
      set_m0(ring);
      bar();
      sfor<0, 4>([&](auto I) { piece(IC<2>{}, I); });
    }
    STAMP(ST + 5);
    sfor<0, 5>([&](auto T) { rdb(IC<0>{}, T); });
    sfor<0, 16>([&](auto S) {
      constexpr int s = S;
      SBAR();
      STAMP2(3, s);
      if constexpr (s == 0) prio_reset();
      if constexpr (GQA) {
        constexpr int nv = 4 + TW(s - 1) + TW(s - 2) + TW(s - 3);
        if constexpr ((s & 3) == 3) wait_vm_bar<nv>();
        else { need(fa[s & 1]); wait_vm_bar_raw<nv>(); }
      } else {
        if constexpr (s == 15) bar();
        else { if constexpr ((s & 3) == 3) bar(); wait_vm<4 + TW(s - 1) + TW(s - 2)>(); }
      }
      if constexpr (s == 8) {
        if (__builtin_expect(grown, 0)) {
          LAS float* em = (LAS float*)(lds + LDS_EXM + (hh * 16 + (lane_id() & 15)) * 48);
          sfor<0, 5>([&](auto T) {
            const float d = __builtin_fmaf(em[384 + T], SCL, mul1(em[T + 0], -SCL));
            const float a = d > GROW ? __builtin_amdgcn_exp2f(-d) : 1.f;
            sfor<0, 4>([&](auto U) { acc[T][U] *= a; });
          });
        }
      }
      if constexpr (s == 11) ov = voff_spl(lane_id(), 896);
      if constexpr (s == 15) ow = hh * 40960 + cq * 10240 + lane_id() * 16;
      sfor<0, 20>([&](auto I) {
        constexpr int i = I, t = i >> 2, u = i & 3;
        acc[t][u] = MFMA(fa[s & 1][u], fb[t], s == 0 ? ZERO4 : acc[t][u]);
        if constexpr (GQA) {
          if constexpr (i == 0) set_m0(ring + (s & 3) * 2048);
          if constexpr (i == 1 || i == 2) piece(IC<s + 4>{}, IC<i - 1>{});
          if constexpr (s < 15 && i >= 4 && i < 7) rda(IC<s + 1>{}, IC<i - 4>{});
          if constexpr (s < 15 && i == 8) rda(IC<s + 1>{}, IC<3>{});
        } else if constexpr (s < 15) {
          if constexpr (i < 4) rda(IC<s + 1>{}, IC<i & 3>{});
          if constexpr (i == 5) set_m0(ring + ((s + 1) & 1) * 4096);
          if constexpr (i == DM0) need(fa[(s + 1) & 1]);
          if constexpr (i >= DM0 && i < DM0 + 4) piece(IC<s + 3>{}, IC<(i - DM0) & 3>{});
        }
        if constexpr (s < 15 && u == 3) rdb(IC<s + 1>{}, IC<t>{});
        if constexpr (s < 10 && i >= 1 && i < 19) mop(IC<(180 + 18 * s + i - 1) % 360>{});
        if constexpr (s < 3 && (i == 5 || i == 10 || i == 14 || i == 18)) {
          constexpr int k = (GQA ? 6 : 0) + 4 * s + (i == 5 ? 0 : i == 10 ? 1 : i == 14 ? 2 : 3);
          if constexpr (k < 10) pput(IC<1>{}, IC<k % 10>{}, pw);
        }
        if constexpr (s >= 3 && s <= 10 && (i == 5 || i == 10 || i == 14)) {
          constexpr int q4 = (s - 3) & 3, k = q4 * 3 - (q4 > 2) + (i == 5 ? 0 : i == 10 ? 1 : 2);
          if constexpr (k < 10 && !(q4 == 2 && i == 14)) pput(IC<s < 7 ? 2 : 3>{}, IC<k % 10>{}, pw);
        }
        if constexpr (s == 10) {
          if constexpr (i == 0) { const int l2 = lane_id(); el = LDS_EXL + ((GQA ? 0 : hh) * 80 + (l2 & 15)) * 48 + (GQA ? w : cq) * 4; eg = l2 >> 4; }
          if constexpr (i == 2) rs[0] = sw16<OpAdd>(ls[0], ls[1]);
          if constexpr (i == 3) rs[1] = sw16<OpAdd>(ls[2], ls[3]);
          if constexpr (i == 4) rs[0] = sw32<OpAdd>(rs[0], rs[1]);
          if constexpr (i == 6) *(LAS float*)(lds + el + eg * 768) = rs[0];
          if constexpr (i == 7) rs[1] = sw16<OpAdd>(ls[4], ls[5 * MT - 1]);
          if constexpr (i == 8) rs[1] = sw32<OpAdd>(rs[1], rs[1]);
          if constexpr (i == 9) *(LAS float*)(lds + el + 3072 + (GQA ? (eg & 1) * 3840 : 0)) = rs[1];
          if constexpr (GQA) {
            if constexpr (i == 11) rs[0] = sw16<OpAdd>(ls[5], ls[6]);
            if constexpr (i == 12) rs[1] = sw16<OpAdd>(ls[7], ls[8]);
            if constexpr (i == 13) rs[0] = sw32<OpAdd>(rs[0], rs[1]);
            if constexpr (i == 16) *(LAS float*)(lds + el + 3840 + eg * 768) = rs[0];
          }
          if constexpr (i >= 2 && i < 14) asm volatile("" : "+v"(rs[0]), "+v"(rs[1]));
        }
        if constexpr (TW(s) && i == 14) { seen = __hip_atomic_load(sync_word<LAST ? 1 : 0>(p, b, j), RLX); asm volatile("" ::: "memory"); }
        if constexpr (s >= 11 && s < 14) {
          constexpr int x = (s - 11) * 20 + i - 2, T = x / 10, c = x % 10;
          if constexpr (x == -2) el = LDS_EXL + (hh * 80 + (lane_id() & 15)) * 48;
          if constexpr (x >= 0 && x < 50) {
            if constexpr (c == 0) { lv[0] = *(LAS const f32x4*)(lds + el + T * 768); if constexpr (GQA) lv[1] = *(LAS const f32x4*)(lds + el + T * 768 + 16); }
            if constexpr (c == 4) rs[0] = (lv[0][0] + lv[0][1]) + lv[0][2];
            if constexpr (c == 5) rs[0] = GQA ? (rs[0] + lv[0][3]) + lv[1][0] : rs[0] + lv[0][3];
            if constexpr (c == 6 && GQA) rs[0] = (rs[0] + lv[1][1]) + lv[1][2];
            if constexpr (c == 7 && GQA) rs[0] += lv[1][3];
            if constexpr (c == 8) rl[T % 5] = __builtin_amdgcn_rcpf(rs[0]);
            if constexpr (c >= 4 && c < 8) asm volatile("" : "+v"(rs[0]));
            if constexpr (c == 8) asm volatile("" : "+v"(rl[T % 5]));
          }
        }
        if constexpr (s == 15 && i >= 6 && (i & 1) == 0) oput(IC<(i - 6) / 4>{}, IC<((i - 6) / 2) % 2>{});
        SBAR();
      });
    });
    STAMP2(3, 16);
    STAMP(ST + 6);
    sfor<7, 10>([&](auto I) { oput(IC<I / 2>{}, IC<I % 2>{}); });
    settle<LAST ? 1 : 0>(p, lds, b, j, seen);
  }

  RedRows red;
  auto oproj = [&](auto NTILE) {
    constexpr int NT = NTILE, NS = 5 * NT, D0 = NT == 4 ? DM0 : 5;
    const rsrc_t rN = mk_rsrc(p.wq[1 - P], (512 * j + 64 * w) * 2, W_BYTES - (512 * j + 64 * w) * 2);
    const int l = lane_id(), g = l >> 4, q = (l >> 2) & 3;
    const unsigned ov = voff_spl(l, 896), we = voff_nat(l, 8192);
    auto piece = [&](auto G, auto I) {
      if constexpr (G < 16) dmap<false, I>(rO, ov, (G * 32 + I * 8) * 896);
      else dmap<false, I>(rN, we ^ ((I & 1) * 64), ((G - 16) * 32 + I * 8) * 8192);
    };
    unsigned a0[NT], o1 = l * 16 + 40960;
    sfor<0, NT>([&](auto U) {
      a0[U] = LDS_RING + w * 8192 + (4 * g + q) * 128 + ((((q >> 1) | ((g & 1) << 1)) ^ (U + (NT == 3 ? cq & 1 : 0))) * 32) + 8 * (l & 3);
    });
    asm volatile("" : "+v"(o1));
    auto rda = [&](auto S, auto U) { lds_t a = lds + a0[U] + (S & 1) * 4096; fa[S & 1][U] = ldtr(a, a + 2048); };
    auto rdb = [&](auto S, auto T) { fb[T] = ld128(lds + (S < 8 ? l * 16 : o1) + ((S & 7) * 5 + T) * 1024); };
    auto needa = [&](auto X) {
      if constexpr (NT == 4) need(fa[X]);
      else need3(fa[X]);
    };
    const rsrc_t rS = mk_rsrc(p.slab, ((P * 32 + b) * 8 + j) * SLAB + (NT == 4 ? w * 10240 : 40960 + cq * 8192), 10240);
    auto sput = [&](auto R) {
      constexpr int r = R;
      constexpr int tu = NT == 4 ? r / 2 : r < 5 ? r : 2 * (r - 5), cu = NT == 4 ? 2 * (r & 1) : r < 5 ? 0 : 2;
      constexpr int tv = NT == 4 ? r / 2 : r < 5 ? r : r == 7 ? 4 : 2 * (r - 5) + 1, cv = NT == 4 ? 2 * (r & 1) + 1 : r < 5 ? 1 : 2;
      const f32x4 u = acc[tu][cu], v = acc[tv][cv];
      auto r0 = __builtin_amdgcn_permlane16_swap(pk2(u[0], u[1]), pk2(v[0], v[1]), false, false);
      auto r1 = __builtin_amdgcn_permlane16_swap(pk2(u[2], u[3]), pk2(v[2], v[3]), false, false);
      __builtin_amdgcn_raw_buffer_store_b128(u32x4{r0[0], r1[0], r0[1], r1[1]}, rS, l * 16 + r * 1024, 0, 16  );
    };
    wait_vm<4>();
    sfor<0, NT>([&](auto U) { rda(IC<0>{}, U); });
    set_m0(ring);
    bar();
    STAMP(ST + 7);
    sfor<0, 5>([&](auto T) { rdb(IC<0>{}, T); });
    sfor<0, 4>([&](auto I) { piece(IC<2>{}, I); });
    sfor<0, 16>([&](auto S) {
      constexpr int s = S;
      SBAR();
      STAMP2(5, s);
      if constexpr (s == 0) prio_reset();
      if constexpr (s == PO) prio_yield(hh);
      if constexpr (s < 15) wait_vm<4>();
      else if constexpr (!LAST) { bar(); issue_x(p, 1 - P, b, w, lds0); }
      sfor<0, NS>([&](auto I) {
        constexpr int i = I, t = i / NT, u = i % NT;
        acc[t][u] = MFMA(fa[s & 1][u], fb[t], s == 0 ? ZERO4 : acc[t][u]);
        if constexpr (s < 15) {
          if constexpr (i < NT) rda(IC<s + 1>{}, IC<i % NT>{});
          if constexpr (u == NT - 1) rdb(IC<s + 1>{}, IC<t>{});
          if constexpr (!(LAST && s >= 13)) {
            if constexpr (i == D0 - 2) set_m0(ring + ((s + 1) & 1) * 4096);
            if constexpr (i == D0) needa(IC<(s + 1) & 1>{});
            if constexpr (i >= D0 && i < D0 + 4) piece(IC<s + 3>{}, IC<(i - D0) & 3>{});
          }
          if constexpr (LAST && s >= 13 && i >= 3 && i < 15 && i % 3 == 0) red_load<0, (4 * (s - 13) + i / 3 - 1) & 7>(red, p, 1 - P, b, j, w);
        } else if constexpr (NT == 4) {
          if constexpr (i >= 6 && (i & 1) == 0) sput(IC<(i - 6) / 2>{});
        } else {
          if constexpr (i == 5 || i == 8 || i == 11 || i == 14) sput(IC<(i - 5) / 3>{});
          if constexpr (i == 10) sput(IC<5>{});
        }
        SBAR();
      });
    });
    STAMP2(5, 16);
    STAMP(ST + 8);
    if constexpr (NT == 4) sfor<7, 10>(sput);
    else { sput(IC<6>{}); sput(IC<4>{}); sput(IC<7>{}); }
    if constexpr (!LAST) wait_vm_bar<NT == 4 ? 10 : 8>();
  };
  if (hh == 0) oproj(IC<4>{});
  else oproj(IC<3>{});
  STAMP(ST + 9);
  if constexpr (LAST) {
    wait_vm<0>();
    STAMP(21);
    bar();
    STAMP(22);
    if (w == 0) arrive<2>(p, lds, b, j);
    if (w == 7) sfor<0, 8>([&](auto J) { red_load<1, J>(red, p, 1 - P, b, j, w); });
    red_store(red, p, 1 - P, b, j, w);
    if (w == 0) settle<2>(p, lds, b, j, __hip_atomic_load(sync_word<2>(p, b, j), RLX));
    bar();
    STAMP(23);
    sfor<0, 8>([&](auto J) { red_load<0, J>(red, p, P, b, j, w); });
    if (w == 7) sfor<0, 8>([&](auto J) { red_load<1, J>(red, p, P, b, j, w); });
    red_store(red, p, P, b, j, w);
  }
}

__global__ __launch_bounds__(512, 2) void fused_attn_kernel(Params p) {
  extern __shared__ __attribute__((aligned(16))) char smem[];
  lds_t lds = (lds_t)smem;
  const unsigned lds0 = (unsigned)(uintptr_t)lds;
  const int w = __builtin_amdgcn_readfirstlane(threadIdx.x >> 6);
  const int j = blockIdx.x & 7, b = blockIdx.x >> 3;
  const int first = b & 1;
  STAMP(0);
  {
    {
      unsigned long long pc;
      asm volatile("s_getpc_b64 %0" : "=s"(pc));
      warm(lds0, mk_rsrc((const void*)(pc & ~127ull), (b * 8 + w) * CODE_LINES * 128, (CODE_LINES - 1) * 128 + 4), lane_id() * 128);
    }
    unsigned found = 0;
    if (w == 7) found = __hip_atomic_load(flag_word(p, b, lane_id() & 7), RLX);
    asm volatile("" ::: "memory");
    issue_x(p, first, b, w, lds0);
    asm volatile("s_barrier" ::: "memory");
    const int l = lane_id();
    const rsrc_t rW = mk_rsrc(p.wq[first], (512 * j + 64 * w) * 2, W_BYTES - (512 * j + 64 * w) * 2);
    const unsigned we = voff_nat(l, 8192), ring = lds0 + LDS_RING + w * 8192;
    sfor<0, 2>([&](auto G) {
      set_m0(ring + G * 4096);
      sfor<0, 4>([&](auto I) { dmap<false, I>(rW, we ^ ((I & 1) * 64), (G * 32 + I * 8) * 8192); });
    });
    const unsigned id = (w & 3) * 64 + l;
    if (w < 4) warm(lds0, mk_rsrc(p.wq[first], 0, W_BYTES), ((b >> 1) * 28 + (id >> 3)) * 8192 + 1024 * j + (id & 7) * 128);
    else warm(lds0, mk_rsrc(p.wo[first], 0, W_BYTES), 512 * j * 896 + (b >> 1) * 28672 + id * 128);
    wait_vm_bar<9>();
    if (w == 7) {
      *(LAS unsigned*)(lds + LDS_OLD + (l & 7) * 4) = found;
      if (l < 8) __hip_atomic_store(ack_word(p, b, l, j), found + 1u, RLX);
    }
  }
  STAMP(10);
  if (first == 0) {
    run_problem<0, false, 0>(p, lds, lds0, fresh(b), fresh(j), fresh(w));
    run_problem<1, true, 10>(p, lds, lds0, fresh(b), fresh(j), fresh(w));
  } else {
    run_problem<1, false, 0>(p, lds, lds0, fresh(b), fresh(j), fresh(w));
    run_problem<0, true, 10>(p, lds, lds0, fresh(b), fresh(j), fresh(w));
  }
  STAMP(20);
}

extern "C" void kernel_launch(void* const* d_in, const int* in_sizes, int n_in, void* d_out, int out_size, void* d_ws,
                              size_t ws_size, hipStream_t stream) {
  Params p;
  for (int i = 0; i < 2; ++i) {
    p.x[i] = d_in[5 * i + 0]; p.k[i] = d_in[5 * i + 1]; p.v[i] = d_in[5 * i + 2]; p.wq[i] = d_in[5 * i + 3]; p.wo[i] = d_in[5 * i + 4];
  }
  char* top = (char*)d_ws + ((ws_size - 512 * (size_t)SLAB - 262144) & ~(size_t)4095);
  p.sync = (unsigned*)top;
  p.slab = top + 262144;
  p.out = d_out;
  p.diag = (unsigned long long*)((char*)d_ws + (64u << 20));
  (void)hipFuncSetAttribute((const void*)fused_attn_kernel, hipFuncAttributeMaxDynamicSharedMemorySize, LDS_TOTAL);
  fused_attn_kernel<<<256, 512, LDS_TOTAL, stream>>>(p);
}
```

```cpp
#include <hip/hip_runtime.h>
#include <hip/hip_bf16.h>
#include <stdint.h>
#include <type_traits>

typedef short bf16x8 __attribute__((ext_vector_type(8)));
typedef short s16x4 __attribute__((ext_vector_type(4)));
typedef float f32x4 __attribute__((ext_vector_type(4)));
typedef float f32x2 __attribute__((ext_vector_type(2)));
typedef __bf16 bf16x2 __attribute__((ext_vector_type(2)));
typedef unsigned u32x4 __attribute__((ext_vector_type(4)));
typedef unsigned u32x2 __attribute__((ext_vector_type(2)));
#define LAS __attribute__((address_space(3)))
typedef __amdgpu_buffer_rsrc_t rsrc_t;
typedef LAS char* lds_t;

#define DIAG 0
#define DLAST 1
#define PO 12
#define NTKA 1
#define NTVA 1
#define NTKB 1
#define NTVB 2
#define FAKEKV 0

constexpr int LDS_RING = 81920;
constexpr int LDS_EXM = 147456;
constexpr int LDS_EXL = 151552;
constexpr int LDS_DUMMY = 159232;
constexpr int LDS_OLD = 159488;
constexpr int LDS_TOTAL = DIAG ? 163840 : 159552;
constexpr unsigned X_BYTES = 80 * 448 * 2;
constexpr unsigned W_BYTES = 448 * 4096 * 2;
constexpr unsigned KV_HEAD = 512 * 256 * 2;
constexpr unsigned SLAB = 72 * 1024;
constexpr int CODE_LINES = 5;
constexpr float SCL = 0.0625f * 1.4426950408889634f;
constexpr float GROW = 20.f;

template <int I, int N, class F>
__device__ __forceinline__ void sfor(F&& f) {
  if constexpr (I < N) { f(std::integral_constant<int, I>{}); sfor<I + 1, N>(f); }
}
#define SBAR() __builtin_amdgcn_sched_barrier(0)
#define MFMA(a, b, c) __builtin_amdgcn_mfma_f32_16x16x32_bf16(a, b, c, 0, 0, 0)
template <int N> __device__ __forceinline__ void wait_vm() { asm volatile("s_waitcnt vmcnt(%0)" ::"i"(N) : "memory"); }
__device__ __forceinline__ void bar() { asm volatile("s_waitcnt lgkmcnt(0)\n\ts_barrier" ::: "memory"); }
template <int N> __device__ __forceinline__ void wait_vm_bar_raw() { asm volatile("s_waitcnt vmcnt(%0)\n\ts_barrier" ::"i"(N) : "memory"); }
template <int N> __device__ __forceinline__ void wait_vm_bar() { asm volatile("s_waitcnt vmcnt(%0) lgkmcnt(0)\n\ts_barrier" ::"i"(N) : "memory"); }
__device__ __forceinline__ void prio_reset() { asm volatile("s_setprio 1"); }
__device__ __forceinline__ void prio_yield(int hh) { asm volatile("s_cmp_lg_u32 %0, 0\n\ts_cbranch_scc1 1f\n\ts_setprio 0\n1:" ::"s"(hh) : "scc"); }
__device__ __forceinline__ int lane_id() {
  int l; asm volatile("v_mbcnt_lo_u32_b32 %0, -1, 0\n\tv_mbcnt_hi_u32_b32 %0, -1, %0" : "=v"(l)); return l;
}
__device__ __forceinline__ int fresh(int x) { asm volatile("" : "+s"(x)); return x; }
__device__ __forceinline__ unsigned pk2(float a, float b) {
  return __builtin_bit_cast(unsigned, __builtin_convertvector(f32x2{a, b}, bf16x2));
}
__device__ __forceinline__ rsrc_t mk_rsrc(const void* p, unsigned off, unsigned bytes) {
  return __builtin_amdgcn_make_buffer_rsrc((void*)((const char*)p + off), (short)0, (int)bytes, 0x00020000);
}
__device__ __forceinline__ void need(const bf16x8 (&f)[4]) { asm volatile("" ::"v"(f[0]), "v"(f[1]), "v"(f[2]), "v"(f[3]) : "memory"); }
__device__ __forceinline__ void need3(const bf16x8 (&f)[4]) { asm volatile("" ::"v"(f[0]), "v"(f[1]), "v"(f[2]) : "memory"); }
__device__ __forceinline__ void set_m0(unsigned m0v) { asm volatile("s_mov_b32 m0, %0\n\ts_nop 0" ::"s"(m0v) : "memory"); }
template <int POL, int I>
__device__ __forceinline__ void dmap(rsrc_t r, unsigned v, unsigned src) {
  if constexpr (POL == 1 && !FAKEKV) asm volatile("buffer_load_dwordx4 %0, %1, %2 offen offset:%3 nt lds" ::"v"(v), "s"(r), "s"(src - 1024u * I), "i"(1024 * I) : "memory");
  else if constexpr (POL == 2) asm volatile("buffer_load_dwordx4 %0, %1, %2 offen offset:%3 sc1 lds" ::"v"(v), "s"(r), "s"(src - 1024u * I), "i"(1024 * I) : "memory");
  else if constexpr (POL == 3) asm volatile("buffer_load_dwordx4 %0, %1, %2 offen offset:%3 sc0 sc1 lds" ::"v"(v), "s"(r), "s"(src - 1024u * I), "i"(1024 * I) : "memory");
  else asm volatile("buffer_load_dwordx4 %0, %1, %2 offen offset:%3 lds" ::"v"(v), "s"(r), "s"(src - 1024u * I), "i"(1024 * I) : "memory");
}
__device__ __forceinline__ void warm(unsigned lds0, rsrc_t r, unsigned v) {
  asm volatile("s_mov_b32 m0, %0\n\ts_nop 0\n\tbuffer_load_dword %1, %2, 0 offen lds" ::"s"(lds0 + LDS_DUMMY), "v"(v), "s"(r) : "memory");
}
extern "C" __device__ float llvm_fmul_legacy(float, float) __asm("llvm.amdgcn.fmul.legacy");
__device__ __forceinline__ float mul1(float a, float b) { return llvm_fmul_legacy(a, b); }
__device__ __forceinline__ bf16x8 ld128(lds_t p) { return *(LAS const bf16x8*)p; }
__device__ __forceinline__ bf16x8 ldtr(lds_t p0, lds_t p1) {
  s16x4 lo = __builtin_amdgcn_ds_read_tr16_b64_v4i16((LAS s16x4*)p0);
  s16x4 hi = __builtin_amdgcn_ds_read_tr16_b64_v4i16((LAS s16x4*)p1);
  return __builtin_shufflevector(lo, hi, 0, 1, 2, 3, 4, 5, 6, 7);
}
__device__ __forceinline__ float max3(float a, float b, float c) { return __builtin_fmaxf(__builtin_fmaxf(a, b), c); }
struct OpMax { static __device__ __forceinline__ float f(float a, float b) { return max3(a, b, b); } };
struct OpAdd { static __device__ __forceinline__ float f(float a, float b) { return a + b; } };
template <class Op> __device__ __forceinline__ float sw16(float a, float b) {
  auto r = __builtin_amdgcn_permlane16_swap(__float_as_uint(a), __float_as_uint(b), false, false);
  return Op::f(__uint_as_float(r[0]), __uint_as_float(r[1]));
}
template <class Op> __device__ __forceinline__ float sw32(float a, float b) {
  auto r = __builtin_amdgcn_permlane32_swap(__float_as_uint(a), __float_as_uint(b), false, false);
  return Op::f(__uint_as_float(r[0]), __uint_as_float(r[1]));
}
template <class Op> __device__ __forceinline__ float red4(float a, float b, float c, float d) { return sw32<Op>(sw16<Op>(a, b), sw16<Op>(c, d)); }
template <class Op> __device__ __forceinline__ float red2(float a, float b) { const float x = sw16<Op>(a, b); return sw32<Op>(x, x); }

__device__ __forceinline__ unsigned voff_nat(int l, unsigned pitch) { return (l >> 3) * pitch + 16 * ((l & 7) ^ (((l >> 4) & 1) << 1)); }
__device__ __forceinline__ unsigned voff_spl(int l, unsigned pitch) { return (l >> 3) * pitch + 16 * ((l & 7) ^ (((l >> 4) & 3) << 1)); }
__device__ __forceinline__ unsigned voff_k(int l) { return (l >> 3) * 512 + 16 * ((l & 7) ^ ((l >> 4) & 3)); }

struct Params {
  const void *x[2], *k[2], *v[2], *wq[2], *wo[2];
  void* slab;
  unsigned* sync;
  void* out;
  unsigned long long* diag;
};

#define STAMP(i)
#define STAMP2(ph, i)

constexpr int DM0 = 8;
template <int V> using IC = std::integral_constant<int, V>;
constexpr f32x4 ZERO4 = {0.f, 0.f, 0.f, 0.f};

__device__ __forceinline__ void issue_x(const Params& p, int P, int b, int w, unsigned lds0) {
  w = fresh(w);
  const int l = lane_id();
  const unsigned v = (l >> 3) * 896 + 16 * ((l & 7) ^ ((l >> 4) & 3));
  const rsrc_t rx = mk_rsrc(p.x[P], b * X_BYTES, X_BYTES);
  sfor<0, 9>([&](auto I) {
    const unsigned pc = w * 9 + I, blk = (pc * 205) >> 11, rg = pc - blk * 10;
    set_m0(lds0 + pc * 1024);
    dmap<0, 0>(rx, v ^ ((rg & 1) * 64), rg * 7168 + blk * 128);
  });
}

typedef __attribute__((address_space(1))) unsigned gu32;
#define RLX __ATOMIC_RELAXED, __HIP_MEMORY_SCOPE_AGENT
__device__ __forceinline__ gu32* flag_word(const Params& p, int b, int j) { return (gu32*)p.sync + (b * 8 + j) * 32; }
__device__ __forceinline__ gu32* ack_word(const Params& p, int b, int prod, int cons) { return (gu32*)p.sync + 8192 + ((b * 8 + prod) * 8 + cons) * 16; }
template <int STAGE>
__device__ __forceinline__ void arrive(const Params& p, lds_t lds, int b, int j) {
  const unsigned old = *(LAS const unsigned*)(lds + LDS_OLD + j * 4);
  if (lane_id() == 0) __hip_atomic_store(flag_word(p, b, j), old + STAGE, RLX);
}
template <int STAGE> __device__ __forceinline__ gu32* sync_word(const Params& p, int b, int j) {
  const int c = lane_id() & 7;
  return STAGE ? flag_word(p, b, c) : ack_word(p, b, j, c);
}
template <int STAGE>
__device__ __forceinline__ void settle(const Params& p, lds_t lds, int b, int j, unsigned seen) {
  const unsigned old = *(LAS const unsigned*)(lds + LDS_OLD + (STAGE ? lane_id() & 7 : j) * 4);
  for (unsigned spins = 0; spins < (1u << 22); ++spins) {
    if (__builtin_amdgcn_ballot_w64(STAGE ? seen - old - STAGE > 2u - STAGE : seen != old + 1u) == 0) break;
    __builtin_amdgcn_s_sleep(2);
    seen = __hip_atomic_load(sync_word<STAGE>(p, b, j), RLX);
  }
  asm volatile("" ::: "memory");
}
struct RedRows { u32x4 v[2][8]; };
template <int K, int J>
__device__ __forceinline__ void red_load(RedRows& r, const Params& p, int P, int b, int j, int w) {
  const rsrc_t rS = mk_rsrc(p.slab, (P * 32 + b) * 8 * SLAB, 8 * SLAB);
  r.v[K][J] = __builtin_amdgcn_raw_buffer_load_b128(rS, lane_id() * 16, J * SLAB + (9 * j + w + K) * 1024, 16  );
}
__device__ __forceinline__ void red_store(const RedRows& r, const Params& p, int P, int b, int j, int w) {
  const int l = lane_id(), n = l & 15, g = l >> 4;
  sfor<0, 2>([&](auto K) {
    if (K == 0 || w == 7) {
      float sm[8];
      sfor<0, 4>([&](auto I) { sm[2 * I] = __uint_as_float(r.v[K][0][I + 0] << 16); sm[2 * I + 1] = __uint_as_float(r.v[K][0][I + 0] & 0xffff0000u); });
      sfor<1, 8>([&](auto J) {
        sfor<0, 4>([&](auto I) { sm[2 * I] += __uint_as_float(r.v[K][J][I + 0] << 16); sm[2 * I + 1] += __uint_as_float(r.v[K][J][I + 0] & 0xffff0000u); });
      });
      const int f = 9 * j + w + K;
      int t, col;
      if (f < 40) { const int ww = f / 10; t = (f - ww * 10) >> 1; col = 64 * ww + 32 * (f & 1) + 16 * (g & 1); }
      else { const int rr = f & 7, c0 = 256 + 48 * ((f - 40) >> 3); t = rr < 5 ? rr : 2 * (rr - 5) + (g & 1); col = c0 + (rr < 5 ? 16 * (g & 1) : 32); }
      if (t < 5)
        *(u32x4*)((char*)p.out + ((size_t)(b * 80 + t * 16 + n) * 896 + P * 448 + col + 8 * (g >> 1)) * 2) =
            u32x4{pk2(sm[0], sm[1]), pk2(sm[2], sm[3]), pk2(sm[4], sm[5]), pk2(sm[6], sm[7])};
    }
  });
}

template <int P  , bool LAST, int ST>
__device__ __forceinline__ void run_problem(const Params& p, lds_t lds, unsigned lds0, int b, int j, int w) {
  constexpr bool GQA = (P == 0);
  const int bk = FAKEKV ? 0 : b;
  const int hh = w >> 2, cq = w & 3;
  const unsigned ring = lds0 + LDS_RING + w * 8192;
  f32x4 acc[5][4];
  bf16x8 fa[2][4], fb[5];

  const unsigned koff = GQA ? (bk * 8 + j) * KV_HEAD + w * 16 * 512 : (bk * 16 + 2 * j + hh) * KV_HEAD + cq * 32 * 512;
  const rsrc_t rK = mk_rsrc(p.k[P], koff, KV_HEAD - (GQA ? w * 16 * 512 : cq * 32 * 512));
  auto k_src = [](int g, int i) -> unsigned {
    return GQA ? ((g >> 2) * 256 + (i >> 1) * 128 + (i & 1) * 8) * 512 + (g & 3) * 128 : ((g >> 2) * 128 + i * 8) * 512 + (g & 3) * 128;
  };

  {
    const rsrc_t rW = mk_rsrc(p.wq[P], (512 * j + 64 * w) * 2, W_BYTES - (512 * j + 64 * w) * 2);
    const int l = lane_id(), n = l & 15, g = l >> 4, q = (l >> 2) & 3;
    const unsigned we = voff_nat(l, 8192), ke = voff_k(l);
    const unsigned a0 = LDS_RING + w * 8192 + (8 * g + q) * 128 + (((q >> 1) | ((g & 1) << 1)) * 32) + 8 * (l & 3);
    unsigned x0[2][2];
    x0[0][0] = n * 128 + 16 * (g ^ ((n >> 1) & 7)); x0[1][0] = x0[0][0] ^ 64; x0[0][1] = x0[0][0] + 40960; x0[1][1] = x0[1][0] + 40960;
    asm volatile("" : "+v"(x0[0][1]), "+v"(x0[1][1]));
    const unsigned qw = hh * 40960 + cq * 10240 + l * 16;
    auto rda = [&](auto S, auto U) { lds_t a = lds + (a0 ^ (U * 32)) + (S & 1) * 4096; fa[S & 1][U] = ldtr(a, a + 512); };
    auto rdb = [&](auto S, auto T) { fb[T] = ld128(lds + x0[S & 1][S >= 8] + ((S >> 1) & 3) * 10240 + T * 2048); };
    auto piece = [&](auto G, auto I) {
      if constexpr (G < 14) dmap<0, I>(rW, we ^ ((I & 1) * 64), (G * 32 + I * 8) * 8192);
      else dmap<(GQA ? NTKA : NTKB), I>(rK, ke ^ ((I & 1) * 64), k_src(G - 14, I));
    };
    auto qput = [&](auto T, auto H) {
      const f32x4 u = acc[T][2 * H], v = acc[T][2 * H + 1];
      *(LAS u32x4*)(lds + qw + H * 5120 + T * 1024) = u32x4{pk2(u[0], u[1]), pk2(u[2], u[3]), pk2(v[0], v[1]), pk2(v[2], v[3])};
    };
    if constexpr (!LAST) wait_vm<5>();
    sfor<0, 4>([&](auto U) { rda(IC<0>{}, U); });
    sfor<0, 5>([&](auto T) { rdb(IC<0>{}, T); });
    set_m0(ring);
    need(fa[0]);
    sfor<0, 4>([&](auto I) { piece(IC<2>{}, I); });
    sfor<0, 14>([&](auto S) {
      constexpr int s = S;
      SBAR();
      STAMP2(4, s);
      if constexpr (s == 0) prio_reset();
      if constexpr (s == 9) prio_yield(hh);
      if constexpr (s == 0) { if constexpr (!LAST) wait_vm<5>(); }
      else if constexpr (s < 13) wait_vm<(LAST && s == 6) ? 0 : 4>();
      else {
        bar();
        *(LAS f32x2*)(lds + LDS_EXM + w * 512 + l * 8) = f32x2{-INFINITY, -INFINITY};
        if constexpr (LAST) { if (w == 0) arrive<1>(p, lds, b, j); }
      }
      sfor<0, 20>([&](auto I) {
        constexpr int i = I, t = i >> 2, u = i & 3;
        acc[t][u] = MFMA(fa[s & 1][u], fb[t], s == 0 ? ZERO4 : acc[t][u]);
        if constexpr (s < 13) {
          if constexpr (i < 4) rda(IC<s + 1>{}, IC<i & 3>{});
          if constexpr (u == 3) rdb(IC<s + 1>{}, IC<t>{});
          if constexpr (i == 5) set_m0(ring + ((s + 1) & 1) * 4096);
          if constexpr (i == DM0) need(fa[(s + 1) & 1]);
          if constexpr (i >= DM0 && i < DM0 + 4) piece(IC<s + 3>{}, IC<(i - DM0) & 3>{});
        } else if constexpr (i >= 6 && (i & 1) == 0) qput(IC<(i - 6) / 4>{}, IC<((i - 6) / 2) % 2>{});
        SBAR();
      });
    });
    STAMP2(4, 14);
    STAMP(ST + 1);
    sfor<7, 10>([&](auto I) { qput(IC<I / 2>{}, IC<I % 2>{}); });
    bar();
  }
  STAMP(ST + 2);

  const rsrc_t rV = GQA ? mk_rsrc(p.v[P], (bk * 8 + j) * KV_HEAD + hh * 16 * 512 + cq * 128, KV_HEAD - (hh * 16 * 512 + cq * 128))
                        : mk_rsrc(p.v[P], (bk * 16 + 2 * j + hh) * KV_HEAD + cq * 128, KV_HEAD - cq * 128);
  constexpr int MT = GQA ? 2 : 1;
  constexpr int NG = GQA ? 8 : 16;
  f32x4 sc[4][10];
  float mx[5 * MT], nm[5 * MT], ls[5 * MT], ev[4];
  u32x2 pq[4][10];
  auto mop = [&](auto ID) {
    constexpr int id = ID, kq = id / 90, k = (id / 9) % 10, part = id % 9, r = GQA ? k : (k >> 1);
    if constexpr (part == 0) { ev[0] = __builtin_fmaf(sc[kq][k][0], SCL, nm[r]); ev[1] = __builtin_fmaf(sc[kq][k][1], SCL, nm[r]); }
    if constexpr (part == 1) ev[0] = __builtin_amdgcn_exp2f(ev[0]);
    if constexpr (part == 2) { ev[2] = __builtin_fmaf(sc[kq][k][2], SCL, nm[r]); ev[3] = __builtin_fmaf(sc[kq][k][3], SCL, nm[r]); }
    if constexpr (part == 3) ev[1] = __builtin_amdgcn_exp2f(ev[1]);
    if constexpr (part == 4) ev[2] = __builtin_amdgcn_exp2f(ev[2]);
    if constexpr (part == 5) { if constexpr (kq == 0 && (GQA || (k & 1) == 0)) ls[r] = ev[0] + ev[1]; else ls[r] = (ls[r] + ev[0]) + ev[1]; }
    if constexpr (part == 6) ev[3] = __builtin_amdgcn_exp2f(ev[3]);
    if constexpr (part == 7) { pq[kq][k][0] = pk2(ev[0], ev[1]); ls[r] += ev[2]; }
    if constexpr (part == 8) { pq[kq][k][1] = pk2(ev[2], ev[3]); ls[r] += ev[3]; }
    if constexpr (part == 0) asm volatile("" : "+v"(ev[0]), "+v"(ev[1]));
    if constexpr (part == 1) asm volatile("" : "+v"(ev[0]));
    if constexpr (part == 2) asm volatile("" : "+v"(ev[2]), "+v"(ev[3]));
    if constexpr (part == 3) asm volatile("" : "+v"(ev[1]));
    if constexpr (part == 4) asm volatile("" : "+v"(ev[2]));
    if constexpr (part == 5) asm volatile("" : "+v"(ls[r]));
    if constexpr (part == 6) asm volatile("" : "+v"(ev[3]));
    if constexpr (part == 7) asm volatile("" : "+v"(ls[r]), "+v"(pq[kq][k][0]));
    if constexpr (part == 8) asm volatile("" : "+v"(ls[r]), "+v"(pq[kq][k][1]));
  };
  auto pput = [&](auto KQ, auto K, unsigned pw) {
    constexpr int kq = KQ, k = K;
    if constexpr (GQA) *(LAS u32x2*)(lds + pw + (kq & 1) * 40960 + (k / 5) * 20480 + (k % 5) * 1024) = pq[kq][k];
    else if constexpr (k & 1) *(LAS u32x4*)(lds + pw + (kq & 1) * 40960 + (k / 2) * 1024) = u32x4{pq[kq][k - 1][0], pq[kq][k - 1][1], pq[kq][k][0], pq[kq][k][1]};
  };
  bool grown;
  unsigned pw;
  {
    const int l = lane_id(), n = l & 15, g = l >> 4;
    const unsigned ke = voff_k(l);
    unsigned vv = 0;
    const unsigned k0 = LDS_RING + w * 8192 + n * 128 + 16 * ((g >> 1) ^ ((n >> 1) & 7)) + 8 * (g & 1);
    const unsigned q0 = (GQA ? 0 : hh * 40960) + l * 16;
    unsigned q1 = q0 + 40960;
    asm volatile("" : "+v"(q1));
    constexpr int NSUB = 2 * MT;
    auto rdk = [&](auto G, auto I) {
      lds_t a = lds + (I & 1) * 2048 + (G & 1) * 4096;
      const s16x4 lo = *(LAS const s16x4*)(a + (k0 ^ ((I >> 1) * 64))), hi = *(LAS const s16x4*)(a + (k0 ^ ((I >> 1) * 64 + 32)));
      fa[G & 1][I] = __builtin_shufflevector(lo, hi, 0, 1, 2, 3, 4, 5, 6, 7);
    };
    auto rdq = [&](auto Z, auto T) {
      constexpr int z = Z, gi = z / NSUB, e = (z % NSUB) / MT, h = z % MT;
      fb[T] = ld128(lds + (h ? q1 : q0) + ((2 * (gi & 3) + e) * 5 + T) * 1024);
    };
    auto piece = [&](auto G, auto I) {
      if constexpr (G < NG) dmap<(GQA ? NTKA : NTKB), I>(rK, ke ^ ((I & 1) * 64), k_src(G, I));
      else if constexpr (!GQA) dmap<(GQA ? NTVA : NTVB), I>(rV, vv, ((G - NG) * 32 + I * 8) * 512);
      else dmap<(GQA ? NTVA : NTVB), I>(rV, vv, ((2 * (G - NG) + (I >> 1)) * 32 + (I & 1) * 8) * 512);
    };
    auto qmax = [&](auto KQ, auto K) {
      constexpr int kq = KQ, k = K, r = GQA ? k : (k >> 1);
      if constexpr ((kq & 1) == 0 && (GQA || (k & 1) == 0)) mx[r] = max3(max3(sc[kq][k][0], sc[kq][k][1], sc[kq][k][2]), sc[kq][k][3], sc[kq][k][3]);
      else mx[r] = max3(max3(mx[r], sc[kq][k][0], sc[kq][k][1]), sc[kq][k][2], sc[kq][k][3]);
      asm volatile("" : "+v"(mx[r]));
    };
    auto xmax = [&](auto HALF, auto C) {
      const int l2 = lane_id(), g2 = l2 >> 4;
      LAS float* em = (LAS float*)(lds + LDS_EXM + HALF * 1536 + ((GQA ? 0 : hh) * 16 + (l2 & 15)) * 48);
      if constexpr (C == 0) __builtin_amdgcn_ds_fmaxf(em + g2, red4<OpMax>(mx[0], mx[1], mx[2], mx[3]), 0, 0, false);
      if constexpr (C == 1 && GQA) __builtin_amdgcn_ds_fmaxf(em + (g2 & 1) * 192 + 4, red2<OpMax>(mx[4], mx[5 * MT - 1]), 0, 0, false);
      if constexpr (C == 1 && !GQA) __builtin_amdgcn_ds_fmaxf(em + 4, red2<OpMax>(mx[4], mx[4]), 0, 0, false);
      if constexpr (C == 2 && GQA) __builtin_amdgcn_ds_fmaxf(em + 192 + g2, red4<OpMax>(mx[5], mx[6], mx[7], mx[8]), 0, 0, false);
    };
    auto rmax = [&](auto HALF) {
      LAS float* em = (LAS float*)(lds + LDS_EXM + HALF * 1536 + ((GQA ? 0 : hh) * 16 + (lane_id() & 15)) * 48);
      sfor<0, MT>([&](auto H) {
        const f32x4 v = *(LAS const f32x4*)(em + H * 192);
        sfor<0, 4>([&](auto T) { mx[H * 5 + T] = v[T + 0]; });
        mx[H * 5 + 4] = em[H * 192 + 4];
      });
    };
    wait_vm<4>();
    sfor<0, 4>([&](auto I) { rdk(IC<0>{}, I); });
    sfor<0, 5>([&](auto T) { rdq(IC<0>{}, T); });
    set_m0(ring);
    need(fa[0]);
    sfor<0, 4>([&](auto I) { piece(IC<2>{}, I); });
    sfor<0, 32>([&](auto Z) {
      constexpr int z = Z, gi = z / NSUB, sub = z % NSUB, e = sub / MT, h = sub % MT;
      SBAR();
      STAMP2(2, z);
      if constexpr (z == 0 || z == 20) prio_reset();
      if constexpr (z == 13 || z == 28) prio_yield(hh);
      if constexpr (z == 19) { bar(); rmax(IC<0>{}); }
      if constexpr (z == (NG - 3) * NSUB) vv = voff_spl(lane_id(), 512);
      if constexpr (sub == 0 && gi + 1 < NG) wait_vm<4>();
      sfor<0, 10>([&](auto I) {
        constexpr int i = I, t = i >> 1, u = i & 1;
        constexpr int kq = GQA ? 2 * (gi >> 2) + u : (gi >> 2), k = GQA ? 5 * h + t : i;
        sc[kq][k] = MFMA(fa[gi & 1][2 * e + u], fb[t], ((gi & 3) == 0 && e == 0) ? ZERO4 : sc[kq][k]);
        if constexpr (u == 1 && z + 1 < 32) rdq(IC<z + 1>{}, IC<t>{});
        if constexpr (gi + 1 < NG) {
          if constexpr (sub == 0 && i < 4) rdk(IC<gi + 1>{}, IC<i & 3>{});
          if constexpr (sub == 0 && i == 5) set_m0(ring + ((gi + 1) & 1) * 4096);
          if constexpr (sub == 0 && i == 6) need(fa[(gi + 1) & 1]);
          if constexpr (sub == 0 && i >= 6) piece(IC<gi + 3>{}, IC<(i - 6) & 3>{});
        }
        if constexpr (z == 16 || z == 17) qmax(IC<z - 16>{}, I);
        if constexpr (z == 18 && (i == 1 || i == 4 || i == 7)) xmax(IC<0>{}, IC<i / 3>{});
        if constexpr (z == 19 && i >= 5) sfor<0, MT>([&](auto H) { nm[H * 5 + i - 5] = mul1(mx[H * 5 + i - 5], -SCL); });
        if constexpr (z >= 20) { constexpr int x = (z - 20) * 10 + i; sfor<(3 * x + 1) / 2, (3 * x + 4) / 2>(mop); }
        if constexpr (!GQA && z == 26) qmax(IC<2>{}, I);
        SBAR();
      });
    });
    STAMP2(2, 32);
    STAMP(ST + 3);
    sfor<4 - MT, 4>([&](auto KQ) { sfor<0, 10>([&](auto K) { qmax(KQ, K); }); });
    sfor<0, 3>([&](auto C) { xmax(IC<1>{}, C); });
    pw = (GQA ? (w >> 1) * 5120 + (w & 1) * 8 : hh * 20480 + cq * 5120) + lane_id() * 16;
    bar();
    rmax(IC<1>{});
    sfor<0, 10>([&](auto K) { pput(IC<0>{}, K, pw); });
    sfor<0, GQA ? 6 : 0>([&](auto K) { pput(IC<1>{}, K, pw); });
    bool any = false;
    sfor<0, 5 * MT>([&](auto R) { any |= __builtin_fmaf(mx[R], SCL, nm[R]) > GROW; });
    grown = __builtin_amdgcn_ballot_w64(any) != 0;
    if (__builtin_expect(grown, 0)) {
      sfor<0, 5 * MT>([&](auto R) {
        const float d = __builtin_fmaf(mx[R], SCL, nm[R]);
        if (d > GROW) { ls[R] *= __builtin_amdgcn_exp2f(-d); nm[R] = -SCL * mx[R]; }
      });
    }
  }
  STAMP(ST + 4);

  const unsigned wwin = hh ? 512 + (cq * 96 & ~63) : w * 128;
  const rsrc_t rO = mk_rsrc(p.wo[P], 512 * j * 896 + wwin, W_BYTES - (512 * j * 896 + wwin));
  {
    const int l = lane_id(), g = l >> 4, q = (l >> 2) & 3;
    const unsigned vv = voff_spl(l, 512);
    const unsigned a0 = LDS_RING + (GQA ? cq : w) * 8192 + (4 * g + q) * 128 + (((q >> 1) | ((g & 1) << 1)) * 32) + 8 * (l & 3);
    const unsigned p0 = hh * 20480 + l * 16;
    auto rda = [&](auto S, auto U) {
      lds_t a = lds + (a0 ^ (U * 32)) + (GQA ? (S & 3) * 2048 : (S & 1) * 4096);
      fa[S & 1][U] = ldtr(a, a + (GQA ? 32768 : 2048));
    };
    auto rdb = [&](auto S, auto T) { fb[T] = ld128(lds + p0 + ((S >> 2) & 1) * 40960 + ((S & 3) * 5 + T) * 1024); };
    unsigned ov = 0, ow = 0;
    auto piece = [&](auto G, auto I) {
      if constexpr (G < 16) dmap<(GQA ? NTVA : NTVB), I>(rV, vv, (G * 32 + I * 8) * 512);
      else dmap<0, I>(rO, ov, ((G - 16) * (GQA ? 16 : 32) + I * 8) * 896);
    };
    constexpr auto TW = [](int s) -> int { return s == 12; };
    unsigned seen = 0;
    float rl[5], rs[2] = {0.f, 0.f};
    f32x4 lv[2];
    unsigned el = 0, eg = 0;
    auto oput = [&](auto T, auto H) {
      const f32x4 u = acc[T][2 * H], v = acc[T][2 * H + 1];
      const float r = rl[T];
      *(LAS u32x4*)(lds + ow + H * 5120 + T * 1024) = u32x4{pk2(mul1(u[0], r), mul1(u[1], r)), pk2(mul1(u[2], r), mul1(u[3], r)),
                                                            pk2(mul1(v[0], r), mul1(v[1], r)), pk2(mul1(v[2], r), mul1(v[3], r))};
    };
    if constexpr (GQA) {
      wait_vm_bar<6>();
      sfor<0, 4>([&](auto U) { rda(IC<0>{}, U); });
    } else {
      wait_vm<4>();
      sfor<0, 4>([&](auto U) { rda(IC<0>{}, U); });
      set_m0(ring);
      bar();
      sfor<0, 4>([&](auto I) { piece(IC<2>{}, I); });
    }
    STAMP(ST + 5);
    sfor<0, 5>([&](auto T) { rdb(IC<0>{}, T); });
    sfor<0, 16>([&](auto S) {
      constexpr int s = S;
      SBAR();
      STAMP2(3, s);
      if constexpr (s == 0) prio_reset();
      if constexpr (GQA) {
        constexpr int nv = 4 + TW(s - 1) + TW(s - 2) + TW(s - 3);
        if constexpr ((s & 3) == 3) wait_vm_bar<nv>();
        else { need(fa[s & 1]); wait_vm_bar_raw<nv>(); }
      } else {
        if constexpr (s == 15) bar();
        else { if constexpr ((s & 3) == 3) bar(); wait_vm<4 + TW(s - 1) + TW(s - 2)>(); }
      }
      if constexpr (s == 8) {
        if (__builtin_expect(grown, 0)) {
          LAS float* em = (LAS float*)(lds + LDS_EXM + (hh * 16 + (lane_id() & 15)) * 48);
          sfor<0, 5>([&](auto T) {
            const float d = __builtin_fmaf(em[384 + T], SCL, mul1(em[T + 0], -SCL));
            const float a = d > GROW ? __builtin_amdgcn_exp2f(-d) : 1.f;
            sfor<0, 4>([&](auto U) { acc[T][U] *= a; });
          });
        }
      }
      if constexpr (s == 11) ov = voff_spl(lane_id(), 896);
      if constexpr (s == 15) ow = hh * 40960 + cq * 10240 + lane_id() * 16;
      sfor<0, 20>([&](auto I) {
        constexpr int i = I, t = i >> 2, u = i & 3;
        acc[t][u] = MFMA(fa[s & 1][u], fb[t], s == 0 ? ZERO4 : acc[t][u]);
        if constexpr (GQA) {
          if constexpr (i == 0) set_m0(ring + (s & 3) * 2048);
          if constexpr (i == 1 || i == 2) piece(IC<s + 4>{}, IC<i - 1>{});
          if constexpr (s < 15 && i >= 4 && i < 7) rda(IC<s + 1>{}, IC<i - 4>{});
          if constexpr (s < 15 && i == 8) rda(IC<s + 1>{}, IC<3>{});
        } else if constexpr (s < 15) {
          if constexpr (i < 4) rda(IC<s + 1>{}, IC<i & 3>{});
          if constexpr (i == 5) set_m0(ring + ((s + 1) & 1) * 4096);
          if constexpr (i == DM0) need(fa[(s + 1) & 1]);
          if constexpr (i >= DM0 && i < DM0 + 4) piece(IC<s + 3>{}, IC<(i - DM0) & 3>{});
        }
        if constexpr (s < 15 && u == 3) rdb(IC<s + 1>{}, IC<t>{});
        if constexpr (s < 10 && i >= 1 && i < 19) mop(IC<(180 + 18 * s + i - 1) % 360>{});
        if constexpr (s < 3 && (i == 5 || i == 10 || i == 14 || i == 18)) {
          constexpr int k = (GQA ? 6 : 0) + 4 * s + (i == 5 ? 0 : i == 10 ? 1 : i == 14 ? 2 : 3);
          if constexpr (k < 10) pput(IC<1>{}, IC<k % 10>{}, pw);
        }
        if constexpr (s >= 3 && s <= 10 && (i == 5 || i == 10 || i == 14)) {
          constexpr int q4 = (s - 3) & 3, k = q4 * 3 - (q4 > 2) + (i == 5 ? 0 : i == 10 ? 1 : 2);
          if constexpr (k < 10 && !(q4 == 2 && i == 14)) pput(IC<s < 7 ? 2 : 3>{}, IC<k % 10>{}, pw);
        }
        if constexpr (s == 10) {
          if constexpr (i == 0) { const int l2 = lane_id(); el = LDS_EXL + ((GQA ? 0 : hh) * 80 + (l2 & 15)) * 48 + (GQA ? w : cq) * 4; eg = l2 >> 4; }
          if constexpr (i == 2) rs[0] = sw16<OpAdd>(ls[0], ls[1]);
          if constexpr (i == 3) rs[1] = sw16<OpAdd>(ls[2], ls[3]);
          if constexpr (i == 4) rs[0] = sw32<OpAdd>(rs[0], rs[1]);
          if constexpr (i == 6) *(LAS float*)(lds + el + eg * 768) = rs[0];
          if constexpr (i == 7) rs[1] = sw16<OpAdd>(ls[4], ls[5 * MT - 1]);
          if constexpr (i == 8) rs[1] = sw32<OpAdd>(rs[1], rs[1]);
          if constexpr (i == 9) *(LAS float*)(lds + el + 3072 + (GQA ? (eg & 1) * 3840 : 0)) = rs[1];
          if constexpr (GQA) {
            if constexpr (i == 11) rs[0] = sw16<OpAdd>(ls[5], ls[6]);
            if constexpr (i == 12) rs[1] = sw16<OpAdd>(ls[7], ls[8]);
            if constexpr (i == 13) rs[0] = sw32<OpAdd>(rs[0], rs[1]);
            if constexpr (i == 16) *(LAS float*)(lds + el + 3840 + eg * 768) = rs[0];
          }
          if constexpr (i >= 2 && i < 14) asm volatile("" : "+v"(rs[0]), "+v"(rs[1]));
        }
        if constexpr (TW(s) && i == 14) { seen = __hip_atomic_load(sync_word<LAST ? 1 : 0>(p, b, j), RLX); asm volatile("" ::: "memory"); }
        if constexpr (s >= 11 && s < 14) {
          constexpr int x = (s - 11) * 20 + i - 2, T = x / 10, c = x % 10;
          if constexpr (x == -2) el = LDS_EXL + (hh * 80 + (lane_id() & 15)) * 48;
          if constexpr (x >= 0 && x < 50) {
            if constexpr (c == 0) { lv[0] = *(LAS const f32x4*)(lds + el + T * 768); if constexpr (GQA) lv[1] = *(LAS const f32x4*)(lds + el + T * 768 + 16); }
            if constexpr (c == 4) rs[0] = (lv[0][0] + lv[0][1]) + lv[0][2];
            if constexpr (c == 5) rs[0] = GQA ? (rs[0] + lv[0][3]) + lv[1][0] : rs[0] + lv[0][3];
            if constexpr (c == 6 && GQA) rs[0] = (rs[0] + lv[1][1]) + lv[1][2];
            if constexpr (c == 7 && GQA) rs[0] += lv[1][3];
            if constexpr (c == 8) rl[T % 5] = __builtin_amdgcn_rcpf(rs[0]);
            if constexpr (c >= 4 && c < 8) asm volatile("" : "+v"(rs[0]));
            if constexpr (c == 8) asm volatile("" : "+v"(rl[T % 5]));
          }
        }
        if constexpr (s == 15 && i >= 6 && (i & 1) == 0) oput(IC<(i - 6) / 4>{}, IC<((i - 6) / 2) % 2>{});
        SBAR();
      });
    });
    STAMP2(3, 16);
    STAMP(ST + 6);
    sfor<7, 10>([&](auto I) { oput(IC<I / 2>{}, IC<I % 2>{}); });
    settle<LAST ? 1 : 0>(p, lds, b, j, seen);
  }

  RedRows red;
  auto oproj = [&](auto NTILE) {
    constexpr int NT = NTILE, NS = 5 * NT, D0 = NT == 4 ? DM0 : 5;
    const rsrc_t rN = mk_rsrc(p.wq[1 - P], (512 * j + 64 * w) * 2, W_BYTES - (512 * j + 64 * w) * 2);
    const int l = lane_id(), g = l >> 4, q = (l >> 2) & 3;
    const unsigned ov = voff_spl(l, 896), we = voff_nat(l, 8192);
    auto piece = [&](auto G, auto I) {
      if constexpr (G < 16) dmap<0, I>(rO, ov, (G * 32 + I * 8) * 896);
      else dmap<0, I>(rN, we ^ ((I & 1) * 64), ((G - 16) * 32 + I * 8) * 8192);
    };
    unsigned a0[NT], o1 = l * 16 + 40960;
    sfor<0, NT>([&](auto U) {
      a0[U] = LDS_RING + w * 8192 + (4 * g + q) * 128 + ((((q >> 1) | ((g & 1) << 1)) ^ (U + (NT == 3 ? cq & 1 : 0))) * 32) + 8 * (l & 3);
    });
    asm volatile("" : "+v"(o1));
    auto rda = [&](auto S, auto U) { lds_t a = lds + a0[U] + (S & 1) * 4096; fa[S & 1][U] = ldtr(a, a + 2048); };
    auto rdb = [&](auto S, auto T) { fb[T] = ld128(lds + (S < 8 ? l * 16 : o1) + ((S & 7) * 5 + T) * 1024); };
    auto needa = [&](auto X) {
      if constexpr (NT == 4) need(fa[X]);
      else need3(fa[X]);
    };
    const rsrc_t rS = mk_rsrc(p.slab, ((P * 32 + b) * 8 + j) * SLAB + (NT == 4 ? w * 10240 : 40960 + cq * 8192), 10240);
    auto sput = [&](auto R) {
      constexpr int r = R;
      constexpr int tu = NT == 4 ? r / 2 : r < 5 ? r : 2 * (r - 5), cu = NT == 4 ? 2 * (r & 1) : r < 5 ? 0 : 2;
      constexpr int tv = NT == 4 ? r / 2 : r < 5 ? r : r == 7 ? 4 : 2 * (r - 5) + 1, cv = NT == 4 ? 2 * (r & 1) + 1 : r < 5 ? 1 : 2;
      const f32x4 u = acc[tu][cu], v = acc[tv][cv];
      auto r0 = __builtin_amdgcn_permlane16_swap(pk2(u[0], u[1]), pk2(v[0], v[1]), false, false);
      auto r1 = __builtin_amdgcn_permlane16_swap(pk2(u[2], u[3]), pk2(v[2], v[3]), false, false);
      __builtin_amdgcn_raw_buffer_store_b128(u32x4{r0[0], r1[0], r0[1], r1[1]}, rS, l * 16 + r * 1024, 0, 16  );
    };
    wait_vm<4>();
    sfor<0, NT>([&](auto U) { rda(IC<0>{}, U); });
    set_m0(ring);
    bar();
    STAMP(ST + 7);
    sfor<0, 5>([&](auto T) { rdb(IC<0>{}, T); });
    sfor<0, 4>([&](auto I) { piece(IC<2>{}, I); });
    sfor<0, 16>([&](auto S) {
      constexpr int s = S;
      SBAR();
      STAMP2(5, s);
      if constexpr (s == 0) prio_reset();
      if constexpr (s == PO) prio_yield(hh);
      if constexpr (s < 15) wait_vm<4>();
      else if constexpr (!LAST) { bar(); issue_x(p, 1 - P, b, w, lds0); }
      sfor<0, NS>([&](auto I) {
        constexpr int i = I, t = i / NT, u = i % NT;
        acc[t][u] = MFMA(fa[s & 1][u], fb[t], s == 0 ? ZERO4 : acc[t][u]);
        if constexpr (s < 15) {
          if constexpr (i < NT) rda(IC<s + 1>{}, IC<i % NT>{});
          if constexpr (u == NT - 1) rdb(IC<s + 1>{}, IC<t>{});
          if constexpr (!(LAST && s >= 13)) {
            if constexpr (i == D0 - 2) set_m0(ring + ((s + 1) & 1) * 4096);
            if constexpr (i == D0) needa(IC<(s + 1) & 1>{});
            if constexpr (i >= D0 && i < D0 + 4) piece(IC<s + 3>{}, IC<(i - D0) & 3>{});
          }
          if constexpr (LAST && s >= 13 && i >= 3 && i < 15 && i % 3 == 0) red_load<0, (4 * (s - 13) + i / 3 - 1) & 7>(red, p, 1 - P, b, j, w);
        } else if constexpr (NT == 4) {
          if constexpr (i >= 6 && (i & 1) == 0) sput(IC<(i - 6) / 2>{});
        } else {
          if constexpr (i == 5 || i == 8 || i == 11 || i == 14) sput(IC<(i - 5) / 3>{});
          if constexpr (i == 10) sput(IC<5>{});
        }
        SBAR();
      });
    });
    STAMP2(5, 16);
    STAMP(ST + 8);
    if constexpr (NT == 4) sfor<7, 10>(sput);
    else { sput(IC<6>{}); sput(IC<4>{}); sput(IC<7>{}); }
    if constexpr (!LAST) wait_vm_bar<NT == 4 ? 10 : 8>();
  };
  if (hh == 0) oproj(IC<4>{});
  else oproj(IC<3>{});
  STAMP(ST + 9);
  if constexpr (LAST) {
    wait_vm<0>();
    STAMP(21);
    bar();
    STAMP(22);
    if (w == 0) arrive<2>(p, lds, b, j);
    if (w == 7) sfor<0, 8>([&](auto J) { red_load<1, J>(red, p, 1 - P, b, j, w); });
    red_store(red, p, 1 - P, b, j, w);
    if (w == 0) settle<2>(p, lds, b, j, __hip_atomic_load(sync_word<2>(p, b, j), RLX));
    bar();
    STAMP(23);
    sfor<0, 8>([&](auto J) { red_load<0, J>(red, p, P, b, j, w); });
    if (w == 7) sfor<0, 8>([&](auto J) { red_load<1, J>(red, p, P, b, j, w); });
    red_store(red, p, P, b, j, w);
  }
}

__global__ __launch_bounds__(512, 2) void fused_attn_kernel(Params p) {
  extern __shared__ __attribute__((aligned(16))) char smem[];
  lds_t lds = (lds_t)smem;
  const unsigned lds0 = (unsigned)(uintptr_t)lds;
  const int w = __builtin_amdgcn_readfirstlane(threadIdx.x >> 6);
  const int j = blockIdx.x & 7, b = blockIdx.x >> 3;
  const int first = b & 1;
  STAMP(0);
  {
    {
      unsigned long long pc;
      asm volatile("s_getpc_b64 %0" : "=s"(pc));
      warm(lds0, mk_rsrc((const void*)(pc & ~127ull), (b * 8 + w) * CODE_LINES * 128, (CODE_LINES - 1) * 128 + 4), lane_id() * 128);
    }
    unsigned found = 0;
    if (w == 7) found = __hip_atomic_load(flag_word(p, b, lane_id() & 7), RLX);
    asm volatile("" ::: "memory");
    issue_x(p, first, b, w, lds0);
    asm volatile("s_barrier" ::: "memory");
    const int l = lane_id();
    const rsrc_t rW = mk_rsrc(p.wq[first], (512 * j + 64 * w) * 2, W_BYTES - (512 * j + 64 * w) * 2);
    const unsigned we = voff_nat(l, 8192), ring = lds0 + LDS_RING + w * 8192;
    sfor<0, 2>([&](auto G) {
      set_m0(ring + G * 4096);
      sfor<0, 4>([&](auto I) { dmap<0, I>(rW, we ^ ((I & 1) * 64), (G * 32 + I * 8) * 8192); });
    });
    const unsigned id = (w & 3) * 64 + l;
    if (w < 4) warm(lds0, mk_rsrc(p.wq[first], 0, W_BYTES), ((b >> 1) * 28 + (id >> 3)) * 8192 + 1024 * j + (id & 7) * 128);
    else warm(lds0, mk_rsrc(p.wo[first], 0, W_BYTES), 512 * j * 896 + (b >> 1) * 28672 + id * 128);
    wait_vm_bar<9>();
    if (w == 7) {
      *(LAS unsigned*)(lds + LDS_OLD + (l & 7) * 4) = found;
      if (l < 8) __hip_atomic_store(ack_word(p, b, l, j), found + 1u, RLX);
    }
  }
  STAMP(10);
  if (first == 0) {
    run_problem<0, false, 0>(p, lds, lds0, fresh(b), fresh(j), fresh(w));
    run_problem<1, true, 10>(p, lds, lds0, fresh(b), fresh(j), fresh(w));
  } else {
    run_problem<1, false, 0>(p, lds, lds0, fresh(b), fresh(j), fresh(w));
    run_problem<0, true, 10>(p, lds, lds0, fresh(b), fresh(j), fresh(w));
  }
  STAMP(20);
}

extern "C" void kernel_launch(void* const* d_in, const int* in_sizes, int n_in, void* d_out, int out_size, void* d_ws,
                              size_t ws_size, hipStream_t stream) {
  Params p;
  for (int i = 0; i < 2; ++i) {
    p.x[i] = d_in[5 * i + 0]; p.k[i] = d_in[5 * i + 1]; p.v[i] = d_in[5 * i + 2]; p.wq[i] = d_in[5 * i + 3]; p.wo[i] = d_in[5 * i + 4];
  }
  char* top = (char*)d_ws + ((ws_size - 512 * (size_t)SLAB - 262144) & ~(size_t)4095);
  p.sync = (unsigned*)top;
  p.slab = top + 262144;
  p.out = d_out;
  p.diag = (unsigned long long*)((char*)d_ws + (64u << 20));
  (void)hipFuncSetAttribute((const void*)fused_attn_kernel, hipFuncAttributeMaxDynamicSharedMemorySize, LDS_TOTAL);
  fused_attn_kernel<<<256, 512, LDS_TOTAL, stream>>>(p);
}
```

```cpp
#include <hip/hip_runtime.h>
#include <hip/hip_bf16.h>
#include <stdint.h>
#include <type_traits>

typedef short bf16x8 __attribute__((ext_vector_type(8)));
typedef short s16x4 __attribute__((ext_vector_type(4)));
typedef float f32x4 __attribute__((ext_vector_type(4)));
typedef float f32x2 __attribute__((ext_vector_type(2)));
typedef __bf16 bf16x2 __attribute__((ext_vector_type(2)));
typedef unsigned u32x4 __attribute__((ext_vector_type(4)));
typedef unsigned u32x2 __attribute__((ext_vector_type(2)));
#define LAS __attribute__((address_space(3)))
typedef __amdgpu_buffer_rsrc_t rsrc_t;
typedef LAS char* lds_t;

constexpr int LDS_RING = 81920;
constexpr int LDS_EXM = 147456;
constexpr int LDS_EXL = 151552;
constexpr int LDS_DUMMY = 159232;
constexpr int LDS_OLD = 159488;
constexpr int LDS_TOTAL = 159552;
constexpr unsigned X_BYTES = 80 * 448 * 2;
constexpr unsigned W_BYTES = 448 * 4096 * 2;
constexpr unsigned KV_HEAD = 512 * 256 * 2;
constexpr unsigned SLAB = 72 * 1024;
constexpr int CODE_LINES = 5;
constexpr float SCL = 0.0625f * 1.4426950408889634f;
constexpr float GROW = 20.f;

template <int I, int N, class F>
__device__ __forceinline__ void sfor(F&& f) {
  if constexpr (I < N) { f(std::integral_constant<int, I>{}); sfor<I + 1, N>(f); }
}
#define SBAR() __builtin_amdgcn_sched_barrier(0)
#define MFMA(a, b, c) __builtin_amdgcn_mfma_f32_16x16x32_bf16(a, b, c, 0, 0, 0)
template <int N> __device__ __forceinline__ void wait_vm() { asm volatile("s_waitcnt vmcnt(%0)" ::"i"(N) : "memory"); }
__device__ __forceinline__ void bar() { asm volatile("s_waitcnt lgkmcnt(0)\n\ts_barrier" ::: "memory"); }
template <int N> __device__ __forceinline__ void wait_vm_bar_raw() { asm volatile("s_waitcnt vmcnt(%0)\n\ts_barrier" ::"i"(N) : "memory"); }
template <int N> __device__ __forceinline__ void wait_vm_bar() { asm volatile("s_waitcnt vmcnt(%0) lgkmcnt(0)\n\ts_barrier" ::"i"(N) : "memory"); }
__device__ __forceinline__ void prio_reset() { asm volatile("s_setprio 1"); }
__device__ __forceinline__ void prio_yield(int hh) { asm volatile("s_cmp_lg_u32 %0, 0\n\ts_cbranch_scc1 1f\n\ts_setprio 0\n1:" ::"s"(hh) : "scc"); }
__device__ __forceinline__ int lane_id() {
  int l; asm volatile("v_mbcnt_lo_u32_b32 %0, -1, 0\n\tv_mbcnt_hi_u32_b32 %0, -1, %0" : "=v"(l)); return l;
}
__device__ __forceinline__ int fresh(int x) { asm volatile("" : "+s"(x)); return x; }
__device__ __forceinline__ unsigned pk2(float a, float b) {
  return __builtin_bit_cast(unsigned, __builtin_convertvector(f32x2{a, b}, bf16x2));
}
__device__ __forceinline__ rsrc_t mk_rsrc(const void* p, unsigned off, unsigned bytes) {
  return __builtin_amdgcn_make_buffer_rsrc((void*)((const char*)p + off), (short)0, (int)bytes, 0x00020000);
}
__device__ __forceinline__ void need(const bf16x8 (&f)[4]) { asm volatile("" ::"v"(f[0]), "v"(f[1]), "v"(f[2]), "v"(f[3]) : "memory"); }
__device__ __forceinline__ void need3(const bf16x8 (&f)[4]) { asm volatile("" ::"v"(f[0]), "v"(f[1]), "v"(f[2]) : "memory"); }
__device__ __forceinline__ void set_m0(unsigned m0v) { asm volatile("s_mov_b32 m0, %0\n\ts_nop 0" ::"s"(m0v) : "memory"); }
template <int POL, int I>
__device__ __forceinline__ void dmap(rsrc_t r, unsigned v, unsigned src) {
  if constexpr (POL == 1) asm volatile("buffer_load_dwordx4 %0, %1, %2 offen offset:%3 nt lds" ::"v"(v), "s"(r), "s"(src - 1024u * I), "i"(1024 * I) : "memory");
  else if constexpr (POL == 2) asm volatile("buffer_load_dwordx4 %0, %1, %2 offen offset:%3 sc1 lds" ::"v"(v), "s"(r), "s"(src - 1024u * I), "i"(1024 * I) : "memory");
  else asm volatile("buffer_load_dwordx4 %0, %1, %2 offen offset:%3 lds" ::"v"(v), "s"(r), "s"(src - 1024u * I), "i"(1024 * I) : "memory");
}
__device__ __forceinline__ void warm(unsigned lds0, rsrc_t r, unsigned v) {
  asm volatile("s_mov_b32 m0, %0\n\ts_nop 0\n\tbuffer_load_dword %1, %2, 0 offen lds" ::"s"(lds0 + LDS_DUMMY), "v"(v), "s"(r) : "memory");
}
__device__ __forceinline__ unsigned touch(const void* a, unsigned off) {
  unsigned d; const char* q = (const char*)a + off;
  asm volatile("s_load_dword %0, %1, 0x0" : "=s"(d) : "s"(q)); return d;
}
extern "C" __device__ float llvm_fmul_legacy(float, float) __asm("llvm.amdgcn.fmul.legacy");
__device__ __forceinline__ float mul1(float a, float b) { return llvm_fmul_legacy(a, b); }
__device__ __forceinline__ bf16x8 ld128(lds_t p) { return *(LAS const bf16x8*)p; }
__device__ __forceinline__ bf16x8 ldtr(lds_t p0, lds_t p1) {
  s16x4 lo = __builtin_amdgcn_ds_read_tr16_b64_v4i16((LAS s16x4*)p0);
  s16x4 hi = __builtin_amdgcn_ds_read_tr16_b64_v4i16((LAS s16x4*)p1);
  return __builtin_shufflevector(lo, hi, 0, 1, 2, 3, 4, 5, 6, 7);
}
__device__ __forceinline__ float max3(float a, float b, float c) { return __builtin_fmaxf(__builtin_fmaxf(a, b), c); }
struct OpMax { static __device__ __forceinline__ float f(float a, float b) { return max3(a, b, b); } };
struct OpAdd { static __device__ __forceinline__ float f(float a, float b) { return a + b; } };
template <class Op> __device__ __forceinline__ float sw16(float a, float b) {
  auto r = __builtin_amdgcn_permlane16_swap(__float_as_uint(a), __float_as_uint(b), false, false);
  return Op::f(__uint_as_float(r[0]), __uint_as_float(r[1]));
}
template <class Op> __device__ __forceinline__ float sw32(float a, float b) {
  auto r = __builtin_amdgcn_permlane32_swap(__float_as_uint(a), __float_as_uint(b), false, false);
  return Op::f(__uint_as_float(r[0]), __uint_as_float(r[1]));
}
template <class Op> __device__ __forceinline__ float red4(float a, float b, float c, float d) { return sw32<Op>(sw16<Op>(a, b), sw16<Op>(c, d)); }
template <class Op> __device__ __forceinline__ float red2(float a, float b) { const float x = sw16<Op>(a, b); return sw32<Op>(x, x); }

__device__ __forceinline__ unsigned voff_nat(int l, unsigned pitch) { return (l >> 3) * pitch + 16 * ((l & 7) ^ (((l >> 4) & 1) << 1)); }
__device__ __forceinline__ unsigned voff_spl(int l, unsigned pitch) { return (l >> 3) * pitch + 16 * ((l & 7) ^ (((l >> 4) & 3) << 1)); }
__device__ __forceinline__ unsigned voff_k(int l) { return (l >> 3) * 512 + 16 * ((l & 7) ^ ((l >> 4) & 3)); }

struct Params {
  const void *x[2], *k[2], *v[2], *wq[2], *wo[2];
  void* slab;
  unsigned* sync;
  void* out;
};


constexpr int DM0 = 8;
template <int V> using IC = std::integral_constant<int, V>;
constexpr f32x4 ZERO4 = {0.f, 0.f, 0.f, 0.f};

__device__ __forceinline__ void issue_x(const Params& p, int P, int b, int w, unsigned lds0) {
  w = fresh(w);
  const int l = lane_id();
  const unsigned v = (l >> 3) * 896 + 16 * ((l & 7) ^ ((l >> 4) & 3));
  const rsrc_t rx = mk_rsrc(p.x[P], b * X_BYTES, X_BYTES);
  sfor<0, 9>([&](auto I) {
    const unsigned pc = w * 9 + I, blk = (pc * 205) >> 11, rg = pc - blk * 10;
    set_m0(lds0 + pc * 1024);
    dmap<0, 0>(rx, v ^ ((rg & 1) * 64), rg * 7168 + blk * 128);
  });
}

typedef __attribute__((address_space(1))) unsigned gu32;
#define RLX __ATOMIC_RELAXED, __HIP_MEMORY_SCOPE_AGENT
__device__ __forceinline__ gu32* flag_word(const Params& p, int b, int j) { return (gu32*)p.sync + (b * 8 + j) * 32; }
__device__ __forceinline__ gu32* ack_word(const Params& p, int b, int prod, int cons) { return (gu32*)p.sync + 8192 + ((b * 8 + prod) * 8 + cons) * 16; }
template <int STAGE>
__device__ __forceinline__ void arrive(const Params& p, lds_t lds, int b, int j) {
  const unsigned old = *(LAS const unsigned*)(lds + LDS_OLD + j * 4);
  if (lane_id() == 0) __hip_atomic_store(flag_word(p, b, j), old + STAGE, RLX);
}
template <int STAGE> __device__ __forceinline__ gu32* sync_word(const Params& p, int b, int j) {
  const int c = lane_id() & 7;
  return STAGE ? flag_word(p, b, c) : ack_word(p, b, j, c);
}
template <int STAGE>
__device__ __forceinline__ void settle(const Params& p, lds_t lds, int b, int j, unsigned seen) {
  const unsigned old = *(LAS const unsigned*)(lds + LDS_OLD + (STAGE ? lane_id() & 7 : j) * 4);
  for (unsigned spins = 0; spins < (1u << 22); ++spins) {
    if (__builtin_amdgcn_ballot_w64(STAGE ? seen - old - STAGE > 2u - STAGE : seen != old + 1u) == 0) break;
    __builtin_amdgcn_s_sleep(2);
    seen = __hip_atomic_load(sync_word<STAGE>(p, b, j), RLX);
  }
  asm volatile("" ::: "memory");
}
struct RedRows { u32x4 v[2][8]; };
template <int K, int J>
__device__ __forceinline__ void red_load(RedRows& r, const Params& p, int P, int b, int j, int w) {
  const rsrc_t rS = mk_rsrc(p.slab, (P * 32 + b) * 8 * SLAB, 8 * SLAB);
  r.v[K][J] = __builtin_amdgcn_raw_buffer_load_b128(rS, lane_id() * 16, J * SLAB + (9 * j + w + K) * 1024, 16  );
}
__device__ __forceinline__ void red_store(const RedRows& r, const Params& p, int P, int b, int j, int w) {
  const int l = lane_id(), n = l & 15, g = l >> 4;
  sfor<0, 2>([&](auto K) {
    if (K == 0 || w == 7) {
      float sm[8];
      sfor<0, 4>([&](auto I) { sm[2 * I] = __uint_as_float(r.v[K][0][I + 0] << 16); sm[2 * I + 1] = __uint_as_float(r.v[K][0][I + 0] & 0xffff0000u); });
      sfor<1, 8>([&](auto J) {
        sfor<0, 4>([&](auto I) { sm[2 * I] += __uint_as_float(r.v[K][J][I + 0] << 16); sm[2 * I + 1] += __uint_as_float(r.v[K][J][I + 0] & 0xffff0000u); });
      });
      const int f = 9 * j + w + K;
      int t, col;
      if (f < 40) { const int ww = f / 10; t = (f - ww * 10) >> 1; col = 64 * ww + 32 * (f & 1) + 16 * (g & 1); }
      else { const int rr = f & 7, c0 = 256 + 48 * ((f - 40) >> 3); t = rr < 5 ? rr : 2 * (rr - 5) + (g & 1); col = c0 + (rr < 5 ? 16 * (g & 1) : 32); }
      if (t < 5)
        *(u32x4*)((char*)p.out + ((size_t)(b * 80 + t * 16 + n) * 896 + P * 448 + col + 8 * (g >> 1)) * 2) =
            u32x4{pk2(sm[0], sm[1]), pk2(sm[2], sm[3]), pk2(sm[4], sm[5]), pk2(sm[6], sm[7])};
    }
  });
}

template <int P  , bool LAST>
__device__ __forceinline__ void run_problem(const Params& p, lds_t lds, unsigned lds0, int b, int j, int w) {
  constexpr bool GQA = (P == 0);
  const int hh = w >> 2, cq = w & 3;
  const unsigned ring = lds0 + LDS_RING + w * 8192;
  f32x4 acc[5][4];
  bf16x8 fa[2][4], fb[5];

  const unsigned koff = GQA ? (b * 8 + j) * KV_HEAD + w * 16 * 512 : (b * 16 + 2 * j + hh) * KV_HEAD + cq * 32 * 512;
  const rsrc_t rK = mk_rsrc(p.k[P], koff, KV_HEAD - (GQA ? w * 16 * 512 : cq * 32 * 512));
  auto k_src = [](int g, int i) -> unsigned {
    return GQA ? ((g >> 2) * 256 + (i >> 1) * 128 + (i & 1) * 8) * 512 + (g & 3) * 128 : ((g >> 2) * 128 + i * 8) * 512 + (g & 3) * 128;
  };

  {
    const rsrc_t rW = mk_rsrc(p.wq[P], (512 * j + 64 * w) * 2, W_BYTES - (512 * j + 64 * w) * 2);
    const int l = lane_id(), n = l & 15, g = l >> 4, q = (l >> 2) & 3;
    const unsigned we = voff_nat(l, 8192), ke = voff_k(l);
    const unsigned a0 = LDS_RING + w * 8192 + (8 * g + q) * 128 + (((q >> 1) | ((g & 1) << 1)) * 32) + 8 * (l & 3);
    unsigned x0[2][2];
    x0[0][0] = n * 128 + 16 * (g ^ ((n >> 1) & 7)); x0[1][0] = x0[0][0] ^ 64; x0[0][1] = x0[0][0] + 40960; x0[1][1] = x0[1][0] + 40960;
    asm volatile("" : "+v"(x0[0][1]), "+v"(x0[1][1]));
    const unsigned qw = hh * 40960 + cq * 10240 + l * 16;
    auto rda = [&](auto S, auto U) { lds_t a = lds + (a0 ^ (U * 32)) + (S & 1) * 4096; fa[S & 1][U] = ldtr(a, a + 512); };
    auto rdb = [&](auto S, auto T) { fb[T] = ld128(lds + x0[S & 1][S >= 8] + ((S >> 1) & 3) * 10240 + T * 2048); };
    auto piece = [&](auto G, auto I) {
      if constexpr (G < 14) dmap<0, I>(rW, we ^ ((I & 1) * 64), (G * 32 + I * 8) * 8192);
      else dmap<1, I>(rK, ke ^ ((I & 1) * 64), k_src(G - 14, I));
    };
    auto qput = [&](auto T, auto H) {
      const f32x4 u = acc[T][2 * H], v = acc[T][2 * H + 1];
      *(LAS u32x4*)(lds + qw + H * 5120 + T * 1024) = u32x4{pk2(u[0], u[1]), pk2(u[2], u[3]), pk2(v[0], v[1]), pk2(v[2], v[3])};
    };
    if constexpr (!LAST) wait_vm<5>();
    sfor<0, 4>([&](auto U) { rda(IC<0>{}, U); });
    sfor<0, 5>([&](auto T) { rdb(IC<0>{}, T); });
    set_m0(ring);
    need(fa[0]);
    sfor<0, 4>([&](auto I) { piece(IC<2>{}, I); });
    sfor<0, 14>([&](auto S) {
      constexpr int s = S;
      SBAR();
      if constexpr (s == 0) prio_reset();
      if constexpr (s == 9) prio_yield(hh);
      if constexpr (s == 0) { if constexpr (!LAST) wait_vm<5>(); }
      else if constexpr (s < 13) wait_vm<(LAST && s == 6) ? 0 : 4>();
      else {
        bar();
        *(LAS f32x2*)(lds + LDS_EXM + w * 512 + l * 8) = f32x2{-INFINITY, -INFINITY};
        if constexpr (LAST) { if (w == 0) arrive<1>(p, lds, b, j); }
      }
      sfor<0, 20>([&](auto I) {
        constexpr int i = I, t = i >> 2, u = i & 3;
        acc[t][u] = MFMA(fa[s & 1][u], fb[t], s == 0 ? ZERO4 : acc[t][u]);
        if constexpr (s < 13) {
          if constexpr (i < 4) rda(IC<s + 1>{}, IC<i & 3>{});
          if constexpr (u == 3) rdb(IC<s + 1>{}, IC<t>{});
          if constexpr (i == 5) set_m0(ring + ((s + 1) & 1) * 4096);
          if constexpr (i == DM0) need(fa[(s + 1) & 1]);
          if constexpr (i >= DM0 && i < DM0 + 4) piece(IC<s + 3>{}, IC<(i - DM0) & 3>{});
        } else if constexpr (i >= 6 && (i & 1) == 0) qput(IC<(i - 6) / 4>{}, IC<((i - 6) / 2) % 2>{});
        SBAR();
      });
    });
    sfor<7, 10>([&](auto I) { qput(IC<I / 2>{}, IC<I % 2>{}); });
    bar();
  }

  const rsrc_t rV = GQA ? mk_rsrc(p.v[P], (b * 8 + j) * KV_HEAD + hh * 16 * 512 + cq * 128, KV_HEAD - (hh * 16 * 512 + cq * 128))
                        : mk_rsrc(p.v[P], (b * 16 + 2 * j + hh) * KV_HEAD + cq * 128, KV_HEAD - cq * 128);
  constexpr int MT = GQA ? 2 : 1;
  constexpr int NG = GQA ? 8 : 16;
  f32x4 sc[4][10];
  float mx[5 * MT], nm[5 * MT], ls[5 * MT], ev[4];
  u32x2 pq[4][10];
  auto mop = [&](auto ID) {
    constexpr int id = ID, kq = id / 90, k = (id / 9) % 10, part = id % 9, r = GQA ? k : (k >> 1);
    if constexpr (part == 0) { ev[0] = __builtin_fmaf(sc[kq][k][0], SCL, nm[r]); ev[1] = __builtin_fmaf(sc[kq][k][1], SCL, nm[r]); }
    if constexpr (part == 1) ev[0] = __builtin_amdgcn_exp2f(ev[0]);
    if constexpr (part == 2) { ev[2] = __builtin_fmaf(sc[kq][k][2], SCL, nm[r]); ev[3] = __builtin_fmaf(sc[kq][k][3], SCL, nm[r]); }
    if constexpr (part == 3) ev[1] = __builtin_amdgcn_exp2f(ev[1]);
    if constexpr (part == 4) ev[2] = __builtin_amdgcn_exp2f(ev[2]);
    if constexpr (part == 5) { if constexpr (kq == 0 && (GQA || (k & 1) == 0)) ls[r] = ev[0] + ev[1]; else ls[r] = (ls[r] + ev[0]) + ev[1]; }
    if constexpr (part == 6) ev[3] = __builtin_amdgcn_exp2f(ev[3]);
    if constexpr (part == 7) { pq[kq][k][0] = pk2(ev[0], ev[1]); ls[r] += ev[2]; }
    if constexpr (part == 8) { pq[kq][k][1] = pk2(ev[2], ev[3]); ls[r] += ev[3]; }
    if constexpr (part == 0) asm volatile("" : "+v"(ev[0]), "+v"(ev[1]));
    if constexpr (part == 1) asm volatile("" : "+v"(ev[0]));
    if constexpr (part == 2) asm volatile("" : "+v"(ev[2]), "+v"(ev[3]));
    if constexpr (part == 3) asm volatile("" : "+v"(ev[1]));
    if constexpr (part == 4) asm volatile("" : "+v"(ev[2]));
    if constexpr (part == 5) asm volatile("" : "+v"(ls[r]));
    if constexpr (part == 6) asm volatile("" : "+v"(ev[3]));
    if constexpr (part == 7) asm volatile("" : "+v"(ls[r]), "+v"(pq[kq][k][0]));
    if constexpr (part == 8) asm volatile("" : "+v"(ls[r]), "+v"(pq[kq][k][1]));
  };
  auto pput = [&](auto KQ, auto K, unsigned pw) {
    constexpr int kq = KQ, k = K;
    if constexpr (GQA) *(LAS u32x2*)(lds + pw + (kq & 1) * 40960 + (k / 5) * 20480 + (k % 5) * 1024) = pq[kq][k];
    else if constexpr (k & 1) *(LAS u32x4*)(lds + pw + (kq & 1) * 40960 + (k / 2) * 1024) = u32x4{pq[kq][k - 1][0], pq[kq][k - 1][1], pq[kq][k][0], pq[kq][k][1]};
  };
  bool grown;
  unsigned pw;
  {
    const int l = lane_id(), n = l & 15, g = l >> 4;
    const unsigned ke = voff_k(l);
    unsigned vv = 0;
    const unsigned k0 = LDS_RING + w * 8192 + n * 128 + 16 * ((g >> 1) ^ ((n >> 1) & 7)) + 8 * (g & 1);
    const unsigned q0 = (GQA ? 0 : hh * 40960) + l * 16;
    unsigned q1 = q0 + 40960;
    asm volatile("" : "+v"(q1));
    constexpr int NSUB = 2 * MT;
    auto rdk = [&](auto G, auto I) {
      lds_t a = lds + (I & 1) * 2048 + (G & 1) * 4096;
      const s16x4 lo = *(LAS const s16x4*)(a + (k0 ^ ((I >> 1) * 64))), hi = *(LAS const s16x4*)(a + (k0 ^ ((I >> 1) * 64 + 32)));
      fa[G & 1][I] = __builtin_shufflevector(lo, hi, 0, 1, 2, 3, 4, 5, 6, 7);
    };
    auto rdq = [&](auto Z, auto T) {
      constexpr int z = Z, gi = z / NSUB, e = (z % NSUB) / MT, h = z % MT;
      fb[T] = ld128(lds + (h ? q1 : q0) + ((2 * (gi & 3) + e) * 5 + T) * 1024);
    };
    auto piece = [&](auto G, auto I) {
      if constexpr (G < NG) dmap<1, I>(rK, ke ^ ((I & 1) * 64), k_src(G, I));
      else if constexpr (!GQA) dmap<(GQA ? 1 : 2), I>(rV, vv, ((G - NG) * 32 + I * 8) * 512);
      else dmap<(GQA ? 1 : 2), I>(rV, vv, ((2 * (G - NG) + (I >> 1)) * 32 + (I & 1) * 8) * 512);
    };
    auto qmax = [&](auto KQ, auto K) {
      constexpr int kq = KQ, k = K, r = GQA ? k : (k >> 1);
      if constexpr ((kq & 1) == 0 && (GQA || (k & 1) == 0)) mx[r] = max3(max3(sc[kq][k][0], sc[kq][k][1], sc[kq][k][2]), sc[kq][k][3], sc[kq][k][3]);
      else mx[r] = max3(max3(mx[r], sc[kq][k][0], sc[kq][k][1]), sc[kq][k][2], sc[kq][k][3]);
      asm volatile("" : "+v"(mx[r]));
    };
    auto xmax = [&](auto HALF, auto C) {
      const int l2 = lane_id(), g2 = l2 >> 4;
      LAS float* em = (LAS float*)(lds + LDS_EXM + HALF * 1536 + ((GQA ? 0 : hh) * 16 + (l2 & 15)) * 48);
      if constexpr (C == 0) __builtin_amdgcn_ds_fmaxf(em + g2, red4<OpMax>(mx[0], mx[1], mx[2], mx[3]), 0, 0, false);
      if constexpr (C == 1 && GQA) __builtin_amdgcn_ds_fmaxf(em + (g2 & 1) * 192 + 4, red2<OpMax>(mx[4], mx[5 * MT - 1]), 0, 0, false);
      if constexpr (C == 1 && !GQA) __builtin_amdgcn_ds_fmaxf(em + 4, red2<OpMax>(mx[4], mx[4]), 0, 0, false);
      if constexpr (C == 2 && GQA) __builtin_amdgcn_ds_fmaxf(em + 192 + g2, red4<OpMax>(mx[5], mx[6], mx[7], mx[8]), 0, 0, false);
    };
    auto rmax = [&](auto HALF) {
      LAS float* em = (LAS float*)(lds + LDS_EXM + HALF * 1536 + ((GQA ? 0 : hh) * 16 + (lane_id() & 15)) * 48);
      sfor<0, MT>([&](auto H) {
        const f32x4 v = *(LAS const f32x4*)(em + H * 192);
        sfor<0, 4>([&](auto T) { mx[H * 5 + T] = v[T + 0]; });
        mx[H * 5 + 4] = em[H * 192 + 4];
      });
    };
    wait_vm<4>();
    sfor<0, 4>([&](auto I) { rdk(IC<0>{}, I); });
    sfor<0, 5>([&](auto T) { rdq(IC<0>{}, T); });
    set_m0(ring);
    need(fa[0]);
    sfor<0, 4>([&](auto I) { piece(IC<2>{}, I); });
    sfor<0, 32>([&](auto Z) {
      constexpr int z = Z, gi = z / NSUB, sub = z % NSUB, e = sub / MT, h = sub % MT;
      SBAR();
      if constexpr (z == 0 || z == 20) prio_reset();
      if constexpr (z == 13 || z == 28) prio_yield(hh);
      if constexpr (z == 19) { bar(); rmax(IC<0>{}); }
      if constexpr (z == (NG - 3) * NSUB) vv = voff_spl(lane_id(), 512);
      if constexpr (sub == 0 && gi + 1 < NG) wait_vm<4>();
      sfor<0, 10>([&](auto I) {
        constexpr int i = I, t = i >> 1, u = i & 1;
        constexpr int kq = GQA ? 2 * (gi >> 2) + u : (gi >> 2), k = GQA ? 5 * h + t : i;
        sc[kq][k] = MFMA(fa[gi & 1][2 * e + u], fb[t], ((gi & 3) == 0 && e == 0) ? ZERO4 : sc[kq][k]);
        if constexpr (u == 1 && z + 1 < 32) rdq(IC<z + 1>{}, IC<t>{});
        if constexpr (gi + 1 < NG) {
          if constexpr (sub == 0 && i < 4) rdk(IC<gi + 1>{}, IC<i & 3>{});
          if constexpr (sub == 0 && i == 5) set_m0(ring + ((gi + 1) & 1) * 4096);
          if constexpr (sub == 0 && i == 6) need(fa[(gi + 1) & 1]);
          if constexpr (sub == 0 && i >= 6) piece(IC<gi + 3>{}, IC<(i - 6) & 3>{});
        }
        if constexpr (z == 16 || z == 17) qmax(IC<z - 16>{}, I);
        if constexpr (z == 18 && (i == 1 || i == 4 || i == 7)) xmax(IC<0>{}, IC<i / 3>{});
        if constexpr (z == 19 && i >= 5) sfor<0, MT>([&](auto H) { nm[H * 5 + i - 5] = mul1(mx[H * 5 + i - 5], -SCL); });
        if constexpr (z >= 20) { constexpr int x = (z - 20) * 10 + i; sfor<(3 * x + 1) / 2, (3 * x + 4) / 2>(mop); }
        if constexpr (!GQA && z == 26) qmax(IC<2>{}, I);
        SBAR();
      });
    });
    sfor<4 - MT, 4>([&](auto KQ) { sfor<0, 10>([&](auto K) { qmax(KQ, K); }); });
    sfor<0, 3>([&](auto C) { xmax(IC<1>{}, C); });
    pw = (GQA ? (w >> 1) * 5120 + (w & 1) * 8 : hh * 20480 + cq * 5120) + lane_id() * 16;
    bar();
    rmax(IC<1>{});
    sfor<0, 10>([&](auto K) { pput(IC<0>{}, K, pw); });
    sfor<0, GQA ? 6 : 0>([&](auto K) { pput(IC<1>{}, K, pw); });
    bool any = false;
    sfor<0, 5 * MT>([&](auto R) { any |= __builtin_fmaf(mx[R], SCL, nm[R]) > GROW; });
    grown = __builtin_amdgcn_ballot_w64(any) != 0;
    if (__builtin_expect(grown, 0)) {
      sfor<0, 5 * MT>([&](auto R) {
        const float d = __builtin_fmaf(mx[R], SCL, nm[R]);
        if (d > GROW) { ls[R] *= __builtin_amdgcn_exp2f(-d); nm[R] = -SCL * mx[R]; }
      });
    }
  }

  const unsigned wwin = hh ? 512 + (cq * 96 & ~63) : w * 128;
  const rsrc_t rO = mk_rsrc(p.wo[P], 512 * j * 896 + wwin, W_BYTES - (512 * j * 896 + wwin));
  {
    const int l = lane_id(), g = l >> 4, q = (l >> 2) & 3;
    const unsigned vv = voff_spl(l, 512);
    const unsigned a0 = LDS_RING + (GQA ? cq : w) * 8192 + (4 * g + q) * 128 + (((q >> 1) | ((g & 1) << 1)) * 32) + 8 * (l & 3);
    const unsigned p0 = hh * 20480 + l * 16;
    auto rda = [&](auto S, auto U) {
      lds_t a = lds + (a0 ^ (U * 32)) + (GQA ? (S & 3) * 2048 : (S & 1) * 4096);
      fa[S & 1][U] = ldtr(a, a + (GQA ? 32768 : 2048));
    };
    auto rdb = [&](auto S, auto T) { fb[T] = ld128(lds + p0 + ((S >> 2) & 1) * 40960 + ((S & 3) * 5 + T) * 1024); };
    unsigned ov = 0, ow = 0;
    auto piece = [&](auto G, auto I) {
      if constexpr (G < 16) dmap<(GQA ? 1 : 2), I>(rV, vv, (G * 32 + I * 8) * 512);
      else dmap<0, I>(rO, ov, ((G - 16) * (GQA ? 16 : 32) + I * 8) * 896);
    };
    constexpr auto TW = [](int s) -> int { return s == 12; };
    unsigned seen = 0;
    float rl[5], rs[2] = {0.f, 0.f};
    f32x4 lv[2];
    unsigned el = 0, eg = 0;
    auto oput = [&](auto T, auto H) {
      const f32x4 u = acc[T][2 * H], v = acc[T][2 * H + 1];
      const float r = rl[T];
      *(LAS u32x4*)(lds + ow + H * 5120 + T * 1024) = u32x4{pk2(mul1(u[0], r), mul1(u[1], r)), pk2(mul1(u[2], r), mul1(u[3], r)),
                                                            pk2(mul1(v[0], r), mul1(v[1], r)), pk2(mul1(v[2], r), mul1(v[3], r))};
    };
    if constexpr (GQA) {
      wait_vm_bar<6>();
      sfor<0, 4>([&](auto U) { rda(IC<0>{}, U); });
    } else {
      wait_vm<4>();
      sfor<0, 4>([&](auto U) { rda(IC<0>{}, U); });
      set_m0(ring);
      bar();
      sfor<0, 4>([&](auto I) { piece(IC<2>{}, I); });
    }
    sfor<0, 5>([&](auto T) { rdb(IC<0>{}, T); });
    sfor<0, 16>([&](auto S) {
      constexpr int s = S;
      SBAR();
      if constexpr (s == 0) prio_reset();
      if constexpr (GQA) {
        constexpr int nv = 4 + TW(s - 1) + TW(s - 2) + TW(s - 3);
        if constexpr ((s & 3) == 3) wait_vm_bar<nv>();
        else { need(fa[s & 1]); wait_vm_bar_raw<nv>(); }
      } else {
        if constexpr (s == 15) bar();
        else { if constexpr ((s & 3) == 3) bar(); wait_vm<4 + TW(s - 1) + TW(s - 2)>(); }
      }
      if constexpr (s == 8) {
        if (__builtin_expect(grown, 0)) {
          LAS float* em = (LAS float*)(lds + LDS_EXM + (hh * 16 + (lane_id() & 15)) * 48);
          sfor<0, 5>([&](auto T) {
            const float d = __builtin_fmaf(em[384 + T], SCL, mul1(em[T + 0], -SCL));
            const float a = d > GROW ? __builtin_amdgcn_exp2f(-d) : 1.f;
            sfor<0, 4>([&](auto U) { acc[T][U] *= a; });
          });
        }
      }
      if constexpr (s == 11) ov = voff_spl(lane_id(), 896);
      if constexpr (s == 15) ow = hh * 40960 + cq * 10240 + lane_id() * 16;
      sfor<0, 20>([&](auto I) {
        constexpr int i = I, t = i >> 2, u = i & 3;
        acc[t][u] = MFMA(fa[s & 1][u], fb[t], s == 0 ? ZERO4 : acc[t][u]);
        if constexpr (GQA) {
          if constexpr (i == 0) set_m0(ring + (s & 3) * 2048);
          if constexpr (i == 1 || i == 2) piece(IC<s + 4>{}, IC<i - 1>{});
          if constexpr (s < 15 && i >= 4 && i < 7) rda(IC<s + 1>{}, IC<i - 4>{});
          if constexpr (s < 15 && i == 8) rda(IC<s + 1>{}, IC<3>{});
        } else if constexpr (s < 15) {
          if constexpr (i < 4) rda(IC<s + 1>{}, IC<i & 3>{});
          if constexpr (i == 5) set_m0(ring + ((s + 1) & 1) * 4096);
          if constexpr (i == DM0) need(fa[(s + 1) & 1]);
          if constexpr (i >= DM0 && i < DM0 + 4) piece(IC<s + 3>{}, IC<(i - DM0) & 3>{});
        }
        if constexpr (s < 15 && u == 3) rdb(IC<s + 1>{}, IC<t>{});
        if constexpr (s < 10 && i >= 1 && i < 19) mop(IC<(180 + 18 * s + i - 1) % 360>{});
        if constexpr (s < 3 && (i == 5 || i == 10 || i == 14 || i == 18)) {
          constexpr int k = (GQA ? 6 : 0) + 4 * s + (i == 5 ? 0 : i == 10 ? 1 : i == 14 ? 2 : 3);
          if constexpr (k < 10) pput(IC<1>{}, IC<k % 10>{}, pw);
        }
        if constexpr (s >= 3 && s <= 10 && (i == 5 || i == 10 || i == 14)) {
          constexpr int q4 = (s - 3) & 3, k = q4 * 3 - (q4 > 2) + (i == 5 ? 0 : i == 10 ? 1 : 2);
          if constexpr (k < 10 && !(q4 == 2 && i == 14)) pput(IC<s < 7 ? 2 : 3>{}, IC<k % 10>{}, pw);
        }
        if constexpr (s == 10) {
          if constexpr (i == 0) { const int l2 = lane_id(); el = LDS_EXL + ((GQA ? 0 : hh) * 80 + (l2 & 15)) * 48 + (GQA ? w : cq) * 4; eg = l2 >> 4; }
          if constexpr (i == 2) rs[0] = sw16<OpAdd>(ls[0], ls[1]);
          if constexpr (i == 3) rs[1] = sw16<OpAdd>(ls[2], ls[3]);
          if constexpr (i == 4) rs[0] = sw32<OpAdd>(rs[0], rs[1]);
          if constexpr (i == 6) *(LAS float*)(lds + el + eg * 768) = rs[0];
          if constexpr (i == 7) rs[1] = sw16<OpAdd>(ls[4], ls[5 * MT - 1]);
          if constexpr (i == 8) rs[1] = sw32<OpAdd>(rs[1], rs[1]);
          if constexpr (i == 9) *(LAS float*)(lds + el + 3072 + (GQA ? (eg & 1) * 3840 : 0)) = rs[1];
          if constexpr (GQA) {
            if constexpr (i == 11) rs[0] = sw16<OpAdd>(ls[5], ls[6]);
            if constexpr (i == 12) rs[1] = sw16<OpAdd>(ls[7], ls[8]);
            if constexpr (i == 13) rs[0] = sw32<OpAdd>(rs[0], rs[1]);
            if constexpr (i == 16) *(LAS float*)(lds + el + 3840 + eg * 768) = rs[0];
          }
          if constexpr (i >= 2 && i < 14) asm volatile("" : "+v"(rs[0]), "+v"(rs[1]));
        }
        if constexpr (TW(s) && i == 14) { seen = __hip_atomic_load(sync_word<LAST ? 1 : 0>(p, b, j), RLX); asm volatile("" ::: "memory"); }
        if constexpr (s >= 11 && s < 14) {
          constexpr int x = (s - 11) * 20 + i - 2, T = x / 10, c = x % 10;
          if constexpr (x == -2) el = LDS_EXL + (hh * 80 + (lane_id() & 15)) * 48;
          if constexpr (x >= 0 && x < 50) {
            if constexpr (c == 0) { lv[0] = *(LAS const f32x4*)(lds + el + T * 768); if constexpr (GQA) lv[1] = *(LAS const f32x4*)(lds + el + T * 768 + 16); }
            if constexpr (c == 4) rs[0] = (lv[0][0] + lv[0][1]) + lv[0][2];
            if constexpr (c == 5) rs[0] = GQA ? (rs[0] + lv[0][3]) + lv[1][0] : rs[0] + lv[0][3];
            if constexpr (c == 6 && GQA) rs[0] = (rs[0] + lv[1][1]) + lv[1][2];
            if constexpr (c == 7 && GQA) rs[0] += lv[1][3];
            if constexpr (c == 8) rl[T % 5] = __builtin_amdgcn_rcpf(rs[0]);
            if constexpr (c >= 4 && c < 8) asm volatile("" : "+v"(rs[0]));
            if constexpr (c == 8) asm volatile("" : "+v"(rl[T % 5]));
          }
        }
        if constexpr (s == 15 && i >= 6 && (i & 1) == 0) oput(IC<(i - 6) / 4>{}, IC<((i - 6) / 2) % 2>{});
        SBAR();
      });
    });
    sfor<7, 10>([&](auto I) { oput(IC<I / 2>{}, IC<I % 2>{}); });
    settle<LAST ? 1 : 0>(p, lds, b, j, seen);
  }

  RedRows red;
  auto oproj = [&](auto NTILE) {
    constexpr int NT = NTILE, NS = 5 * NT, D0 = NT == 4 ? DM0 : 5;
    const rsrc_t rN = mk_rsrc(p.wq[1 - P], (512 * j + 64 * w) * 2, W_BYTES - (512 * j + 64 * w) * 2);
    const int l = lane_id(), g = l >> 4, q = (l >> 2) & 3;
    const unsigned ov = voff_spl(l, 896), we = voff_nat(l, 8192);
    auto piece = [&](auto G, auto I) {
      if constexpr (G < 16) dmap<0, I>(rO, ov, (G * 32 + I * 8) * 896);
      else dmap<0, I>(rN, we ^ ((I & 1) * 64), ((G - 16) * 32 + I * 8) * 8192);
    };
    unsigned a0[NT], o1 = l * 16 + 40960;
    sfor<0, NT>([&](auto U) {
      a0[U] = LDS_RING + w * 8192 + (4 * g + q) * 128 + ((((q >> 1) | ((g & 1) << 1)) ^ (U + (NT == 3 ? cq & 1 : 0))) * 32) + 8 * (l & 3);
    });
    asm volatile("" : "+v"(o1));
    auto rda = [&](auto S, auto U) { lds_t a = lds + a0[U] + (S & 1) * 4096; fa[S & 1][U] = ldtr(a, a + 2048); };
    auto rdb = [&](auto S, auto T) { fb[T] = ld128(lds + (S < 8 ? l * 16 : o1) + ((S & 7) * 5 + T) * 1024); };
    auto needa = [&](auto X) {
      if constexpr (NT == 4) need(fa[X]);
      else need3(fa[X]);
    };
    const rsrc_t rS = mk_rsrc(p.slab, ((P * 32 + b) * 8 + j) * SLAB + (NT == 4 ? w * 10240 : 40960 + cq * 8192), 10240);
    auto sput = [&](auto R) {
      constexpr int r = R;
      constexpr int tu = NT == 4 ? r / 2 : r < 5 ? r : 2 * (r - 5), cu = NT == 4 ? 2 * (r & 1) : r < 5 ? 0 : 2;
      constexpr int tv = NT == 4 ? r / 2 : r < 5 ? r : r == 7 ? 4 : 2 * (r - 5) + 1, cv = NT == 4 ? 2 * (r & 1) + 1 : r < 5 ? 1 : 2;
      const f32x4 u = acc[tu][cu], v = acc[tv][cv];
      auto r0 = __builtin_amdgcn_permlane16_swap(pk2(u[0], u[1]), pk2(v[0], v[1]), false, false);
      auto r1 = __builtin_amdgcn_permlane16_swap(pk2(u[2], u[3]), pk2(v[2], v[3]), false, false);
      __builtin_amdgcn_raw_buffer_store_b128(u32x4{r0[0], r1[0], r0[1], r1[1]}, rS, l * 16 + r * 1024, 0, 16  );
    };
    wait_vm<4>();
    sfor<0, NT>([&](auto U) { rda(IC<0>{}, U); });
    set_m0(ring);
    bar();
    sfor<0, 5>([&](auto T) { rdb(IC<0>{}, T); });
    sfor<0, 4>([&](auto I) { piece(IC<2>{}, I); });
    sfor<0, 16>([&](auto S) {
      constexpr int s = S;
      SBAR();
      if constexpr (s == 0) prio_reset();
      if constexpr (s == 12) prio_yield(hh);
      if constexpr (s < 15) wait_vm<4>();
      else if constexpr (!LAST) { bar(); issue_x(p, 1 - P, b, w, lds0); }
      sfor<0, NS>([&](auto I) {
        constexpr int i = I, t = i / NT, u = i % NT;
        acc[t][u] = MFMA(fa[s & 1][u], fb[t], s == 0 ? ZERO4 : acc[t][u]);
        if constexpr (s < 15) {
          if constexpr (i < NT) rda(IC<s + 1>{}, IC<i % NT>{});
          if constexpr (u == NT - 1) rdb(IC<s + 1>{}, IC<t>{});
          if constexpr (!(LAST && s >= 13)) {
            if constexpr (i == D0 - 2) set_m0(ring + ((s + 1) & 1) * 4096);
            if constexpr (i == D0) needa(IC<(s + 1) & 1>{});
            if constexpr (i >= D0 && i < D0 + 4) piece(IC<s + 3>{}, IC<(i - D0) & 3>{});
          }
          if constexpr (LAST && s >= 13 && i >= 3 && i < 15 && i % 3 == 0) red_load<0, (4 * (s - 13) + i / 3 - 1) & 7>(red, p, 1 - P, b, j, w);
        } else if constexpr (NT == 4) {
          if constexpr (i >= 6 && (i & 1) == 0) sput(IC<(i - 6) / 2>{});
        } else {
          if constexpr (i == 5 || i == 8 || i == 11 || i == 14) sput(IC<(i - 5) / 3>{});
          if constexpr (i == 10) sput(IC<5>{});
        }
        SBAR();
      });
    });
    if constexpr (NT == 4) sfor<7, 10>(sput);
    else { sput(IC<6>{}); sput(IC<4>{}); sput(IC<7>{}); }
    if constexpr (!LAST) wait_vm_bar<NT == 4 ? 10 : 8>();
  };
  if (hh == 0) oproj(IC<4>{});
  else oproj(IC<3>{});
  if constexpr (LAST) {
    wait_vm<0>();
    bar();
    if (w == 0) arrive<2>(p, lds, b, j);
    if (w == 7) sfor<0, 8>([&](auto J) { red_load<1, J>(red, p, 1 - P, b, j, w); });
    red_store(red, p, 1 - P, b, j, w);
    settle<2>(p, lds, b, j, __hip_atomic_load(sync_word<2>(p, b, j), RLX));
    sfor<0, 8>([&](auto J) { red_load<0, J>(red, p, P, b, j, w); });
    if (w == 7) sfor<0, 8>([&](auto J) { red_load<1, J>(red, p, P, b, j, w); });
    red_store(red, p, P, b, j, w);
  }
}

__global__ __launch_bounds__(512, 2) void fused_attn_kernel(Params p) {
  extern __shared__ __attribute__((aligned(16))) char smem[];
  lds_t lds = (lds_t)smem;
  const unsigned lds0 = (unsigned)(uintptr_t)lds;
  const int w = __builtin_amdgcn_readfirstlane(threadIdx.x >> 6);
  const int j = blockIdx.x & 7, b = blockIdx.x >> 3;
  const int first = b & 1;
  {
    {
      unsigned long long pc;
      asm volatile("s_getpc_b64 %0" : "=s"(pc));
      warm(lds0, mk_rsrc((const void*)(pc & ~127ull), (b * 8 + w) * CODE_LINES * 128, (CODE_LINES - 1) * 128 + 4), lane_id() * 128);
    }
    unsigned found = 0;
    if (w == 7) found = __hip_atomic_load(flag_word(p, b, lane_id() & 7), RLX);
    asm volatile("" ::: "memory");
    issue_x(p, first, b, w, lds0);
    asm volatile("s_barrier" ::: "memory");
    const int l = lane_id();
    const rsrc_t rW = mk_rsrc(p.wq[first], (512 * j + 64 * w) * 2, W_BYTES - (512 * j + 64 * w) * 2);
    const unsigned we = voff_nat(l, 8192), ring = lds0 + LDS_RING + w * 8192;
    sfor<0, 2>([&](auto G) {
      set_m0(ring + G * 4096);
      sfor<0, 4>([&](auto I) { dmap<0, I>(rW, we ^ ((I & 1) * 64), (G * 32 + I * 8) * 8192); });
    });
    const unsigned id = (w & 3) * 64 + l;
    if (w < 4) warm(lds0, mk_rsrc(p.wq[first], 0, W_BYTES), ((b >> 1) * 28 + (id >> 3)) * 8192 + 1024 * j + (id & 7) * 128);
    else warm(lds0, mk_rsrc(p.wo[first], 0, W_BYTES), 512 * j * 896 + (b >> 1) * 28672 + id * 128);
    unsigned t0, t1, t2;
    {
      const int P = w & 1;
      const unsigned kv = P ? (b * 16 + 2 * j) * KV_HEAD : (b * 8 + j) * KV_HEAD, kvn = (P ? 2 * KV_HEAD : KV_HEAD) - 64;
      const void *a0, *a1, *a2; unsigned o0, o1, o2;
      if ((w >> 1) == 0) { a0 = p.k[P]; o0 = kv; a1 = p.k[P]; o1 = kv + kvn; a2 = p.x[P]; o2 = b * X_BYTES; }
      else if ((w >> 1) == 1) { a0 = p.v[P]; o0 = kv; a1 = p.v[P]; o1 = kv + kvn; a2 = p.out; o2 = b * 80 * 896 * 2 + P * (80 * 896 * 2 - 64); }
      else if ((w >> 1) == 2) { a0 = p.slab; o0 = (P * 32 + b) * 8 * SLAB; a1 = p.slab; o1 = (P * 32 + b + 1) * 8 * SLAB - 64; a2 = p.wo[P]; o2 = 512 * j * 896; }
      else { a0 = p.wq[P]; o0 = 0; a1 = p.wq[P]; o1 = W_BYTES / 2; a2 = p.wq[P]; o2 = W_BYTES - 64; }
      t0 = touch(a0, o0); t1 = touch(a1, o1); t2 = touch(a2, o2);
    }
    wait_vm_bar<9>();
    asm volatile("" ::"s"(t0), "s"(t1), "s"(t2));
    if (w == 7) {
      *(LAS unsigned*)(lds + LDS_OLD + (l & 7) * 4) = found;
      if (l < 8) __hip_atomic_store(ack_word(p, b, l, j), found + 1u, RLX);
    }
  }
  if (first == 0) {
    run_problem<0, false>(p, lds, lds0, fresh(b), fresh(j), fresh(w));
    run_problem<1, true>(p, lds, lds0, fresh(b), fresh(j), fresh(w));
  } else {
    run_problem<1, false>(p, lds, lds0, fresh(b), fresh(j), fresh(w));
    run_problem<0, true>(p, lds, lds0, fresh(b), fresh(j), fresh(w));
  }
}

extern "C" void kernel_launch(void* const* d_in, const int* in_sizes, int n_in, void* d_out, int out_size, void* d_ws,
                              size_t ws_size, hipStream_t stream) {
  Params p;
  for (int i = 0; i < 2; ++i) {
    p.x[i] = d_in[5 * i + 0]; p.k[i] = d_in[5 * i + 1]; p.v[i] = d_in[5 * i + 2]; p.wq[i] = d_in[5 * i + 3]; p.wo[i] = d_in[5 * i + 4];
  }
  char* top = (char*)d_ws + ((ws_size - 512 * (size_t)SLAB - 262144) & ~(size_t)4095);
  p.sync = (unsigned*)top;
  p.slab = top + 262144;
  p.out = d_out;
  (void)hipFuncSetAttribute((const void*)fused_attn_kernel, hipFuncAttributeMaxDynamicSharedMemorySize, LDS_TOTAL);
  fused_attn_kernel<<<256, 512, LDS_TOTAL, stream>>>(p);
}
```

```cpp
#include <hip/hip_runtime.h>
#include <hip/hip_bf16.h>
#include <stdint.h>
#include <type_traits>

typedef short bf16x8 __attribute__((ext_vector_type(8)));
typedef short s16x4 __attribute__((ext_vector_type(4)));
typedef float f32x4 __attribute__((ext_vector_type(4)));
typedef float f32x2 __attribute__((ext_vector_type(2)));
typedef __bf16 bf16x2 __attribute__((ext_vector_type(2)));
typedef unsigned u32x4 __attribute__((ext_vector_type(4)));
typedef unsigned u32x2 __attribute__((ext_vector_type(2)));
#define LAS __attribute__((address_space(3)))
typedef __amdgpu_buffer_rsrc_t rsrc_t;
typedef LAS char* lds_t;

#define DIAG 0
#define DLAST 1
#define PO 12
#define FORCE_SAFE 0
#define PRE1F 0
#define PSF 22
#define ABL 0
#define SAUX 16
#define LAUX 16
#define XW 0
#define ONT 0
#define KVPOL "nt"
#define WPOL ""
#define X2NT 0
#define WSTAIL 0
#define CODEW 1
#define K1E 2
#define FQ 0
#define PBB 1
#define PBO 1
#define FAKEKV 0

constexpr int LDS_RING = 81920;
constexpr int LDS_EXM = 147456;
constexpr int LDS_EXL = 151552;
constexpr int LDS_DUMMY = 159232;
constexpr int LDS_OLD = 159488;
constexpr int LDS_BAD = LDS_OLD + 32;
constexpr int LDS_TOTAL = DIAG ? 163840 : 159552;
constexpr unsigned X_BYTES = 80 * 448 * 2;
constexpr unsigned W_BYTES = 448 * 4096 * 2;
constexpr unsigned KV_HEAD = 512 * 256 * 2;
constexpr unsigned SLAB = 72 * 1024;
constexpr float SCL = 0.0625f * 1.4426950408889634f;
constexpr float SUM_LO = 0x1p-60f, SUM_HI = 0x1p60f;
constexpr float GROW = 20.f;

template <int I, int N, class F>
__device__ __forceinline__ void sfor(F&& f) {
  if constexpr (I < N) { f(std::integral_constant<int, I>{}); sfor<I + 1, N>(f); }
}
#define SBAR() __builtin_amdgcn_sched_barrier(0)
__device__ __forceinline__ f32x4 MFMA(bf16x8 a, bf16x8 b, f32x4 c) {
  f32x4 d = __builtin_amdgcn_mfma_f32_16x16x32_bf16(a, b, c, 0, 0, 0);
  asm volatile("" : "+v"(d));
  return d;
}
template <int N> __device__ __forceinline__ void wait_vm() { asm volatile("s_waitcnt vmcnt(%0)" ::"i"(N) : "memory"); }
__device__ __forceinline__ void bar() { asm volatile("s_waitcnt lgkmcnt(0)\n\ts_barrier" ::: "memory"); }
template <int N> __device__ __forceinline__ void wait_vm_bar_raw() { asm volatile("s_waitcnt vmcnt(%0)\n\ts_barrier" ::"i"(N) : "memory"); }
template <int N> __device__ __forceinline__ void wait_vm_bar() { asm volatile("s_waitcnt vmcnt(%0) lgkmcnt(0)\n\ts_barrier" ::"i"(N) : "memory"); }
__device__ __forceinline__ void prio_reset() { asm volatile("s_setprio 1"); }
__device__ __forceinline__ void prio_yield(int hh) { asm volatile("s_cmp_lg_u32 %0, 0\n\ts_cbranch_scc1 1f\n\ts_setprio 0\n1:" ::"s"(hh) : "scc"); }
__device__ __forceinline__ int lane_id() {
  int l; asm volatile("v_mbcnt_lo_u32_b32 %0, -1, 0\n\tv_mbcnt_hi_u32_b32 %0, -1, %0" : "=v"(l)); return l;
}
__device__ __forceinline__ int fresh(int x) { asm volatile("" : "+s"(x)); return x; }
__device__ __forceinline__ unsigned pk2(float a, float b) {
  return __builtin_bit_cast(unsigned, __builtin_convertvector(f32x2{a, b}, bf16x2));
}
__device__ __forceinline__ rsrc_t mk_rsrc(const void* p, unsigned off, unsigned bytes) {
  return __builtin_amdgcn_make_buffer_rsrc((void*)((const char*)p + off), (short)0, (int)bytes, 0x00020000);
}
__device__ __forceinline__ void need(const bf16x8 (&f)[4]) { asm volatile("" ::"v"(f[0]), "v"(f[1]), "v"(f[2]), "v"(f[3]) : "memory"); }
__device__ __forceinline__ void need3(const bf16x8 (&f)[4]) { asm volatile("" ::"v"(f[0]), "v"(f[1]), "v"(f[2]) : "memory"); }
__device__ __forceinline__ void set_m0(unsigned m0v) { asm volatile("s_mov_b32 m0, %0\n\ts_nop 0" ::"s"(m0v) : "memory"); }
template <int POL, int I>
__device__ __forceinline__ void dmap(rsrc_t r, unsigned v, unsigned src) {
  if constexpr (POL == 1) asm volatile("buffer_load_dwordx4 %0, %1, %2 offen offset:%3 nt lds" ::"v"(v), "s"(r), "s"(src - 1024u * I), "i"(1024 * I) : "memory");
  else if constexpr (POL == 2) asm volatile("buffer_load_dwordx4 %0, %1, %2 offen offset:%3 sc1 lds" ::"v"(v), "s"(r), "s"(src - 1024u * I), "i"(1024 * I) : "memory");
  else asm volatile("buffer_load_dwordx4 %0, %1, %2 offen offset:%3 lds" ::"v"(v), "s"(r), "s"(src - 1024u * I), "i"(1024 * I) : "memory");
}
__device__ __forceinline__ void warm(unsigned lds0, rsrc_t r, unsigned v) {
  asm volatile("s_mov_b32 m0, %0\n\ts_nop 0\n\tbuffer_load_dword %1, %2, 0 offen " WPOL " lds" ::"s"(lds0 + LDS_DUMMY), "v"(v), "s"(r) : "memory");
}
extern "C" __device__ float llvm_fmul_legacy(float, float) __asm("llvm.amdgcn.fmul.legacy");
__device__ __forceinline__ float mul1(float a, float b) { return llvm_fmul_legacy(a, b); }
__device__ __forceinline__ bf16x8 ld128(lds_t p) { return *(LAS const bf16x8*)p; }
__device__ __forceinline__ bf16x8 ldtr(lds_t p0, lds_t p1) {
  s16x4 lo = __builtin_amdgcn_ds_read_tr16_b64_v4i16((LAS s16x4*)p0);
  s16x4 hi = __builtin_amdgcn_ds_read_tr16_b64_v4i16((LAS s16x4*)p1);
  return __builtin_shufflevector(lo, hi, 0, 1, 2, 3, 4, 5, 6, 7);
}
__device__ __forceinline__ float max3(float a, float b, float c) { return __builtin_fmaxf(__builtin_fmaxf(a, b), c); }
struct OpMax { static __device__ __forceinline__ float f(float a, float b) { return max3(a, b, b); } };
struct OpAdd { static __device__ __forceinline__ float f(float a, float b) { return a + b; } };
template <class Op> __device__ __forceinline__ float sw16(float a, float b) {
  auto r = __builtin_amdgcn_permlane16_swap(__float_as_uint(a), __float_as_uint(b), false, false);
  return Op::f(__uint_as_float(r[0]), __uint_as_float(r[1]));
}
template <class Op> __device__ __forceinline__ float sw32(float a, float b) {
  auto r = __builtin_amdgcn_permlane32_swap(__float_as_uint(a), __float_as_uint(b), false, false);
  return Op::f(__uint_as_float(r[0]), __uint_as_float(r[1]));
}
template <class Op> __device__ __forceinline__ float red4(float a, float b, float c, float d) { return sw32<Op>(sw16<Op>(a, b), sw16<Op>(c, d)); }
template <class Op> __device__ __forceinline__ float red2(float a, float b) { const float x = sw16<Op>(a, b); return sw32<Op>(x, x); }

__device__ __forceinline__ unsigned voff_nat(int l, unsigned pitch) { return (l >> 3) * pitch + 16 * ((l & 7) ^ (((l >> 4) & 1) << 1)); }
__device__ __forceinline__ unsigned voff_spl(int l, unsigned pitch) { return (l >> 3) * pitch + 16 * ((l & 7) ^ (((l >> 4) & 3) << 1)); }
__device__ __forceinline__ unsigned voff_k(int l) { return (l >> 3) * 512 + 16 * ((l & 7) ^ ((l >> 4) & 3)); }

struct Params {
  const void *x[2], *k[2], *v[2], *wq[2], *wo[2];
  void* slab;
  unsigned* sync;
  void* out;
  unsigned long long* diag;
};

#define STAMP(i)
#define STAMP2(ph, i)

constexpr int DM0 = 8;
constexpr int fin_tile(int grp) { return grp == 0 ? 0 : grp == 9 ? 4 : (grp & 1) ? (grp + 1) / 2 : grp / 2 - 1; }
constexpr int fin_half(int grp) { return grp == 9 || (grp > 0 && !(grp & 1)); }
template <int V> using IC = std::integral_constant<int, V>;
constexpr f32x4 ZERO4 = {0.f, 0.f, 0.f, 0.f};

template <bool NT = false>
__device__ __forceinline__ void issue_x(const Params& p, int P, int b, int w, unsigned lds0) {
  w = fresh(w);
  const int l = lane_id();
  const unsigned v = (l >> 3) * 896 + 16 * ((l & 7) ^ ((l >> 4) & 3));
  const rsrc_t rx = mk_rsrc(p.x[P], b * X_BYTES, X_BYTES);
  sfor<0, 9>([&](auto I) {
    const unsigned pc = w * 9 + I, blk = (pc * 205) >> 11, rg = pc - blk * 10;
    set_m0(lds0 + pc * 1024);
    dmap<NT, 0>(rx, v ^ ((rg & 1) * 64), rg * 7168 + blk * 128);
  });
}

typedef __attribute__((address_space(1))) unsigned gu32;
#define RLX __ATOMIC_RELAXED, __HIP_MEMORY_SCOPE_AGENT
__device__ __forceinline__ gu32* flag_word(const Params& p, int b, int j) { return (gu32*)p.sync + (b * 8 + j) * 32; }
__device__ __forceinline__ gu32* ack_word(const Params& p, int b, int prod, int cons) { return (gu32*)p.sync + 8192 + ((b * 8 + prod) * 8 + cons) * 16; }
template <int STAGE>
__device__ __forceinline__ void arrive(const Params& p, lds_t lds, int b, int j) {
  const unsigned old = *(LAS const unsigned*)(lds + LDS_OLD + j * 4);
  if (lane_id() == 0) __hip_atomic_store(flag_word(p, b, j), old + STAGE, RLX);
}
template <int STAGE> __device__ __forceinline__ gu32* sync_word(const Params& p, int b, int j) {
  const int c = lane_id() & 7;
  return STAGE ? flag_word(p, b, c) : ack_word(p, b, j, c);
}
template <int STAGE>
__device__ __forceinline__ void settle(const Params& p, lds_t lds, int b, int j, unsigned seen) {
  const unsigned old = *(LAS const unsigned*)(lds + LDS_OLD + (STAGE ? lane_id() & 7 : j) * 4);
  for (unsigned spins = 0; spins < (1u << 22); ++spins) {
    if (__builtin_amdgcn_ballot_w64(STAGE ? seen - old - STAGE > 2u - STAGE : seen != old + 1u) == 0) break;
    __builtin_amdgcn_s_sleep(2);
    seen = __hip_atomic_load(sync_word<STAGE>(p, b, j), RLX);
  }
  asm volatile("" ::: "memory");
}
struct RedRows { u32x4 v[2][8]; };
template <int K, int J>
__device__ __forceinline__ void red_load(RedRows& r, const Params& p, int P, int b, int j, int w) {
  const rsrc_t rS = mk_rsrc(p.slab, (P * 32 + b) * 8 * SLAB, 8 * SLAB);
  r.v[K][J] = __builtin_amdgcn_raw_buffer_load_b128(rS, lane_id() * 16, J * SLAB + (9 * j + w + K) * 1024, LAUX  );
}
__device__ __forceinline__ void red_store(const RedRows& r, const Params& p, int P, int b, int j, int w) {
  const int l = lane_id(), n = l & 15, g = l >> 4;
  sfor<0, 2>([&](auto K) {
    if (K == 0 || w == 7) {
      float sm[8];
      sfor<0, 4>([&](auto I) { sm[2 * I] = __uint_as_float(r.v[K][0][I + 0] << 16); sm[2 * I + 1] = __uint_as_float(r.v[K][0][I + 0] & 0xffff0000u); });
      sfor<1, 8>([&](auto J) {
        sfor<0, 4>([&](auto I) { sm[2 * I] += __uint_as_float(r.v[K][J][I + 0] << 16); sm[2 * I + 1] += __uint_as_float(r.v[K][J][I + 0] & 0xffff0000u); });
      });
      const int f = 9 * j + w + K;
      int t, col;
      if (f < 40) { const int ww = f / 10; t = (f - ww * 10) >> 1; col = 64 * ww + 32 * (f & 1) + 16 * (g & 1); }
      else { const int rr = f & 7, c0 = 256 + 48 * ((f - 40) >> 3); t = rr < 5 ? rr : 2 * (rr - 5) + (g & 1); col = c0 + (rr < 5 ? 16 * (g & 1) : 32); }
      if (t < 5)
      {
        u32x4* dst = (u32x4*)((char*)p.out + ((size_t)(b * 80 + t * 16 + n) * 896 + P * 448 + col + 8 * (g >> 1)) * 2);
        const u32x4 val = u32x4{pk2(sm[0], sm[1]), pk2(sm[2], sm[3]), pk2(sm[4], sm[5]), pk2(sm[6], sm[7])};
        if constexpr (ONT) __builtin_nontemporal_store(val, dst); else *dst = val;
      }
    }
  });
}

template <int P  , bool LAST, int ST, bool SAFE>
__device__ __forceinline__ bool front(const Params& p, lds_t lds, unsigned lds0, int b, int j, int w) {
  constexpr bool GQA = (P == 0);
  const int bk = FAKEKV ? 0 : b;
  const int hh = w >> 2, cq = w & 3;
  const unsigned ring = lds0 + LDS_RING + w * 8192;
  f32x4 acc[5][4];
  bf16x8 fa[2][4], fb[5];

  const unsigned koff = GQA ? (bk * 8 + j) * KV_HEAD + w * 16 * 512 : (bk * 16 + 2 * j + hh) * KV_HEAD + cq * 32 * 512;
  const rsrc_t rK = mk_rsrc(p.k[P], koff, KV_HEAD - (GQA ? w * 16 * 512 : cq * 32 * 512));
  auto k_src = [](int g, int i) -> unsigned {
    return GQA ? ((g >> 2) * 256 + (i >> 1) * 128 + (i & 1) * 8) * 512 + (g & 3) * 128 : ((g >> 2) * 128 + i * 8) * 512 + (g & 3) * 128;
  };

  {
    const rsrc_t rW = mk_rsrc(p.wq[P], (512 * j + 64 * w) * 2, W_BYTES - (512 * j + 64 * w) * 2);
    const int l = lane_id(), n = l & 15, g = l >> 4, q = (l >> 2) & 3;
    const unsigned we = voff_nat(l, 8192), ke = voff_k(l);
    const unsigned a0 = LDS_RING + w * 8192 + (8 * g + q) * 128 + (((q >> 1) | ((g & 1) << 1)) * 32) + 8 * (l & 3);
    unsigned x0[2][2];
    x0[0][0] = n * 128 + 16 * (g ^ ((n >> 1) & 7)); x0[1][0] = x0[0][0] ^ 64; x0[0][1] = x0[0][0] + 40960; x0[1][1] = x0[1][0] + 40960;
    asm volatile("" : "+v"(x0[0][1]), "+v"(x0[1][1]));
    const unsigned qw = hh * 40960 + cq * 10240 + l * 16;
    float scl = SCL;
    asm volatile("" : "+s"(scl));
    auto rda = [&](auto S, auto U) { lds_t a = lds + (a0 ^ (U * 32)) + (S & 1) * 4096; fa[S & 1][U] = ldtr(a, a + 512); };
    auto ldb = [&](auto S, auto T) { return ld128(lds + x0[S & 1][S >= 8] + ((S >> 1) & 3) * 10240 + T * 2048); };
    auto piece = [&](auto G, auto I) {
      if constexpr (G < 14) dmap<0, I>(rW, we ^ ((I & 1) * 64), (G * 32 + I * 8) * 8192);
      else dmap<1, I>(rK, ke ^ ((I & 1) * 64), k_src(G - 14, I));
    };
    u32x4 cv;
    auto qput = [&](auto X) {
      constexpr int T = X / 8, H = (X / 4) & 1, c = X & 3;
      const f32x4 u = acc[T][2 * H + c / 2];
      cv[c] = SAFE ? pk2(u[2 * (c & 1)], u[2 * (c & 1) + 1]) : pk2(mul1(u[2 * (c & 1)], scl), mul1(u[2 * (c & 1) + 1], scl));
      if constexpr (c == 3) *(LAS u32x4*)(lds + qw + H * 5120 + T * 1024) = cv;
    };
    bf16x8 fb2[5];
    if constexpr (!LAST && !SAFE) wait_vm<5 + XW>();
    sfor<0, 4>([&](auto U) { rda(IC<0>{}, U); });
    sfor<0, 5>([&](auto T) { fb[T] = ldb(IC<0>{}, T); });
    set_m0(ring);
    need(fa[0]);
    sfor<0, 4>([&](auto I) { piece(IC<2>{}, I); });
    sfor<0, FQ ? 12 : 13>([&](auto S) {
      constexpr int s = S;
      SBAR();
      STAMP2(4, s);
      if constexpr (s == 0) prio_reset();
      if constexpr (s == (FQ ? 8 : 9)) prio_yield(hh);
      if constexpr (s == 0) { if constexpr (!LAST && !SAFE) wait_vm<5 + XW>(); }
      else wait_vm<(LAST && s == 6) ? 0 : 4>();
      sfor<0, 20>([&](auto I) {
        constexpr int i = I, t = i >> 2, u = i & 3;
        acc[t][u] = MFMA(fa[s & 1][u], fb[t], s == 0 ? ZERO4 : acc[t][u]);
        if constexpr (i < 4) rda(IC<s + 1>{}, IC<i & 3>{});
        if constexpr (u == 3) fb[t] = ldb(IC<s + 1>{}, IC<t>{});
        if constexpr (FQ && s == 11 && u == 1) fb2[t] = ldb(IC<13>{}, IC<t>{});
        if constexpr (i == 5) set_m0(ring + ((s + 1) & 1) * 4096);
        if constexpr (i == DM0) need(fa[(s + 1) & 1]);
        if constexpr (i >= DM0 && i < DM0 + 4) piece(IC<s + 3>{}, IC<(i - DM0) & 3>{});
        SBAR();
      });
    });
    STAMP2(4, 12);
    bar();
    if constexpr (SAFE) *(LAS f32x2*)(lds + LDS_EXM + w * 512 + l * 8) = f32x2{-INFINITY, -INFINITY};
    else *(LAS unsigned*)(lds + LDS_BAD) = 0u;
    if constexpr (!FQ) {
      sfor<0, 20>([&](auto I) {
        constexpr int i = I, t = i >> 2, u = i & 3;
        acc[t][u] = MFMA(fa[1][u], fb[t], acc[t][u]);
        if constexpr (i >= 6) { qput(IC<i >= 6 ? 2 * (i - 6) : 0>{}); qput(IC<i >= 6 ? 2 * (i - 6) + 1 : 0>{}); }
        SBAR();
      });
    } else wait_vm<4>();
    if constexpr (FQ) sfor<0, 40>([&](auto I) {
      constexpr int i = I, t = fin_tile(i >> 2), hf = fin_half(i >> 2), u = i & 3;
      acc[t][u] = MFMA(fa[hf][u], hf ? fb2[t] : fb[t], acc[t][u]);
      if constexpr (i < 4) rda(IC<13>{}, IC<i & 3>{});
      if constexpr (i == 5) set_m0(ring + 4096);
      if constexpr (i == DM0) need(fa[1]);
      if constexpr (i >= DM0 && i < DM0 + K1E) piece(IC<15>{}, IC<(i - DM0) & 3>{});
      if constexpr (i >= 13) qput(IC<i >= 13 ? i - 13 : 0>{});
      SBAR();
    });
    STAMP2(4, 13);
    STAMP2(4, 14);
    STAMP(ST + 1);
    sfor<FQ ? 27 : 28, 40>(qput);
    if constexpr (FQ) sfor<K1E, 4>([&](auto I) { piece(IC<15>{}, I); });
    bar();
    if constexpr (LAST) { if (w == 0) arrive<1>(p, lds, b, j); }
  }
  STAMP(ST + 2);

  const rsrc_t rV = GQA ? mk_rsrc(p.v[P], (bk * 8 + j) * KV_HEAD + hh * 16 * 512 + cq * 128, KV_HEAD - (hh * 16 * 512 + cq * 128))
                        : mk_rsrc(p.v[P], (bk * 16 + 2 * j + hh) * KV_HEAD + cq * 128, KV_HEAD - cq * 128);
  constexpr int MT = GQA ? 2 : 1;
  constexpr int NG = GQA ? 8 : 16;
  f32x4 sc[4][10];
  float mx[5 * MT], nm[5 * MT], ls[5 * MT], ev[4];
  u32x2 pq[4][10];
  auto mop = [&](auto ID) {
    constexpr int id = ID, kq = id / 90, k = (id / 9) % 10, part = id % 9, r = GQA ? k : (k >> 1);
    if constexpr (part == 0 || part == 2) asm volatile("" : "+v"(nm[r]));
    if constexpr (part == 0) { ev[0] = __builtin_fmaf(sc[kq][k][0], SCL, nm[r]); ev[1] = __builtin_fmaf(sc[kq][k][1], SCL, nm[r]); }
    if constexpr (part == 1) ev[0] = __builtin_amdgcn_exp2f(ev[0]);
    if constexpr (part == 2) { ev[2] = __builtin_fmaf(sc[kq][k][2], SCL, nm[r]); ev[3] = __builtin_fmaf(sc[kq][k][3], SCL, nm[r]); }
    if constexpr (part == 3) ev[1] = __builtin_amdgcn_exp2f(ev[1]);
    if constexpr (part == 4) ev[2] = __builtin_amdgcn_exp2f(ev[2]);
    if constexpr (part == 5) { if constexpr (kq == 0 && (GQA || (k & 1) == 0)) ls[r] = ev[0] + ev[1]; else ls[r] = (ls[r] + ev[0]) + ev[1]; }
    if constexpr (part == 6) ev[3] = __builtin_amdgcn_exp2f(ev[3]);
    if constexpr (part == 7) { pq[kq][k][0] = pk2(ev[0], ev[1]); ls[r] += ev[2]; }
    if constexpr (part == 8) { pq[kq][k][1] = pk2(ev[2], ev[3]); ls[r] += ev[3]; }
    if constexpr (part == 0) asm volatile("" : "+v"(ev[0]), "+v"(ev[1]));
    if constexpr (part == 1) asm volatile("" : "+v"(ev[0]));
    if constexpr (part == 2) asm volatile("" : "+v"(ev[2]), "+v"(ev[3]));
    if constexpr (part == 3) asm volatile("" : "+v"(ev[1]));
    if constexpr (part == 4) asm volatile("" : "+v"(ev[2]));
    if constexpr (part == 5) asm volatile("" : "+v"(ls[r]));
    if constexpr (part == 6) asm volatile("" : "+v"(ev[3]));
    if constexpr (part == 7) asm volatile("" : "+v"(ls[r]), "+v"(pq[kq][k][0]));
    if constexpr (part == 8) asm volatile("" : "+v"(ls[r]), "+v"(pq[kq][k][1]));
  };
  auto fop = [&](auto ID) {
    constexpr int id = ID, kq = id / 70, k = (id / 7) % 10, part = id % 7, r = GQA ? k : (k >> 1);
    if constexpr (part == 0) asm volatile("" : "+v"(sc[kq][k]));
    if constexpr (part == 0) { ev[0] = __builtin_amdgcn_exp2f(sc[kq][k][0]); asm volatile("" : "+v"(ev[0])); }
    if constexpr (part == 1) { ev[1] = __builtin_amdgcn_exp2f(sc[kq][k][1]); asm volatile("" : "+v"(ev[1])); }
    if constexpr (part == 2) { ev[2] = __builtin_amdgcn_exp2f(sc[kq][k][2]); asm volatile("" : "+v"(ev[2])); }
    if constexpr (part == 3) {
      if constexpr (kq == 0 && (GQA || (k & 1) == 0)) ls[r] = ev[0] + ev[1]; else ls[r] = (ls[r] + ev[0]) + ev[1];
      asm volatile("" : "+v"(ls[r]));
    }
    if constexpr (part == 4) { ev[3] = __builtin_amdgcn_exp2f(sc[kq][k][3]); asm volatile("" : "+v"(ev[3])); }
    if constexpr (part == 5) { pq[kq][k][0] = pk2(ev[0], ev[1]); ls[r] += ev[2]; asm volatile("" : "+v"(ls[r]), "+v"(pq[kq][k][0])); }
    if constexpr (part == 6) { pq[kq][k][1] = pk2(ev[2], ev[3]); ls[r] += ev[3]; asm volatile("" : "+v"(ls[r]), "+v"(pq[kq][k][1])); }
  };
  auto pput = [&](auto KQ, auto K, unsigned pw) {
    constexpr int kq = KQ, k = K;
    if constexpr (GQA) *(LAS u32x2*)(lds + pw + (kq & 1) * 40960 + (k / 5) * 20480 + (k % 5) * 1024) = pq[kq][k];
    else if constexpr (k & 1) *(LAS u32x4*)(lds + pw + (kq & 1) * 40960 + (k / 2) * 1024) = u32x4{pq[kq][k - 1][0], pq[kq][k - 1][1], pq[kq][k][0], pq[kq][k][1]};
  };
  bool grown = false;
  unsigned pw;
  constexpr int PRE1 = SAFE ? 10 : GQA ? PRE1F : 0;
  {
    const int l = lane_id(), n = l & 15, g = l >> 4;
    const unsigned ke = voff_k(l);
    unsigned vv = 0;
    const unsigned k0 = LDS_RING + w * 8192 + n * 128 + 16 * ((g >> 1) ^ ((n >> 1) & 7)) + 8 * (g & 1);
    const unsigned q0 = (GQA ? 0 : hh * 40960) + l * 16;
    unsigned q1 = q0 + 40960;
    asm volatile("" : "+v"(q1));
    constexpr int NSUB = 2 * MT;
    auto rdk = [&](auto G, auto I) {
      lds_t a = lds + (I & 1) * 2048 + (G & 1) * 4096;
      const s16x4 lo = *(LAS const s16x4*)(a + (k0 ^ ((I >> 1) * 64))), hi = *(LAS const s16x4*)(a + (k0 ^ ((I >> 1) * 64 + 32)));
      fa[G & 1][I] = __builtin_shufflevector(lo, hi, 0, 1, 2, 3, 4, 5, 6, 7);
    };
    auto rdq = [&](auto Z, auto T) {
      constexpr int z = Z, gi = z / NSUB, e = (z % NSUB) / MT, h = z % MT;
      fb[T] = ld128(lds + (h ? q1 : q0) + ((2 * (gi & 3) + e) * 5 + T) * 1024);
    };
    auto piece = [&](auto G, auto I) {
      if constexpr (G < NG) dmap<1, I>(rK, ke ^ ((I & 1) * 64), k_src(G, I));
      else if constexpr (!GQA) dmap<(GQA ? 1 : 2), I>(rV, vv, ((G - NG) * 32 + I * 8) * 512);
      else dmap<(GQA ? 1 : 2), I>(rV, vv, ((2 * (G - NG) + (I >> 1)) * 32 + (I & 1) * 8) * 512);
    };
    auto qmax = [&](auto KQ, auto K) {
      constexpr int kq = KQ, k = K, r = GQA ? k : (k >> 1);
      if constexpr ((kq & 1) == 0 && (GQA || (k & 1) == 0)) mx[r] = max3(max3(sc[kq][k][0], sc[kq][k][1], sc[kq][k][2]), sc[kq][k][3], sc[kq][k][3]);
      else mx[r] = max3(max3(mx[r], sc[kq][k][0], sc[kq][k][1]), sc[kq][k][2], sc[kq][k][3]);
      asm volatile("" : "+v"(mx[r]));
    };
    auto xmax = [&](auto HALF, auto C) {
      const int l2 = lane_id(), g2 = l2 >> 4;
      LAS float* em = (LAS float*)(lds + LDS_EXM + HALF * 1536 + ((GQA ? 0 : hh) * 16 + (l2 & 15)) * 48);
      if constexpr (C == 0) __builtin_amdgcn_ds_fmaxf(em + g2, red4<OpMax>(mx[0], mx[1], mx[2], mx[3]), 0, 0, false);
      if constexpr (C == 1 && GQA) __builtin_amdgcn_ds_fmaxf(em + (g2 & 1) * 192 + 4, red2<OpMax>(mx[4], mx[5 * MT - 1]), 0, 0, false);
      if constexpr (C == 1 && !GQA) __builtin_amdgcn_ds_fmaxf(em + 4, red2<OpMax>(mx[4], mx[4]), 0, 0, false);
      if constexpr (C == 2 && GQA) __builtin_amdgcn_ds_fmaxf(em + 192 + g2, red4<OpMax>(mx[5], mx[6], mx[7], mx[8]), 0, 0, false);
    };
    auto rmax = [&](auto HALF) {
      LAS float* em = (LAS float*)(lds + LDS_EXM + HALF * 1536 + ((GQA ? 0 : hh) * 16 + (lane_id() & 15)) * 48);
      sfor<0, MT>([&](auto H) {
        const f32x4 v = *(LAS const f32x4*)(em + H * 192);
        sfor<0, 4>([&](auto T) { mx[H * 5 + T] = v[T + 0]; });
        mx[H * 5 + 4] = em[H * 192 + 4];
      });
    };
    wait_vm<4>();
    sfor<0, 4>([&](auto I) { rdk(IC<0>{}, I); });
    sfor<0, 5>([&](auto T) { rdq(IC<0>{}, T); });
    if constexpr (!SAFE) {
      set_m0(ring);
      need(fa[0]);
      sfor<0, 4>([&](auto I) { piece(IC<2>{}, I); });
    }
    sfor<0, 32>([&](auto Z) {
      constexpr int z = Z, gi = z / NSUB, sub = z % NSUB, e = sub / MT, h = sub % MT;
      SBAR();
      STAMP2(2, z);
      if constexpr (z == 0 || (SAFE && z == 20)) prio_reset();
      if constexpr (SAFE ? (z == 13 || z == 28) : z == PSF) prio_yield(hh);
      if constexpr (SAFE && z == 19) { bar(); rmax(IC<0>{}); }
      if constexpr (z == (NG - (SAFE ? 2 : 3)) * NSUB) vv = voff_spl(lane_id(), 512);
      if constexpr (sub == (SAFE ? NSUB - 1 : 0) && gi + 1 < NG) wait_vm<4>();
      sfor<0, 10>([&](auto I) {
        constexpr int i = I, t = i >> 1, u = i & 1;
        constexpr int kq = GQA ? 2 * (gi >> 2) + u : (gi >> 2), k = GQA ? 5 * h + t : i;
        sc[kq][k] = MFMA(fa[gi & 1][2 * e + u], fb[t], ((gi & 3) == 0 && e == 0) ? ZERO4 : sc[kq][k]);
        if constexpr (u == 1 && z + 1 < 32) rdq(IC<z + 1>{}, IC<t>{});
        if constexpr (SAFE) {
          if constexpr (sub == 0 && i == 1) { set_m0(ring + (gi & 1) * 4096); need(fa[gi & 1]); }
          if constexpr (sub == 0 && i >= 2 && i < 6) piece(IC<gi + 2>{}, IC<(i - 2) & 3>{});
          if constexpr (sub == NSUB - 1 && i >= 6 && gi + 1 < NG) rdk(IC<gi + 1>{}, IC<(i - 6) & 3>{});
        } else if constexpr (gi + 1 < NG) {
          if constexpr (sub == 0 && i < 4) rdk(IC<gi + 1>{}, IC<i & 3>{});
          if constexpr (sub == 0 && i == 5) set_m0(ring + ((gi + 1) & 1) * 4096);
          if constexpr (sub == 0 && i == 6) need(fa[(gi + 1) & 1]);
          if constexpr (sub == 0 && i >= 6) piece(IC<gi + 3>{}, IC<(i - 6) & 3>{});
        }
        if constexpr (SAFE) {
          if constexpr (z == 16 || z == 17) qmax(IC<(z - 16) & 1>{}, I);
          if constexpr (z == 18 && (i == 1 || i == 4 || i == 7)) xmax(IC<0>{}, IC<i / 3>{});
          if constexpr (z == 19 && i >= 5) sfor<0, MT>([&](auto H) { nm[H * 5 + i - 5] = mul1(mx[H * 5 + i - 5], -SCL); });
          if constexpr (z >= 20) { constexpr int x = (z - 20) * 10 + i; sfor<(3 * x + 1) / 2, (3 * x + 4) / 2>(mop); }
          if constexpr (!GQA && z == 26) qmax(IC<2>{}, I);
        } else if constexpr (GQA) {
          if constexpr (z >= 17 && z < 31) fop(IC<(z >= 17 ? z - 17 : 0) * 10 + i>{});
        } else {
          if constexpr (z >= 9 && (z & 7) >= 1) fop(IC<(z >= 9 ? (z / 8 - 1) * 70 + ((z & 7) - 1) * 10 + i : 0)>{});
        }
        SBAR();
      });
    });
    STAMP2(2, 32);
    STAMP(ST + 3);
    if constexpr (SAFE) {
      sfor<4 - MT, 4>([&](auto KQ) { sfor<0, 10>([&](auto K) { qmax(KQ, K); }); });
      sfor<0, 3>([&](auto C) { xmax(IC<1>{}, C); });
    }
    pw = (GQA ? (w >> 1) * 5120 + (w & 1) * 8 : hh * 20480 + cq * 5120) + lane_id() * 16;
    bar();
    if constexpr (SAFE) rmax(IC<1>{});
    sfor<0, 10>([&](auto K) { pput(IC<0>{}, K, pw); });
    sfor<0, PRE1>([&](auto K) { pput(IC<1>{}, K, pw); });
    if constexpr (SAFE) {
      bool any = false;
      sfor<0, 5 * MT>([&](auto R) { any |= __builtin_fmaf(mx[R], SCL, nm[R]) > GROW; });
      grown = __builtin_amdgcn_ballot_w64(any) != 0;
      if (grown) {
        sfor<0, 5 * MT>([&](auto R) {
          const float d = __builtin_fmaf(mx[R], SCL, nm[R]);
          if (d > GROW) { ls[R] *= __builtin_amdgcn_exp2f(-d); nm[R] = -SCL * mx[R]; }
        });
      }
      sfor<180, 216>(mop);
    }
  }
  STAMP(ST + 4);

  unsigned redo = 0;
  const unsigned wwin = hh ? 512 + (cq * 96 & ~63) : w * 128;
  const rsrc_t rO = mk_rsrc(p.wo[P], 512 * j * 896 + wwin, W_BYTES - (512 * j * 896 + wwin));
  {
    const int l = lane_id(), g = l >> 4, q = (l >> 2) & 3;
    const unsigned vv = voff_spl(l, 512);
    const unsigned a0 = LDS_RING + (GQA ? cq : w) * 8192 + (4 * g + q) * 128 + (((q >> 1) | ((g & 1) << 1)) * 32) + 8 * (l & 3);
    const unsigned p0 = hh * 20480 + l * 16;
    auto rda = [&](auto S, auto U) {
      lds_t a = lds + (a0 ^ (U * 32)) + (GQA ? (S & 3) * 2048 : (S & 1) * 4096);
      fa[S & 1][U] = ldtr(a, a + (GQA ? 32768 : 2048));
    };
    auto rdb = [&](auto S, auto T) { fb[T] = ld128(lds + p0 + ((S >> 2) & 1) * 40960 + ((S & 3) * 5 + T) * 1024); };
    unsigned ov = 0, ow = 0;
    auto piece = [&](auto G, auto I) {
      if constexpr (G < 16) dmap<(GQA ? 1 : 2), I>(rV, vv, (G * 32 + I * 8) * 512);
      else dmap<0, I>(rO, ov, ((G - 16) * (GQA ? 16 : 32) + I * 8) * 896);
    };
    constexpr auto TW = [](int s) -> int { return s == 12; };
    unsigned seen = 0;
    bool bad = false;
    float rl[5], rs[2] = {0.f, 0.f};
    f32x4 lv[2];
    unsigned el = 0, eg = 0;
    auto oput = [&](auto T, auto H) {
      const f32x4 u = acc[T][2 * H], v = acc[T][2 * H + 1];
      const float r = rl[T];
      *(LAS u32x4*)(lds + ow + H * 5120 + T * 1024) = u32x4{pk2(mul1(u[0], r), mul1(u[1], r)), pk2(mul1(u[2], r), mul1(u[3], r)),
                                                            pk2(mul1(v[0], r), mul1(v[1], r)), pk2(mul1(v[2], r), mul1(v[3], r))};
    };
    if constexpr (GQA) {
      wait_vm_bar<6>();
      sfor<0, 4>([&](auto U) { rda(IC<0>{}, U); });
    } else if constexpr (PBB) {
      wait_vm<4>();
      sfor<0, 4>([&](auto U) { rda(IC<0>{}, U); });
      set_m0(ring);
      bar();
      sfor<0, 4>([&](auto I) { piece(IC<2>{}, I); });
    } else {
      bar();
      wait_vm<4>();
      sfor<0, 4>([&](auto U) { rda(IC<0>{}, U); });
    }
    STAMP(ST + 5);
    sfor<0, 5>([&](auto T) { rdb(IC<0>{}, T); });
    if constexpr (!GQA && !PBB) { set_m0(ring); need(fa[0]); sfor<0, 4>([&](auto I) { piece(IC<2>{}, I); }); }
    sfor<0, 16>([&](auto S) {
      constexpr int s = S;
      SBAR();
      STAMP2(3, s);
      if constexpr (s == 0) prio_reset();
      if constexpr (GQA) {
        constexpr int nv = 4 + TW(s - 1) + TW(s - 2) + TW(s - 3);
        if constexpr ((s & 3) == 3) wait_vm_bar<nv>();
        else { need(fa[s & 1]); if constexpr (ABL & 2) wait_vm<nv>(); else wait_vm_bar_raw<nv>(); }
      } else {
        if constexpr (s == 15) bar();
        else { if constexpr ((s & 3) == 3 && !(ABL & 2)) bar(); wait_vm<4 + TW(s - 1) + TW(s - 2)>(); }
      }
      if constexpr (SAFE && s == 8) {
        if (grown) {
          LAS float* em = (LAS float*)(lds + LDS_EXM + (hh * 16 + (lane_id() & 15)) * 48);
          sfor<0, 5>([&](auto T) {
            const float d = __builtin_fmaf(em[384 + T], SCL, mul1(em[T + 0], -SCL));
            const float a = d > GROW ? __builtin_amdgcn_exp2f(-d) : 1.f;
            sfor<0, 4>([&](auto U) { acc[T][U] *= a; });
          });
        }
      }
      if constexpr (!SAFE && s == 14) { if (__builtin_amdgcn_ballot_w64(bad) != 0) *(LAS unsigned*)(lds + LDS_BAD) = 1u; }
      if constexpr (!SAFE && s == 15) redo = *(LAS const unsigned*)(lds + LDS_BAD);
      if constexpr (s == 11) ov = voff_spl(lane_id(), 896);
      if constexpr (s == 15) ow = hh * 40960 + cq * 10240 + lane_id() * 16;
      sfor<0, 20>([&](auto I) {
        constexpr int i = I, t = i >> 2, u = i & 3;
        acc[t][u] = MFMA(fa[s & 1][u], fb[t], s == 0 ? ZERO4 : acc[t][u]);
        if constexpr (GQA) {
          if constexpr (i == 0) set_m0(ring + (s & 3) * 2048);
          if constexpr (i == 1 || i == 2) piece(IC<s + 4>{}, IC<i - 1>{});
          if constexpr (s < 15 && i >= 4 && i < 7) rda(IC<s + 1>{}, IC<i - 4>{});
          if constexpr (s < 15 && i == 8) rda(IC<s + 1>{}, IC<3>{});
        } else if constexpr (s < 15) {
          if constexpr (i < 4) rda(IC<s + 1>{}, IC<i & 3>{});
          if constexpr (i == 5) set_m0(ring + ((s + 1) & 1) * 4096);
          if constexpr (i == DM0) need(fa[(s + 1) & 1]);
          if constexpr (i >= DM0 && i < DM0 + 4) piece(IC<s + 3>{}, IC<(i - DM0) & 3>{});
        }
        if constexpr (s < 15 && u == 3) rdb(IC<s + 1>{}, IC<t>{});
        if constexpr (i >= 1 && i < 19) {
          constexpr int x = 18 * s + i - 1, f0 = GQA ? 140 : 210;
          if constexpr (SAFE && 216 + x < 360) mop(IC<(216 + x) % 360>{});
          if constexpr (!SAFE && f0 + x < 280 && !(ABL & 4)) fop(IC<(f0 + x) % 280>{});
        }
        if constexpr (!(ABL & 1) && s < 3 && (i == 5 || i == 10 || i == 14 || i == 18)) {
          constexpr int k = PRE1 + 4 * s + (i == 5 ? 0 : i == 10 ? 1 : i == 14 ? 2 : 3);
          if constexpr (k < 10) pput(IC<1>{}, IC<k % 10>{}, pw);
        }
        if constexpr (!(ABL & 1) && s >= 3 && s <= 10 && (i == 5 || i == 10 || i == 14)) {
          constexpr int q4 = (s - 3) & 3, k = q4 * 3 - (q4 > 2) + (i == 5 ? 0 : i == 10 ? 1 : 2);
          if constexpr (k < 10 && !(q4 == 2 && i == 14)) pput(IC<s < 7 ? 2 : 3>{}, IC<k % 10>{}, pw);
        }
        if constexpr (s == 10) {
          if constexpr (i == 0) { const int l2 = lane_id(); el = LDS_EXL + ((GQA ? 0 : hh) * 80 + (l2 & 15)) * 48 + (GQA ? w : cq) * 4; eg = l2 >> 4; }
          if constexpr (i == 2) rs[0] = sw16<OpAdd>(ls[0], ls[1]);
          if constexpr (i == 3) rs[1] = sw16<OpAdd>(ls[2], ls[3]);
          if constexpr (i == 4) rs[0] = sw32<OpAdd>(rs[0], rs[1]);
          if constexpr (i == 6) *(LAS float*)(lds + el + eg * 768) = rs[0];
          if constexpr (i == 7) rs[1] = sw16<OpAdd>(ls[4], ls[5 * MT - 1]);
          if constexpr (i == 8) rs[1] = sw32<OpAdd>(rs[1], rs[1]);
          if constexpr (i == 9) *(LAS float*)(lds + el + 3072 + (GQA ? (eg & 1) * 3840 : 0)) = rs[1];
          if constexpr (GQA) {
            if constexpr (i == 11) rs[0] = sw16<OpAdd>(ls[5], ls[6]);
            if constexpr (i == 12) rs[1] = sw16<OpAdd>(ls[7], ls[8]);
            if constexpr (i == 13) rs[0] = sw32<OpAdd>(rs[0], rs[1]);
            if constexpr (i == 16) *(LAS float*)(lds + el + 3840 + eg * 768) = rs[0];
          }
          if constexpr (i >= 2 && i < 14) asm volatile("" : "+v"(rs[0]), "+v"(rs[1]));
        }
        if constexpr (TW(s) && i == 14) { seen = __hip_atomic_load(sync_word<LAST ? 1 : 0>(p, b, j), RLX); asm volatile("" ::: "memory"); }
        if constexpr (s >= 11 && s < 14) {
          constexpr int x = (s - 11) * 20 + i - 2, T = x / 10, c = x % 10;
          if constexpr (x == -2) el = LDS_EXL + (hh * 80 + (lane_id() & 15)) * 48;
          if constexpr (x >= 0 && x < 50) {
            if constexpr (c == 0) { lv[0] = *(LAS const f32x4*)(lds + el + T * 768); if constexpr (GQA) lv[1] = *(LAS const f32x4*)(lds + el + T * 768 + 16); }
            if constexpr (c == 4) rs[0] = (lv[0][0] + lv[0][1]) + lv[0][2];
            if constexpr (c == 5) rs[0] = GQA ? (rs[0] + lv[0][3]) + lv[1][0] : rs[0] + lv[0][3];
            if constexpr (c == 6 && GQA) rs[0] = (rs[0] + lv[1][1]) + lv[1][2];
            if constexpr (c == 7 && GQA) rs[0] += lv[1][3];
            if constexpr (c == 8) rl[T % 5] = __builtin_amdgcn_rcpf(rs[0]);
            if constexpr (c == 9 && !SAFE) bad |= !(rs[0] >= SUM_LO && rs[0] <= SUM_HI);
            if constexpr (c >= 4 && c < 8) asm volatile("" : "+v"(rs[0]));
            if constexpr (c == 8) asm volatile("" : "+v"(rl[T % 5]));
          }
        }
        if constexpr (!(ABL & 8) && s == 15 && i >= 6 && (i & 1) == 0) oput(IC<(i - 6) / 4>{}, IC<((i - 6) / 2) % 2>{});
        SBAR();
      });
    });
    STAMP2(3, 16);
    STAMP(ST + 6);
    sfor<7, 10>([&](auto I) { oput(IC<I / 2>{}, IC<I % 2>{}); });
    settle<LAST ? 1 : 0>(p, lds, b, j, seen);
  }
  return __builtin_amdgcn_readfirstlane(redo) != 0;
}

__device__ __forceinline__ void issue_wq01(const Params& p, int P, int j, int w, unsigned lds0) {
  const rsrc_t rW = mk_rsrc(p.wq[P], (512 * j + 64 * w) * 2, W_BYTES - (512 * j + 64 * w) * 2);
  const unsigned we = voff_nat(lane_id(), 8192);
  sfor<0, 2>([&](auto G) {
    set_m0(lds0 + LDS_RING + w * 8192 + G * 4096);
    sfor<0, 4>([&](auto I) { dmap<0, I>(rW, we ^ ((I & 1) * 64), (G * 32 + I * 8) * 8192); });
  });
}

template <int P, bool LAST, int ST>
__device__ __forceinline__ void run_problem(const Params& p, lds_t lds, unsigned lds0, int b, int j, int w) {
  if (__builtin_expect((front<P, LAST, ST, false>(p, lds, lds0, b, j, w) && !ABL) || FORCE_SAFE, 0)) {
    wait_vm_bar<0>();
    issue_x(p, P, b, w, lds0);
    issue_wq01(p, P, j, w, lds0);
    wait_vm_bar<0>();
    front<P, LAST, 30, true>(p, lds, lds0, fresh(b), fresh(j), fresh(w));
  }
  b = fresh(b); j = fresh(j); w = fresh(w);
  const int hh = w >> 2, cq = w & 3;
  const unsigned ring = lds0 + LDS_RING + w * 8192;
  f32x4 acc[5][4];
  bf16x8 fa[2][4], fb[5];
  const unsigned wwin = hh ? 512 + (cq * 96 & ~63) : w * 128;
  const rsrc_t rO = mk_rsrc(p.wo[P], 512 * j * 896 + wwin, W_BYTES - (512 * j * 896 + wwin));

  RedRows red;
  auto oproj = [&](auto NTILE) {
    constexpr int NT = NTILE, NS = 5 * NT, D0 = NT == 4 ? DM0 : 5;
    const rsrc_t rN = mk_rsrc(p.wq[1 - P], (512 * j + 64 * w) * 2, W_BYTES - (512 * j + 64 * w) * 2);
    const int l = lane_id(), g = l >> 4, q = (l >> 2) & 3;
    const unsigned ov = voff_spl(l, 896), we = voff_nat(l, 8192);
    auto piece = [&](auto G, auto I) {
      if constexpr (G < 16) dmap<0, I>(rO, ov, (G * 32 + I * 8) * 896);
      else dmap<0, I>(rN, we ^ ((I & 1) * 64), ((G - 16) * 32 + I * 8) * 8192);
    };
    unsigned a0[NT], o1 = l * 16 + 40960;
    sfor<0, NT>([&](auto U) {
      a0[U] = LDS_RING + w * 8192 + (4 * g + q) * 128 + ((((q >> 1) | ((g & 1) << 1)) ^ (U + (NT == 3 ? cq & 1 : 0))) * 32) + 8 * (l & 3);
    });
    asm volatile("" : "+v"(o1));
    auto rda = [&](auto S, auto U) { lds_t a = lds + a0[U] + (S & 1) * 4096; fa[S & 1][U] = ldtr(a, a + 2048); };
    auto rdb = [&](auto S, auto T) { fb[T] = ld128(lds + (S < 8 ? l * 16 : o1) + ((S & 7) * 5 + T) * 1024); };
    auto needa = [&](auto X) {
      if constexpr (NT == 4) need(fa[X]);
      else need3(fa[X]);
    };
    const rsrc_t rS = mk_rsrc(p.slab, ((P * 32 + b) * 8 + j) * SLAB + (NT == 4 ? w * 10240 : 40960 + cq * 8192), 10240);
    auto sput = [&](auto R) {
      constexpr int r = R;
      constexpr int tu = NT == 4 ? r / 2 : r < 5 ? r : 2 * (r - 5), cu = NT == 4 ? 2 * (r & 1) : r < 5 ? 0 : 2;
      constexpr int tv = NT == 4 ? r / 2 : r < 5 ? r : r == 7 ? 4 : 2 * (r - 5) + 1, cv = NT == 4 ? 2 * (r & 1) + 1 : r < 5 ? 1 : 2;
      const f32x4 u = acc[tu][cu], v = acc[tv][cv];
      auto r0 = __builtin_amdgcn_permlane16_swap(pk2(u[0], u[1]), pk2(v[0], v[1]), false, false);
      auto r1 = __builtin_amdgcn_permlane16_swap(pk2(u[2], u[3]), pk2(v[2], v[3]), false, false);
      __builtin_amdgcn_raw_buffer_store_b128(u32x4{r0[0], r1[0], r0[1], r1[1]}, rS, l * 16 + r * 1024, 0, SAUX  );
    };
    if constexpr (!PBO) bar();
    wait_vm<4>();
    sfor<0, NT>([&](auto U) { rda(IC<0>{}, U); });
    set_m0(ring);
    if constexpr (PBO) bar();
    STAMP(ST + 7);
    sfor<0, 5>([&](auto T) { rdb(IC<0>{}, T); });
    if constexpr (!PBO) needa(IC<0>{});
    sfor<0, 4>([&](auto I) { piece(IC<2>{}, I); });
    sfor<0, 16>([&](auto S) {
      constexpr int s = S;
      SBAR();
      STAMP2(5, s);
      if constexpr (s == 0) prio_reset();
      if constexpr (s == PO) prio_yield(hh);
      if constexpr (s < 15) wait_vm<4>();
      else if constexpr (!LAST) { bar(); issue_x<X2NT != 0>(p, 1 - P, b, w, lds0); }
      sfor<0, NS>([&](auto I) {
        constexpr int i = I, t = i / NT, u = i % NT;
        acc[t][u] = MFMA(fa[s & 1][u], fb[t], s == 0 ? ZERO4 : acc[t][u]);
        if constexpr (s < 15) {
          if constexpr (i < NT) rda(IC<s + 1>{}, IC<i % NT>{});
          if constexpr (u == NT - 1) rdb(IC<s + 1>{}, IC<t>{});
          if constexpr (!(LAST && s >= 13)) {
            if constexpr (i == D0 - 2) set_m0(ring + ((s + 1) & 1) * 4096);
            if constexpr (i == D0) needa(IC<(s + 1) & 1>{});
            if constexpr (i >= D0 && i < D0 + 4) piece(IC<s + 3>{}, IC<(i - D0) & 3>{});
          }
          if constexpr (LAST && s >= 13 && i >= 3 && i < 15 && i % 3 == 0) red_load<0, (4 * (s - 13) + i / 3 - 1) & 7>(red, p, 1 - P, b, j, w);
        } else if constexpr (NT == 4) {
          if constexpr (i >= 6 && (i & 1) == 0) sput(IC<(i - 6) / 2>{});
        } else {
          if constexpr (i == 5 || i == 8 || i == 11 || i == 14) sput(IC<(i - 5) / 3>{});
          if constexpr (i == 10) sput(IC<5>{});
        }
        SBAR();
      });
    });
    STAMP2(5, 16);
    STAMP(ST + 8);
    if constexpr (NT == 4) sfor<7, 10>(sput);
    else { sput(IC<6>{}); sput(IC<4>{}); sput(IC<7>{}); }
    if constexpr (!LAST) wait_vm_bar<NT == 4 ? 10 : 8>();
  };
  if (hh == 0) oproj(IC<4>{});
  else oproj(IC<3>{});
  STAMP(ST + 9);
  if constexpr (LAST) {
    wait_vm<0>();
    STAMP(21);
    bar();
    STAMP(22);
    if (w == 0) arrive<2>(p, lds, b, j);
    if (w == 7) sfor<0, 8>([&](auto J) { red_load<1, J>(red, p, 1 - P, b, j, w); });
    red_store(red, p, 1 - P, b, j, w);
    settle<2>(p, lds, b, j, __hip_atomic_load(sync_word<2>(p, b, j), RLX));
    STAMP(23);
    sfor<0, 8>([&](auto J) { red_load<0, J>(red, p, P, b, j, w); });
    if (w == 7) sfor<0, 8>([&](auto J) { red_load<1, J>(red, p, P, b, j, w); });
    red_store(red, p, P, b, j, w);
  }
}

__global__ __launch_bounds__(512, 2) void fused_attn_kernel(Params p) {
  extern __shared__ __attribute__((aligned(16))) char smem[];
  lds_t lds = (lds_t)smem;
  const unsigned lds0 = (unsigned)(uintptr_t)lds;
  const int w = __builtin_amdgcn_readfirstlane(threadIdx.x >> 6);
  const int j = blockIdx.x & 7, b = blockIdx.x >> 3;
  const int first = b & 1;
  STAMP(0);
  {
    if constexpr (CODEW) {
      unsigned long long pc;
      asm volatile("s_getpc_b64 %0" : "=s"(pc));
      warm(lds0, mk_rsrc((const void*)(pc & ~127ull), (b * 8 + w) * 5 * 128, 4 * 128 + 4), lane_id() * 128);
    }
    unsigned found = 0;
    if (w == 7) found = __hip_atomic_load(flag_word(p, b, lane_id() & 7), RLX);
    asm volatile("" ::: "memory");
    issue_x(p, first, b, w, lds0);
    asm volatile("s_barrier" ::: "memory");
    const int l = lane_id();
    issue_wq01(p, first, j, w, lds0);
    const unsigned id = (w & 3) * 64 + l;
    if (w < 4) warm(lds0, mk_rsrc(p.wq[first], 0, W_BYTES), ((b >> 1) * 28 + (id >> 3)) * 8192 + 1024 * j + (id & 7) * 128);
    else warm(lds0, mk_rsrc(p.wo[first], 0, W_BYTES), 512 * j * 896 + (b >> 1) * 28672 + id * 128);
    if constexpr (XW) warm(lds0, mk_rsrc(p.x[1 - first], b * X_BYTES + (j * 70 + w * 9) * 128, w == 7 ? 6 * 128 + 4 : 8 * 128 + 4), l * 128);
    wait_vm_bar<9 + XW>();
    if (w == 7) {
      *(LAS unsigned*)(lds + LDS_OLD + (l & 7) * 4) = found;
      if (l < 8) __hip_atomic_store(ack_word(p, b, l, j), found + 1u, RLX);
    }
  }
  STAMP(10);
  if (first == 0) {
    run_problem<0, false, 0>(p, lds, lds0, fresh(b), fresh(j), fresh(w));
    run_problem<1, true, 10>(p, lds, lds0, fresh(b), fresh(j), fresh(w));
  } else {
    run_problem<1, false, 0>(p, lds, lds0, fresh(b), fresh(j), fresh(w));
    run_problem<0, true, 10>(p, lds, lds0, fresh(b), fresh(j), fresh(w));
  }
  STAMP(20);
}

extern "C" void kernel_launch(void* const* d_in, const int* in_sizes, int n_in, void* d_out, int out_size, void* d_ws,
                              size_t ws_size, hipStream_t stream) {
  Params p;
  for (int i = 0; i < 2; ++i) {
    p.x[i] = d_in[5 * i + 0]; p.k[i] = d_in[5 * i + 1]; p.v[i] = d_in[5 * i + 2]; p.wq[i] = d_in[5 * i + 3]; p.wo[i] = d_in[5 * i + 4];
  }
  char* top = (char*)d_ws + ((ws_size - 512 * (size_t)SLAB - 262144) & ~(size_t)4095);
  p.sync = (unsigned*)top;
  p.slab = top + 262144;
  p.out = d_out;
  p.diag = (unsigned long long*)((char*)d_ws + (64u << 20));
  (void)hipFuncSetAttribute((const void*)fused_attn_kernel, hipFuncAttributeMaxDynamicSharedMemorySize, LDS_TOTAL);
  fused_attn_kernel<<<256, 512, LDS_TOTAL, stream>>>(p);
}
```

```cpp
#include <hip/hip_runtime.h>
#include <hip/hip_bf16.h>
#include <stdint.h>
#include <type_traits>

typedef short bf16x8 __attribute__((ext_vector_type(8)));
typedef short s16x4 __attribute__((ext_vector_type(4)));
typedef float f32x4 __attribute__((ext_vector_type(4)));
typedef float f32x2 __attribute__((ext_vector_type(2)));
typedef __bf16 bf16x2 __attribute__((ext_vector_type(2)));
typedef unsigned u32x4 __attribute__((ext_vector_type(4)));
typedef unsigned u32x2 __attribute__((ext_vector_type(2)));
#define LAS __attribute__((address_space(3)))
typedef __amdgpu_buffer_rsrc_t rsrc_t;
typedef LAS char* lds_t;

constexpr int LDS_RING = 81920;
constexpr int LDS_EXM = 147456;
constexpr int LDS_EXL = 151552;
constexpr int LDS_DUMMY = 159232;
constexpr int LDS_OLD = 159488;
constexpr int LDS_TOTAL = 159552;
constexpr unsigned X_BYTES = 80 * 448 * 2;
constexpr unsigned W_BYTES = 448 * 4096 * 2;
constexpr unsigned KV_HEAD = 512 * 256 * 2;
constexpr unsigned SLAB = 72 * 1024;
constexpr int CODE_LINES = 5;
constexpr float SCL = 0.0625f * 1.4426950408889634f;
constexpr float GROW = 20.f;

template <int I, int N, class F>
__device__ __forceinline__ void sfor(F&& f) {
  if constexpr (I < N) { f(std::integral_constant<int, I>{}); sfor<I + 1, N>(f); }
}
#define SBAR() __builtin_amdgcn_sched_barrier(0)
#define MFMA(a, b, c) __builtin_amdgcn_mfma_f32_16x16x32_bf16(a, b, c, 0, 0, 0)
template <int N> __device__ __forceinline__ void wait_vm() { asm volatile("s_waitcnt vmcnt(%0)" ::"i"(N) : "memory"); }
__device__ __forceinline__ void bar() { asm volatile("s_waitcnt lgkmcnt(0)\n\ts_barrier" ::: "memory"); }
template <int N> __device__ __forceinline__ void wait_vm_bar_raw() { asm volatile("s_waitcnt vmcnt(%0)\n\ts_barrier" ::"i"(N) : "memory"); }
template <int N> __device__ __forceinline__ void wait_vm_bar() { asm volatile("s_waitcnt vmcnt(%0) lgkmcnt(0)\n\ts_barrier" ::"i"(N) : "memory"); }
__device__ __forceinline__ void prio_reset() { asm volatile("s_setprio 1"); }
__device__ __forceinline__ void prio_yield(int hh) { asm volatile("s_cmp_lg_u32 %0, 0\n\ts_cbranch_scc1 1f\n\ts_setprio 0\n1:" ::"s"(hh) : "scc"); }
__device__ __forceinline__ int lane_id() {
  int l; asm volatile("v_mbcnt_lo_u32_b32 %0, -1, 0\n\tv_mbcnt_hi_u32_b32 %0, -1, %0" : "=v"(l)); return l;
}
__device__ __forceinline__ int fresh(int x) { asm volatile("" : "+s"(x)); return x; }
__device__ __forceinline__ unsigned pk2(float a, float b) {
  return __builtin_bit_cast(unsigned, __builtin_convertvector(f32x2{a, b}, bf16x2));
}
__device__ __forceinline__ rsrc_t mk_rsrc(const void* p, unsigned off, unsigned bytes) {
  return __builtin_amdgcn_make_buffer_rsrc((void*)((const char*)p + off), (short)0, (int)bytes, 0x00020000);
}
__device__ __forceinline__ void need(const bf16x8 (&f)[4]) { asm volatile("" ::"v"(f[0]), "v"(f[1]), "v"(f[2]), "v"(f[3]) : "memory"); }
__device__ __forceinline__ void need3(const bf16x8 (&f)[4]) { asm volatile("" ::"v"(f[0]), "v"(f[1]), "v"(f[2]) : "memory"); }
__device__ __forceinline__ void set_m0(unsigned m0v) { asm volatile("s_mov_b32 m0, %0\n\ts_nop 0" ::"s"(m0v) : "memory"); }
template <int POL, int I>
__device__ __forceinline__ void dmap(rsrc_t r, unsigned v, unsigned src) {
  if constexpr (POL == 1) asm volatile("buffer_load_dwordx4 %0, %1, %2 offen offset:%3 nt lds" ::"v"(v), "s"(r), "s"(src - 1024u * I), "i"(1024 * I) : "memory");
  else if constexpr (POL == 2) asm volatile("buffer_load_dwordx4 %0, %1, %2 offen offset:%3 sc1 lds" ::"v"(v), "s"(r), "s"(src - 1024u * I), "i"(1024 * I) : "memory");
  else asm volatile("buffer_load_dwordx4 %0, %1, %2 offen offset:%3 lds" ::"v"(v), "s"(r), "s"(src - 1024u * I), "i"(1024 * I) : "memory");
}
__device__ __forceinline__ void warm(unsigned lds0, rsrc_t r, unsigned v) {
  asm volatile("s_mov_b32 m0, %0\n\ts_nop 0\n\tbuffer_load_dword %1, %2, 0 offen lds" ::"s"(lds0 + LDS_DUMMY), "v"(v), "s"(r) : "memory");
}
extern "C" __device__ float llvm_fmul_legacy(float, float) __asm("llvm.amdgcn.fmul.legacy");
__device__ __forceinline__ float mul1(float a, float b) { return llvm_fmul_legacy(a, b); }
__device__ __forceinline__ bf16x8 ld128(lds_t p) { return *(LAS const bf16x8*)p; }
__device__ __forceinline__ bf16x8 ldtr(lds_t p0, lds_t p1) {
  s16x4 lo = __builtin_amdgcn_ds_read_tr16_b64_v4i16((LAS s16x4*)p0);
  s16x4 hi = __builtin_amdgcn_ds_read_tr16_b64_v4i16((LAS s16x4*)p1);
  return __builtin_shufflevector(lo, hi, 0, 1, 2, 3, 4, 5, 6, 7);
}
__device__ __forceinline__ float max3(float a, float b, float c) { return __builtin_fmaxf(__builtin_fmaxf(a, b), c); }
struct OpMax { static __device__ __forceinline__ float f(float a, float b) { return max3(a, b, b); } };
struct OpAdd { static __device__ __forceinline__ float f(float a, float b) { return a + b; } };
template <class Op> __device__ __forceinline__ float sw16(float a, float b) {
  auto r = __builtin_amdgcn_permlane16_swap(__float_as_uint(a), __float_as_uint(b), false, false);
  return Op::f(__uint_as_float(r[0]), __uint_as_float(r[1]));
}
template <class Op> __device__ __forceinline__ float sw32(float a, float b) {
  auto r = __builtin_amdgcn_permlane32_swap(__float_as_uint(a), __float_as_uint(b), false, false);
  return Op::f(__uint_as_float(r[0]), __uint_as_float(r[1]));
}
template <class Op> __device__ __forceinline__ float red4(float a, float b, float c, float d) { return sw32<Op>(sw16<Op>(a, b), sw16<Op>(c, d)); }
template <class Op> __device__ __forceinline__ float red2(float a, float b) { const float x = sw16<Op>(a, b); return sw32<Op>(x, x); }

__device__ __forceinline__ unsigned voff_nat(int l, unsigned pitch) { return (l >> 3) * pitch + 16 * ((l & 7) ^ (((l >> 4) & 1) << 1)); }
__device__ __forceinline__ unsigned voff_spl(int l, unsigned pitch) { return (l >> 3) * pitch + 16 * ((l & 7) ^ (((l >> 4) & 3) << 1)); }
__device__ __forceinline__ unsigned voff_k(int l) { return (l >> 3) * 512 + 16 * ((l & 7) ^ ((l >> 4) & 3)); }

struct Params {
  const void *x[2], *k[2], *v[2], *wq[2], *wo[2];
  void* slab;
  unsigned* sync;
  void* out;
};


constexpr int DM0 = 8;
template <int V> using IC = std::integral_constant<int, V>;
constexpr f32x4 ZERO4 = {0.f, 0.f, 0.f, 0.f};

__device__ __forceinline__ void issue_x(const Params& p, int P, int b, int w, unsigned lds0) {
  w = fresh(w);
  const int l = lane_id();
  const unsigned v = (l >> 3) * 896 + 16 * ((l & 7) ^ ((l >> 4) & 3));
  const rsrc_t rx = mk_rsrc(p.x[P], b * X_BYTES, X_BYTES);
  sfor<0, 9>([&](auto I) {
    const unsigned pc = w * 9 + I, blk = (pc * 205) >> 11, rg = pc - blk * 10;
    set_m0(lds0 + pc * 1024);
    dmap<0, 0>(rx, v ^ ((rg & 1) * 64), rg * 7168 + blk * 128);
  });
}

typedef __attribute__((address_space(1))) unsigned gu32;
#define RLX __ATOMIC_RELAXED, __HIP_MEMORY_SCOPE_AGENT
__device__ __forceinline__ gu32* flag_word(const Params& p, int b, int j) { return (gu32*)p.sync + (b * 8 + j) * 32; }
__device__ __forceinline__ gu32* ack_word(const Params& p, int b, int prod, int cons) { return (gu32*)p.sync + 8192 + ((b * 8 + prod) * 8 + cons) * 16; }
template <int STAGE>
__device__ __forceinline__ void arrive(const Params& p, lds_t lds, int b, int j) {
  const unsigned old = *(LAS const unsigned*)(lds + LDS_OLD + j * 4);
  if (lane_id() == 0) __hip_atomic_store(flag_word(p, b, j), old + STAGE, RLX);
}
template <int STAGE> __device__ __forceinline__ gu32* sync_word(const Params& p, int b, int j) {
  const int c = lane_id() & 7;
  return STAGE ? flag_word(p, b, c) : ack_word(p, b, j, c);
}
template <int STAGE>
__device__ __forceinline__ void settle(const Params& p, lds_t lds, int b, int j, unsigned seen) {
  const unsigned old = *(LAS const unsigned*)(lds + LDS_OLD + (STAGE ? lane_id() & 7 : j) * 4);
  for (unsigned spins = 0; spins < (1u << 22); ++spins) {
    if (__builtin_amdgcn_ballot_w64(STAGE ? seen - old - STAGE > 2u - STAGE : seen != old + 1u) == 0) break;
    __builtin_amdgcn_s_sleep(2);
    seen = __hip_atomic_load(sync_word<STAGE>(p, b, j), RLX);
  }
  asm volatile("" ::: "memory");
}
struct RedRows { u32x4 v[2][8]; };
template <int K, int J>
__device__ __forceinline__ void red_load(RedRows& r, const Params& p, int P, int b, int j, int w) {
  const rsrc_t rS = mk_rsrc(p.slab, (P * 32 + b) * 8 * SLAB, 8 * SLAB);
  r.v[K][J] = __builtin_amdgcn_raw_buffer_load_b128(rS, lane_id() * 16, J * SLAB + (9 * j + w + K) * 1024, 16  );
}
__device__ __forceinline__ void red_store(const RedRows& r, const Params& p, int P, int b, int j, int w) {
  const int l = lane_id(), n = l & 15, g = l >> 4;
  sfor<0, 2>([&](auto K) {
    if (K == 0 || w == 7) {
      float sm[8];
      sfor<0, 4>([&](auto I) { sm[2 * I] = __uint_as_float(r.v[K][0][I + 0] << 16); sm[2 * I + 1] = __uint_as_float(r.v[K][0][I + 0] & 0xffff0000u); });
      sfor<1, 8>([&](auto J) {
        sfor<0, 4>([&](auto I) { sm[2 * I] += __uint_as_float(r.v[K][J][I + 0] << 16); sm[2 * I + 1] += __uint_as_float(r.v[K][J][I + 0] & 0xffff0000u); });
      });
      const int f = 9 * j + w + K;
      int t, col;
      if (f < 40) { const int ww = f / 10; t = (f - ww * 10) >> 1; col = 64 * ww + 32 * (f & 1) + 16 * (g & 1); }
      else { const int rr = f & 7, c0 = 256 + 48 * ((f - 40) >> 3); t = rr < 5 ? rr : 2 * (rr - 5) + (g & 1); col = c0 + (rr < 5 ? 16 * (g & 1) : 32); }
      if (t < 5)
        *(u32x4*)((char*)p.out + ((size_t)(b * 80 + t * 16 + n) * 896 + P * 448 + col + 8 * (g >> 1)) * 2) =
            u32x4{pk2(sm[0], sm[1]), pk2(sm[2], sm[3]), pk2(sm[4], sm[5]), pk2(sm[6], sm[7])};
    }
  });
}

template <int P  , bool LAST>
__device__ __forceinline__ void run_problem(const Params& p, lds_t lds, unsigned lds0, int b, int j, int w) {
  constexpr bool GQA = (P == 0);
  const int hh = w >> 2, cq = w & 3;
  const unsigned ring = lds0 + LDS_RING + w * 8192;
  f32x4 acc[5][4];
  bf16x8 fa[2][4], fb[5];

  const unsigned koff = GQA ? (b * 8 + j) * KV_HEAD + w * 16 * 512 : (b * 16 + 2 * j + hh) * KV_HEAD + cq * 32 * 512;
  const rsrc_t rK = mk_rsrc(p.k[P], koff, KV_HEAD - (GQA ? w * 16 * 512 : cq * 32 * 512));
  auto k_src = [](int g, int i) -> unsigned {
    return GQA ? ((g >> 2) * 256 + (i >> 1) * 128 + (i & 1) * 8) * 512 + (g & 3) * 128 : ((g >> 2) * 128 + i * 8) * 512 + (g & 3) * 128;
  };

  {
    const rsrc_t rW = mk_rsrc(p.wq[P], (512 * j + 64 * w) * 2, W_BYTES - (512 * j + 64 * w) * 2);
    const int l = lane_id(), n = l & 15, g = l >> 4, q = (l >> 2) & 3;
    const unsigned we = voff_nat(l, 8192), ke = voff_k(l);
    const unsigned a0 = LDS_RING + w * 8192 + (8 * g + q) * 128 + (((q >> 1) | ((g & 1) << 1)) * 32) + 8 * (l & 3);
    unsigned x0[2][2];
    x0[0][0] = n * 128 + 16 * (g ^ ((n >> 1) & 7)); x0[1][0] = x0[0][0] ^ 64; x0[0][1] = x0[0][0] + 40960; x0[1][1] = x0[1][0] + 40960;
    asm volatile("" : "+v"(x0[0][1]), "+v"(x0[1][1]));
    const unsigned qw = hh * 40960 + cq * 10240 + l * 16;
    auto rda = [&](auto S, auto U) { lds_t a = lds + (a0 ^ (U * 32)) + (S & 1) * 4096; fa[S & 1][U] = ldtr(a, a + 512); };
    auto rdb = [&](auto S, auto T) { fb[T] = ld128(lds + x0[S & 1][S >= 8] + ((S >> 1) & 3) * 10240 + T * 2048); };
    auto piece = [&](auto G, auto I) {
      if constexpr (G < 14) dmap<0, I>(rW, we ^ ((I & 1) * 64), (G * 32 + I * 8) * 8192);
      else dmap<1, I>(rK, ke ^ ((I & 1) * 64), k_src(G - 14, I));
    };
    auto qput = [&](auto T, auto H) {
      const f32x4 u = acc[T][2 * H], v = acc[T][2 * H + 1];
      *(LAS u32x4*)(lds + qw + H * 5120 + T * 1024) = u32x4{pk2(u[0], u[1]), pk2(u[2], u[3]), pk2(v[0], v[1]), pk2(v[2], v[3])};
    };
    if constexpr (!LAST) wait_vm<5>();
    sfor<0, 4>([&](auto U) { rda(IC<0>{}, U); });
    sfor<0, 5>([&](auto T) { rdb(IC<0>{}, T); });
    set_m0(ring);
    need(fa[0]);
    sfor<0, 4>([&](auto I) { piece(IC<2>{}, I); });
    sfor<0, 14>([&](auto S) {
      constexpr int s = S;
      SBAR();
      if constexpr (s == 0) prio_reset();
      if constexpr (s == 9) prio_yield(hh);
      if constexpr (s == 0) { if constexpr (!LAST) wait_vm<5>(); }
      else if constexpr (s < 13) wait_vm<(LAST && s == 6) ? 0 : 4>();
      else {
        bar();
        *(LAS f32x2*)(lds + LDS_EXM + w * 512 + l * 8) = f32x2{-INFINITY, -INFINITY};
        if constexpr (LAST) { if (w == 0) arrive<1>(p, lds, b, j); }
      }
      sfor<0, 20>([&](auto I) {
        constexpr int i = I, t = i >> 2, u = i & 3;
        acc[t][u] = MFMA(fa[s & 1][u], fb[t], s == 0 ? ZERO4 : acc[t][u]);
        if constexpr (s < 13) {
          if constexpr (i < 4) rda(IC<s + 1>{}, IC<i & 3>{});
          if constexpr (u == 3) rdb(IC<s + 1>{}, IC<t>{});
          if constexpr (i == 5) set_m0(ring + ((s + 1) & 1) * 4096);
          if constexpr (i == DM0) need(fa[(s + 1) & 1]);
          if constexpr (i >= DM0 && i < DM0 + 4) piece(IC<s + 3>{}, IC<(i - DM0) & 3>{});
        } else if constexpr (i >= 6 && (i & 1) == 0) qput(IC<(i - 6) / 4>{}, IC<((i - 6) / 2) % 2>{});
        SBAR();
      });
    });
    sfor<7, 10>([&](auto I) { qput(IC<I / 2>{}, IC<I % 2>{}); });
    bar();
  }

  const rsrc_t rV = GQA ? mk_rsrc(p.v[P], (b * 8 + j) * KV_HEAD + hh * 16 * 512 + cq * 128, KV_HEAD - (hh * 16 * 512 + cq * 128))
                        : mk_rsrc(p.v[P], (b * 16 + 2 * j + hh) * KV_HEAD + cq * 128, KV_HEAD - cq * 128);
  constexpr int MT = GQA ? 2 : 1;
  constexpr int NG = GQA ? 8 : 16;
  f32x4 sc[4][10];
  float mx[5 * MT], nm[5 * MT], ls[5 * MT], ev[4];
  u32x2 pq[4][10];
  auto mop = [&](auto ID) {
    constexpr int id = ID, kq = id / 90, k = (id / 9) % 10, part = id % 9, r = GQA ? k : (k >> 1);
    if constexpr (part == 0) { ev[0] = __builtin_fmaf(sc[kq][k][0], SCL, nm[r]); ev[1] = __builtin_fmaf(sc[kq][k][1], SCL, nm[r]); }
    if constexpr (part == 1) ev[0] = __builtin_amdgcn_exp2f(ev[0]);
    if constexpr (part == 2) { ev[2] = __builtin_fmaf(sc[kq][k][2], SCL, nm[r]); ev[3] = __builtin_fmaf(sc[kq][k][3], SCL, nm[r]); }
    if constexpr (part == 3) ev[1] = __builtin_amdgcn_exp2f(ev[1]);
    if constexpr (part == 4) ev[2] = __builtin_amdgcn_exp2f(ev[2]);
    if constexpr (part == 5) { if constexpr (kq == 0 && (GQA || (k & 1) == 0)) ls[r] = ev[0] + ev[1]; else ls[r] = (ls[r] + ev[0]) + ev[1]; }
    if constexpr (part == 6) ev[3] = __builtin_amdgcn_exp2f(ev[3]);
    if constexpr (part == 7) { pq[kq][k][0] = pk2(ev[0], ev[1]); ls[r] += ev[2]; }
    if constexpr (part == 8) { pq[kq][k][1] = pk2(ev[2], ev[3]); ls[r] += ev[3]; }
    if constexpr (part == 0) asm volatile("" : "+v"(ev[0]), "+v"(ev[1]));
    if constexpr (part == 1) asm volatile("" : "+v"(ev[0]));
    if constexpr (part == 2) asm volatile("" : "+v"(ev[2]), "+v"(ev[3]));
    if constexpr (part == 3) asm volatile("" : "+v"(ev[1]));
    if constexpr (part == 4) asm volatile("" : "+v"(ev[2]));
    if constexpr (part == 5) asm volatile("" : "+v"(ls[r]));
    if constexpr (part == 6) asm volatile("" : "+v"(ev[3]));
    if constexpr (part == 7) asm volatile("" : "+v"(ls[r]), "+v"(pq[kq][k][0]));
    if constexpr (part == 8) asm volatile("" : "+v"(ls[r]), "+v"(pq[kq][k][1]));
  };
  auto pput = [&](auto KQ, auto K, unsigned pw) {
    constexpr int kq = KQ, k = K;
    if constexpr (GQA) *(LAS u32x2*)(lds + pw + (kq & 1) * 40960 + (k / 5) * 20480 + (k % 5) * 1024) = pq[kq][k];
    else if constexpr (k & 1) *(LAS u32x4*)(lds + pw + (kq & 1) * 40960 + (k / 2) * 1024) = u32x4{pq[kq][k - 1][0], pq[kq][k - 1][1], pq[kq][k][0], pq[kq][k][1]};
  };
  bool grown;
  unsigned pw;
  {
    const int l = lane_id(), n = l & 15, g = l >> 4;
    const unsigned ke = voff_k(l);
    unsigned vv = 0;
    const unsigned k0 = LDS_RING + w * 8192 + n * 128 + 16 * ((g >> 1) ^ ((n >> 1) & 7)) + 8 * (g & 1);
    const unsigned q0 = (GQA ? 0 : hh * 40960) + l * 16;
    unsigned q1 = q0 + 40960;
    asm volatile("" : "+v"(q1));
    constexpr int NSUB = 2 * MT;
    auto rdk = [&](auto G, auto I) {
      lds_t a = lds + (I & 1) * 2048 + (G & 1) * 4096;
      const s16x4 lo = *(LAS const s16x4*)(a + (k0 ^ ((I >> 1) * 64))), hi = *(LAS const s16x4*)(a + (k0 ^ ((I >> 1) * 64 + 32)));
      fa[G & 1][I] = __builtin_shufflevector(lo, hi, 0, 1, 2, 3, 4, 5, 6, 7);
    };
    auto rdq = [&](auto Z, auto T) {
      constexpr int z = Z, gi = z / NSUB, e = (z % NSUB) / MT, h = z % MT;
      fb[T] = ld128(lds + (h ? q1 : q0) + ((2 * (gi & 3) + e) * 5 + T) * 1024);
    };
    auto piece = [&](auto G, auto I) {
      if constexpr (G < NG) dmap<1, I>(rK, ke ^ ((I & 1) * 64), k_src(G, I));
      else if constexpr (!GQA) dmap<(GQA ? 1 : 2), I>(rV, vv, ((G - NG) * 32 + I * 8) * 512);
      else dmap<(GQA ? 1 : 2), I>(rV, vv, ((2 * (G - NG) + (I >> 1)) * 32 + (I & 1) * 8) * 512);
    };
    auto qmax = [&](auto KQ, auto K) {
      constexpr int kq = KQ, k = K, r = GQA ? k : (k >> 1);
      if constexpr ((kq & 1) == 0 && (GQA || (k & 1) == 0)) mx[r] = max3(max3(sc[kq][k][0], sc[kq][k][1], sc[kq][k][2]), sc[kq][k][3], sc[kq][k][3]);
      else mx[r] = max3(max3(mx[r], sc[kq][k][0], sc[kq][k][1]), sc[kq][k][2], sc[kq][k][3]);
      asm volatile("" : "+v"(mx[r]));
    };
    auto xmax = [&](auto HALF, auto C) {
      const int l2 = lane_id(), g2 = l2 >> 4;
      LAS float* em = (LAS float*)(lds + LDS_EXM + HALF * 1536 + ((GQA ? 0 : hh) * 16 + (l2 & 15)) * 48);
      if constexpr (C == 0) __builtin_amdgcn_ds_fmaxf(em + g2, red4<OpMax>(mx[0], mx[1], mx[2], mx[3]), 0, 0, false);
      if constexpr (C == 1 && GQA) __builtin_amdgcn_ds_fmaxf(em + (g2 & 1) * 192 + 4, red2<OpMax>(mx[4], mx[5 * MT - 1]), 0, 0, false);
      if constexpr (C == 1 && !GQA) __builtin_amdgcn_ds_fmaxf(em + 4, red2<OpMax>(mx[4], mx[4]), 0, 0, false);
      if constexpr (C == 2 && GQA) __builtin_amdgcn_ds_fmaxf(em + 192 + g2, red4<OpMax>(mx[5], mx[6], mx[7], mx[8]), 0, 0, false);
    };
    auto rmax = [&](auto HALF) {
      LAS float* em = (LAS float*)(lds + LDS_EXM + HALF * 1536 + ((GQA ? 0 : hh) * 16 + (lane_id() & 15)) * 48);
      sfor<0, MT>([&](auto H) {
        const f32x4 v = *(LAS const f32x4*)(em + H * 192);
        sfor<0, 4>([&](auto T) { mx[H * 5 + T] = v[T + 0]; });
        mx[H * 5 + 4] = em[H * 192 + 4];
      });
    };
    wait_vm<4>();
    sfor<0, 4>([&](auto I) { rdk(IC<0>{}, I); });
    sfor<0, 5>([&](auto T) { rdq(IC<0>{}, T); });
    set_m0(ring);
    need(fa[0]);
    sfor<0, 4>([&](auto I) { piece(IC<2>{}, I); });
    sfor<0, 32>([&](auto Z) {
      constexpr int z = Z, gi = z / NSUB, sub = z % NSUB, e = sub / MT, h = sub % MT;
      SBAR();
      if constexpr (z == 0 || z == 20) prio_reset();
      if constexpr (z == 13 || z == 28) prio_yield(hh);
      if constexpr (z == 19) { bar(); rmax(IC<0>{}); }
      if constexpr (z == (NG - 3) * NSUB) vv = voff_spl(lane_id(), 512);
      if constexpr (sub == 0 && gi + 1 < NG) wait_vm<4>();
      sfor<0, 10>([&](auto I) {
        constexpr int i = I, t = i >> 1, u = i & 1;
        constexpr int kq = GQA ? 2 * (gi >> 2) + u : (gi >> 2), k = GQA ? 5 * h + t : i;
        sc[kq][k] = MFMA(fa[gi & 1][2 * e + u], fb[t], ((gi & 3) == 0 && e == 0) ? ZERO4 : sc[kq][k]);
        if constexpr (u == 1 && z + 1 < 32) rdq(IC<z + 1>{}, IC<t>{});
        if constexpr (gi + 1 < NG) {
          if constexpr (sub == 0 && i < 4) rdk(IC<gi + 1>{}, IC<i & 3>{});
          if constexpr (sub == 0 && i == 5) set_m0(ring + ((gi + 1) & 1) * 4096);
          if constexpr (sub == 0 && i == 6) need(fa[(gi + 1) & 1]);
          if constexpr (sub == 0 && i >= 6) piece(IC<gi + 3>{}, IC<(i - 6) & 3>{});
        }
        if constexpr (z == 16 || z == 17) qmax(IC<z - 16>{}, I);
        if constexpr (z == 18 && (i == 1 || i == 4 || i == 7)) xmax(IC<0>{}, IC<i / 3>{});
        if constexpr (z == 19 && i >= 5) sfor<0, MT>([&](auto H) { nm[H * 5 + i - 5] = mul1(mx[H * 5 + i - 5], -SCL); });
        if constexpr (z >= 20) { constexpr int x = (z - 20) * 10 + i; sfor<(3 * x + 1) / 2, (3 * x + 4) / 2>(mop); }
        if constexpr (!GQA && z == 26) qmax(IC<2>{}, I);
        SBAR();
      });
    });
    sfor<4 - MT, 4>([&](auto KQ) { sfor<0, 10>([&](auto K) { qmax(KQ, K); }); });
    sfor<0, 3>([&](auto C) { xmax(IC<1>{}, C); });
    pw = (GQA ? (w >> 1) * 5120 + (w & 1) * 8 : hh * 20480 + cq * 5120) + lane_id() * 16;
    bar();
    rmax(IC<1>{});
    sfor<0, 10>([&](auto K) { pput(IC<0>{}, K, pw); });
    sfor<0, GQA ? 6 : 0>([&](auto K) { pput(IC<1>{}, K, pw); });
    bool any = false;
    sfor<0, 5 * MT>([&](auto R) { any |= __builtin_fmaf(mx[R], SCL, nm[R]) > GROW; });
    grown = __builtin_amdgcn_ballot_w64(any) != 0;
    if (__builtin_expect(grown, 0)) {
      sfor<0, 5 * MT>([&](auto R) {
        const float d = __builtin_fmaf(mx[R], SCL, nm[R]);
        if (d > GROW) { ls[R] *= __builtin_amdgcn_exp2f(-d); nm[R] = -SCL * mx[R]; }
      });
    }
  }

  const unsigned wwin = hh ? 512 + (cq * 96 & ~63) : w * 128;
  const rsrc_t rO = mk_rsrc(p.wo[P], 512 * j * 896 + wwin, W_BYTES - (512 * j * 896 + wwin));
  {
    const int l = lane_id(), g = l >> 4, q = (l >> 2) & 3;
    const unsigned vv = voff_spl(l, 512);
    const unsigned a0 = LDS_RING + (GQA ? cq : w) * 8192 + (4 * g + q) * 128 + (((q >> 1) | ((g & 1) << 1)) * 32) + 8 * (l & 3);
    const unsigned p0 = hh * 20480 + l * 16;
    auto rda = [&](auto S, auto U) {
      lds_t a = lds + (a0 ^ (U * 32)) + (GQA ? (S & 3) * 2048 : (S & 1) * 4096);
      fa[S & 1][U] = ldtr(a, a + (GQA ? 32768 : 2048));
    };
    auto rdb = [&](auto S, auto T) { fb[T] = ld128(lds + p0 + ((S >> 2) & 1) * 40960 + ((S & 3) * 5 + T) * 1024); };
    unsigned ov = 0, ow = 0;
    auto piece = [&](auto G, auto I) {
      if constexpr (G < 16) dmap<(GQA ? 1 : 2), I>(rV, vv, (G * 32 + I * 8) * 512);
      else dmap<0, I>(rO, ov, ((G - 16) * (GQA ? 16 : 32) + I * 8) * 896);
    };
    constexpr auto TW = [](int s) -> int { return s == 12; };
    unsigned seen = 0;
    float rl[5], rs[2] = {0.f, 0.f};
    f32x4 lv[2];
    unsigned el = 0, eg = 0;
    auto oput = [&](auto T, auto H) {
      const f32x4 u = acc[T][2 * H], v = acc[T][2 * H + 1];
      const float r = rl[T];
      *(LAS u32x4*)(lds + ow + H * 5120 + T * 1024) = u32x4{pk2(mul1(u[0], r), mul1(u[1], r)), pk2(mul1(u[2], r), mul1(u[3], r)),
                                                            pk2(mul1(v[0], r), mul1(v[1], r)), pk2(mul1(v[2], r), mul1(v[3], r))};
    };
    if constexpr (GQA) {
      wait_vm_bar<6>();
      sfor<0, 4>([&](auto U) { rda(IC<0>{}, U); });
    } else {
      wait_vm<4>();
      sfor<0, 4>([&](auto U) { rda(IC<0>{}, U); });
      set_m0(ring);
      bar();
      sfor<0, 4>([&](auto I) { piece(IC<2>{}, I); });
    }
    sfor<0, 5>([&](auto T) { rdb(IC<0>{}, T); });
    sfor<0, 16>([&](auto S) {
      constexpr int s = S;
      SBAR();
      if constexpr (s == 0) prio_reset();
      if constexpr (GQA) {
        constexpr int nv = 4 + TW(s - 1) + TW(s - 2) + TW(s - 3);
        if constexpr ((s & 3) == 3) wait_vm_bar<nv>();
        else { need(fa[s & 1]); wait_vm_bar_raw<nv>(); }
      } else {
        if constexpr (s == 15) bar();
        else { if constexpr ((s & 3) == 3) bar(); wait_vm<4 + TW(s - 1) + TW(s - 2)>(); }
      }
      if constexpr (s == 8) {
        if (__builtin_expect(grown, 0)) {
          LAS float* em = (LAS float*)(lds + LDS_EXM + (hh * 16 + (lane_id() & 15)) * 48);
          sfor<0, 5>([&](auto T) {
            const float d = __builtin_fmaf(em[384 + T], SCL, mul1(em[T + 0], -SCL));
            const float a = d > GROW ? __builtin_amdgcn_exp2f(-d) : 1.f;
            sfor<0, 4>([&](auto U) { acc[T][U] *= a; });
          });
        }
      }
      if constexpr (s == 11) ov = voff_spl(lane_id(), 896);
      if constexpr (s == 15) ow = hh * 40960 + cq * 10240 + lane_id() * 16;
      sfor<0, 20>([&](auto I) {
        constexpr int i = I, t = i >> 2, u = i & 3;
        acc[t][u] = MFMA(fa[s & 1][u], fb[t], s == 0 ? ZERO4 : acc[t][u]);
        if constexpr (GQA) {
          if constexpr (i == 0) set_m0(ring + (s & 3) * 2048);
          if constexpr (i == 1 || i == 2) piece(IC<s + 4>{}, IC<i - 1>{});
          if constexpr (s < 15 && i >= 4 && i < 7) rda(IC<s + 1>{}, IC<i - 4>{});
          if constexpr (s < 15 && i == 8) rda(IC<s + 1>{}, IC<3>{});
        } else if constexpr (s < 15) {
          if constexpr (i < 4) rda(IC<s + 1>{}, IC<i & 3>{});
          if constexpr (i == 5) set_m0(ring + ((s + 1) & 1) * 4096);
          if constexpr (i == DM0) need(fa[(s + 1) & 1]);
          if constexpr (i >= DM0 && i < DM0 + 4) piece(IC<s + 3>{}, IC<(i - DM0) & 3>{});
        }
        if constexpr (s < 15 && u == 3) rdb(IC<s + 1>{}, IC<t>{});
        if constexpr (s < 10 && i >= 1 && i < 19) mop(IC<(180 + 18 * s + i - 1) % 360>{});
        if constexpr (s < 3 && (i == 5 || i == 10 || i == 14 || i == 18)) {
          constexpr int k = (GQA ? 6 : 0) + 4 * s + (i == 5 ? 0 : i == 10 ? 1 : i == 14 ? 2 : 3);
          if constexpr (k < 10) pput(IC<1>{}, IC<k % 10>{}, pw);
        }
        if constexpr (s >= 3 && s <= 10 && (i == 5 || i == 10 || i == 14)) {
          constexpr int q4 = (s - 3) & 3, k = q4 * 3 - (q4 > 2) + (i == 5 ? 0 : i == 10 ? 1 : 2);
          if constexpr (k < 10 && !(q4 == 2 && i == 14)) pput(IC<s < 7 ? 2 : 3>{}, IC<k % 10>{}, pw);
        }
        if constexpr (s == 10) {
          if constexpr (i == 0) { const int l2 = lane_id(); el = LDS_EXL + ((GQA ? 0 : hh) * 80 + (l2 & 15)) * 48 + (GQA ? w : cq) * 4; eg = l2 >> 4; }
          if constexpr (i == 2) rs[0] = sw16<OpAdd>(ls[0], ls[1]);
          if constexpr (i == 3) rs[1] = sw16<OpAdd>(ls[2], ls[3]);
          if constexpr (i == 4) rs[0] = sw32<OpAdd>(rs[0], rs[1]);
          if constexpr (i == 6) *(LAS float*)(lds + el + eg * 768) = rs[0];
          if constexpr (i == 7) rs[1] = sw16<OpAdd>(ls[4], ls[5 * MT - 1]);
          if constexpr (i == 8) rs[1] = sw32<OpAdd>(rs[1], rs[1]);
          if constexpr (i == 9) *(LAS float*)(lds + el + 3072 + (GQA ? (eg & 1) * 3840 : 0)) = rs[1];
          if constexpr (GQA) {
            if constexpr (i == 11) rs[0] = sw16<OpAdd>(ls[5], ls[6]);
            if constexpr (i == 12) rs[1] = sw16<OpAdd>(ls[7], ls[8]);
            if constexpr (i == 13) rs[0] = sw32<OpAdd>(rs[0], rs[1]);
            if constexpr (i == 16) *(LAS float*)(lds + el + 3840 + eg * 768) = rs[0];
          }
          if constexpr (i >= 2 && i < 14) asm volatile("" : "+v"(rs[0]), "+v"(rs[1]));
        }
        if constexpr (TW(s) && i == 14) { seen = __hip_atomic_load(sync_word<LAST ? 1 : 0>(p, b, j), RLX); asm volatile("" ::: "memory"); }
        if constexpr (s >= 11 && s < 14) {
          constexpr int x = (s - 11) * 20 + i - 2, T = x / 10, c = x % 10;
          if constexpr (x == -2) el = LDS_EXL + (hh * 80 + (lane_id() & 15)) * 48;
          if constexpr (x >= 0 && x < 50) {
            if constexpr (c == 0) { lv[0] = *(LAS const f32x4*)(lds + el + T * 768); if constexpr (GQA) lv[1] = *(LAS const f32x4*)(lds + el + T * 768 + 16); }
            if constexpr (c == 4) rs[0] = (lv[0][0] + lv[0][1]) + lv[0][2];
            if constexpr (c == 5) rs[0] = GQA ? (rs[0] + lv[0][3]) + lv[1][0] : rs[0] + lv[0][3];
            if constexpr (c == 6 && GQA) rs[0] = (rs[0] + lv[1][1]) + lv[1][2];
            if constexpr (c == 7 && GQA) rs[0] += lv[1][3];
            if constexpr (c == 8) rl[T % 5] = __builtin_amdgcn_rcpf(rs[0]);
            if constexpr (c >= 4 && c < 8) asm volatile("" : "+v"(rs[0]));
            if constexpr (c == 8) asm volatile("" : "+v"(rl[T % 5]));
          }
        }
        if constexpr (s == 15 && i >= 6 && (i & 1) == 0) oput(IC<(i - 6) / 4>{}, IC<((i - 6) / 2) % 2>{});
        SBAR();
      });
    });
    sfor<7, 10>([&](auto I) { oput(IC<I / 2>{}, IC<I % 2>{}); });
    settle<LAST ? 1 : 0>(p, lds, b, j, seen);
  }

  RedRows red;
  auto oproj = [&](auto NTILE) {
    constexpr int NT = NTILE, NS = 5 * NT, D0 = NT == 4 ? DM0 : 5;
    const rsrc_t rN = mk_rsrc(p.wq[1 - P], (512 * j + 64 * w) * 2, W_BYTES - (512 * j + 64 * w) * 2);
    const int l = lane_id(), g = l >> 4, q = (l >> 2) & 3;
    const unsigned ov = voff_spl(l, 896), we = voff_nat(l, 8192);
    auto piece = [&](auto G, auto I) {
      if constexpr (G < 16) dmap<0, I>(rO, ov, (G * 32 + I * 8) * 896);
      else dmap<0, I>(rN, we ^ ((I & 1) * 64), ((G - 16) * 32 + I * 8) * 8192);
    };
    unsigned a0[NT], o1 = l * 16 + 40960;
    sfor<0, NT>([&](auto U) {
      a0[U] = LDS_RING + w * 8192 + (4 * g + q) * 128 + ((((q >> 1) | ((g & 1) << 1)) ^ (U + (NT == 3 ? cq & 1 : 0))) * 32) + 8 * (l & 3);
    });
    asm volatile("" : "+v"(o1));
    auto rda = [&](auto S, auto U) { lds_t a = lds + a0[U] + (S & 1) * 4096; fa[S & 1][U] = ldtr(a, a + 2048); };
    auto rdb = [&](auto S, auto T) { fb[T] = ld128(lds + (S < 8 ? l * 16 : o1) + ((S & 7) * 5 + T) * 1024); };
    auto needa = [&](auto X) {
      if constexpr (NT == 4) need(fa[X]);
      else need3(fa[X]);
    };
    const rsrc_t rS = mk_rsrc(p.slab, ((P * 32 + b) * 8 + j) * SLAB + (NT == 4 ? w * 10240 : 40960 + cq * 8192), 10240);
    auto sput = [&](auto R) {
      constexpr int r = R;
      constexpr int tu = NT == 4 ? r / 2 : r < 5 ? r : 2 * (r - 5), cu = NT == 4 ? 2 * (r & 1) : r < 5 ? 0 : 2;
      constexpr int tv = NT == 4 ? r / 2 : r < 5 ? r : r == 7 ? 4 : 2 * (r - 5) + 1, cv = NT == 4 ? 2 * (r & 1) + 1 : r < 5 ? 1 : 2;
      const f32x4 u = acc[tu][cu], v = acc[tv][cv];
      auto r0 = __builtin_amdgcn_permlane16_swap(pk2(u[0], u[1]), pk2(v[0], v[1]), false, false);
      auto r1 = __builtin_amdgcn_permlane16_swap(pk2(u[2], u[3]), pk2(v[2], v[3]), false, false);
      __builtin_amdgcn_raw_buffer_store_b128(u32x4{r0[0], r1[0], r0[1], r1[1]}, rS, l * 16 + r * 1024, 0, 16  );
    };
    wait_vm<4>();
    sfor<0, NT>([&](auto U) { rda(IC<0>{}, U); });
    set_m0(ring);
    bar();
    sfor<0, 5>([&](auto T) { rdb(IC<0>{}, T); });
    sfor<0, 4>([&](auto I) { piece(IC<2>{}, I); });
    sfor<0, 16>([&](auto S) {
      constexpr int s = S;
      SBAR();
      if constexpr (s == 0) prio_reset();
      if constexpr (s == 12) prio_yield(hh);
      if constexpr (s < 15) wait_vm<4>();
      else if constexpr (!LAST) { bar(); issue_x(p, 1 - P, b, w, lds0); }
      sfor<0, NS>([&](auto I) {
        constexpr int i = I, t = i / NT, u = i % NT;
        acc[t][u] = MFMA(fa[s & 1][u], fb[t], s == 0 ? ZERO4 : acc[t][u]);
        if constexpr (s < 15) {
          if constexpr (i < NT) rda(IC<s + 1>{}, IC<i % NT>{});
          if constexpr (u == NT - 1) rdb(IC<s + 1>{}, IC<t>{});
          if constexpr (!(LAST && s >= 13)) {
            if constexpr (i == D0 - 2) set_m0(ring + ((s + 1) & 1) * 4096);
            if constexpr (i == D0) needa(IC<(s + 1) & 1>{});
            if constexpr (i >= D0 && i < D0 + 4) piece(IC<s + 3>{}, IC<(i - D0) & 3>{});
          }
          if constexpr (LAST && s >= 13 && i >= 3 && i < 15 && i % 3 == 0) red_load<0, (4 * (s - 13) + i / 3 - 1) & 7>(red, p, 1 - P, b, j, w);
        } else if constexpr (NT == 4) {
          if constexpr (i >= 6 && (i & 1) == 0) sput(IC<(i - 6) / 2>{});
        } else {
          if constexpr (i == 5 || i == 8 || i == 11 || i == 14) sput(IC<(i - 5) / 3>{});
          if constexpr (i == 10) sput(IC<5>{});
        }
        SBAR();
      });
    });
    if constexpr (NT == 4) sfor<7, 10>(sput);
    else { sput(IC<6>{}); sput(IC<4>{}); sput(IC<7>{}); }
    if constexpr (!LAST) wait_vm_bar<NT == 4 ? 10 : 8>();
  };
  if (hh == 0) oproj(IC<4>{});
  else oproj(IC<3>{});
  if constexpr (LAST) {
    wait_vm<0>();
    bar();
    if (w == 0) arrive<2>(p, lds, b, j);
    if (w == 7) sfor<0, 8>([&](auto J) { red_load<1, J>(red, p, 1 - P, b, j, w); });
    red_store(red, p, 1 - P, b, j, w);
    settle<2>(p, lds, b, j, __hip_atomic_load(sync_word<2>(p, b, j), RLX));
    sfor<0, 8>([&](auto J) { red_load<0, J>(red, p, P, b, j, w); });
    if (w == 7) sfor<0, 8>([&](auto J) { red_load<1, J>(red, p, P, b, j, w); });
    red_store(red, p, P, b, j, w);
  }
}

__global__ __launch_bounds__(512, 2) void fused_attn_kernel(Params p) {
  extern __shared__ __attribute__((aligned(16))) char smem[];
  lds_t lds = (lds_t)smem;
  const unsigned lds0 = (unsigned)(uintptr_t)lds;
  const int w = __builtin_amdgcn_readfirstlane(threadIdx.x >> 6);
  const int j = blockIdx.x & 7, b = blockIdx.x >> 3;
  const int first = b & 1;
  {
    {
      unsigned long long pc;
      asm volatile("s_getpc_b64 %0" : "=s"(pc));
      warm(lds0, mk_rsrc((const void*)(pc & ~127ull), (b * 8 + w) * CODE_LINES * 128, (CODE_LINES - 1) * 128 + 4), lane_id() * 128);
    }
    unsigned found = 0;
    if (w == 7) found = __hip_atomic_load(flag_word(p, b, lane_id() & 7), RLX);
    asm volatile("" ::: "memory");
    issue_x(p, first, b, w, lds0);
    asm volatile("s_barrier" ::: "memory");
    const int l = lane_id();
    const rsrc_t rW = mk_rsrc(p.wq[first], (512 * j + 64 * w) * 2, W_BYTES - (512 * j + 64 * w) * 2);
    const unsigned we = voff_nat(l, 8192), ring = lds0 + LDS_RING + w * 8192;
    sfor<0, 2>([&](auto G) {
      set_m0(ring + G * 4096);
      sfor<0, 4>([&](auto I) { dmap<0, I>(rW, we ^ ((I & 1) * 64), (G * 32 + I * 8) * 8192); });
    });
    const unsigned id = (w & 3) * 64 + l;
    if (w < 4) warm(lds0, mk_rsrc(p.wq[first], 0, W_BYTES), ((b >> 1) * 28 + (id >> 3)) * 8192 + 1024 * j + (id & 7) * 128);
    else warm(lds0, mk_rsrc(p.wo[first], 0, W_BYTES), 512 * j * 896 + (b >> 1) * 28672 + id * 128);
    wait_vm_bar<9>();
    if (w == 7) {
      *(LAS unsigned*)(lds + LDS_OLD + (l & 7) * 4) = found;
      if (l < 8) __hip_atomic_store(ack_word(p, b, l, j), found + 1u, RLX);
    }
  }
  if (first == 0) {
    run_problem<0, false>(p, lds, lds0, fresh(b), fresh(j), fresh(w));
    run_problem<1, true>(p, lds, lds0, fresh(b), fresh(j), fresh(w));
  } else {
    run_problem<1, false>(p, lds, lds0, fresh(b), fresh(j), fresh(w));
    run_problem<0, true>(p, lds, lds0, fresh(b), fresh(j), fresh(w));
  }
}

extern "C" void kernel_launch(void* const* d_in, const int* in_sizes, int n_in, void* d_out, int out_size, void* d_ws,
                              size_t ws_size, hipStream_t stream) {
  Params p;
  for (int i = 0; i < 2; ++i) {
    p.x[i] = d_in[5 * i + 0]; p.k[i] = d_in[5 * i + 1]; p.v[i] = d_in[5 * i + 2]; p.wq[i] = d_in[5 * i + 3]; p.wo[i] = d_in[5 * i + 4];
  }
  char* top = (char*)d_ws + ((ws_size - 512 * (size_t)SLAB - 262144) & ~(size_t)4095);
  p.sync = (unsigned*)top;
  p.slab = top + 262144;
  p.out = d_out;
  (void)hipFuncSetAttribute((const void*)fused_attn_kernel, hipFuncAttributeMaxDynamicSharedMemorySize, LDS_TOTAL);
  fused_attn_kernel<<<256, 512, LDS_TOTAL, stream>>>(p);
}
```
